```python
import torch

def _softmax(x, axis=-1):
    return torch.softmax(x, dim=axis)
def _gelu(x):
    return torch.nn.functional.gelu(x, approximate="tanh")
def _silu(x):
    return torch.nn.functional.silu(x)
def _sigmoid(x):
    return torch.sigmoid(x)
def _layernorm(x, axis=-1, eps=1e-5):
    m = x.mean(dim=axis, keepdim=True)
    v = x.var(dim=axis, keepdim=True, unbiased=False)
    return (x - m) / torch.sqrt(v + eps)
def _scatter_add(tgt, idx, src, axis=0):
    return tgt.clone().index_add_(axis, idx, src)
def _segment_sum(src, seg, n):
    out = torch.zeros((n,) + src.shape[1:], dtype=src.dtype)
    return out.index_add_(0, seg, src)
def _pad(x, pw):
    flat = tuple(v for pair in reversed(pw) for v in pair)
    return torch.nn.functional.pad(x, flat)

kernel_name = 'gated_mix__gqa_block_cached__geglu_ffn__decode'

def setup_inputs():

    _w = (lambda a: (lambda t: t.to(torch.bfloat16) if t.is_floating_point() else t)(torch.as_tensor(a)))
    def _A_setup():
        B, Sq, D = 2, 1, 576
        Hq, Hkv, Sk, Dh = 32, 16, 32768, 128
        assert Hq % Hkv == 0, (Hq, Hkv)
        return {
            "x": _w((lambda *s: torch.randn(*s, dtype=torch.bfloat16) * 0.05)(B, Sq, D)),
            "k_cache": _w((lambda *s: torch.randn(*s, dtype=torch.bfloat16) * 6.4)(B, Hkv, Sk, Dh)),
            "v_cache": _w((lambda *s: torch.randn(*s, dtype=torch.bfloat16) * 0.4)(B, Hkv, Sk, Dh)),
            "wq": _w((lambda *s: torch.randn(*s, dtype=torch.bfloat16) * 6.4)(D, Hq * Dh) * (D ** -0.5)),
            "wo": _w((lambda *s: torch.randn(*s, dtype=torch.bfloat16) * 0.4)(Hq * Dh, D) * ((Hq * Dh) ** -0.5)),
        }
    _A_in = _A_setup()

    def _B_setup():
        B, S, D, F = 2, 1, 576, 4608
        return {
            "x": _w((lambda *s: torch.randn(*s, dtype=torch.bfloat16) * 0.05)(B, S, D)),
            "w_gate": _w((lambda *s: torch.randn(*s, dtype=torch.bfloat16) * 0.05)(D, F)),
            "w_up": _w((lambda *s: torch.randn(*s, dtype=torch.bfloat16) * 0.05)(D, F)),
            "w_down": _w((lambda *s: torch.randn(*s, dtype=torch.bfloat16) * 0.05)(F, D)),
        }
    _B_in = _B_setup()

    return {
        "A_x": _A_in["x"],
        "A_k_cache": _A_in["k_cache"],
        "A_v_cache": _A_in["v_cache"],
        "A_wq": _A_in["wq"],
        "A_wo": _A_in["wo"],
        "B_x": _B_in["x"],
        "B_w_gate": _B_in["w_gate"],
        "B_w_up": _B_in["w_up"],
        "B_w_down": _B_in["w_down"],
        "gate_w": _w((lambda *s: torch.randn(*s, dtype=torch.bfloat16) * 0.05)(576) * 16.0),
    }

def reference(A_x, A_k_cache, A_v_cache, A_wq, A_wo, B_x, B_w_gate, B_w_up, B_w_down, gate_w):
    def _A_ref(x, k_cache, v_cache, wq, wo):
        B, Sq, D = x.shape
        Hq, Dh = 32, 128
        Hkv = k_cache.shape[1]
        rep = Hq // Hkv
        q = (lambda x, *d: x.permute(*d))((x @ wq).reshape(B, Sq, Hq, Dh), 0, 2, 1, 3)
        k = (lambda x, n, axis: torch.repeat_interleave(x, n, dim=axis))(k_cache, rep, axis=1)
        v = (lambda x, n, axis: torch.repeat_interleave(x, n, dim=axis))(v_cache, rep, axis=1)
        scores = torch.einsum("bhqd,bhkd->bhqk", q, k) * (Dh ** -0.5)
        p = _softmax(scores, axis=-1)
        o = torch.einsum("bhqk,bhkd->bhqd", p, v)
        return (lambda x, *d: x.permute(*d))(o, 0, 2, 1, 3).reshape(B, Sq, Hq * Dh) @ wo

    def _B_ref(x, w_gate, w_up, w_down):
        return (_gelu(x @ w_gate) * (x @ w_up)) @ w_down

    x = A_x
    g = _sigmoid((x * gate_w).sum(-1, keepdims=True))
    a = _A_ref(x, A_k_cache, A_v_cache, A_wq, A_wo)
    b = _B_ref(x, B_w_gate, B_w_up, B_w_down)
    return g * a + (1 - g) * b
```

```cpp
#include <hip/hip_runtime.h>
#include <hip/hip_bf16.h>
#include <stdint.h>

typedef unsigned v4u __attribute__((ext_vector_type(4)));
typedef float f32x4 __attribute__((ext_vector_type(4)));
typedef short bf16x8 __attribute__((ext_vector_type(8)));
typedef __bf16 bf16x2 __attribute__((ext_vector_type(2)));

constexpr int B = 2, D = 576, HQ = 32, HKV = 16, SK = 32768, DH = 128, F = 4608;
constexpr int NSPLIT = 8, NW = 8, NTHR = NW * 64;
constexpr int KEYS_PER_WG = SK / NSPLIT;
constexpr int TILES_PER_WAVE = KEYS_PER_WG / 16 / NW;

constexpr size_t WS_Q = 0;
constexpr size_t WS_H = 16384;
constexpr size_t WS_O = 65536;
constexpr size_t WS_P = 131072;
constexpr int PREC = 132;

__device__ __forceinline__ float bf2f(unsigned short b) { return __builtin_bit_cast(float, (unsigned)b << 16); }
__device__ __forceinline__ float bflo(unsigned u) { return __builtin_bit_cast(float, u << 16); }
__device__ __forceinline__ float bfhi(unsigned u) { return __builtin_bit_cast(float, u & 0xffff0000u); }
__device__ __forceinline__ unsigned short f2bf(float f) { return __builtin_bit_cast(unsigned short, (__bf16)f); }
__device__ __forceinline__ float rbf(float f) { return bf2f(f2bf(f)); }
__device__ __forceinline__ float dot2(unsigned a, unsigned b, float c) {
  return __builtin_amdgcn_fdot2_f32_bf16(__builtin_bit_cast(bf16x2, a), __builtin_bit_cast(bf16x2, b), c, false);
}
template <int CTRL> __device__ __forceinline__ float dpp(float x) {
  return __builtin_bit_cast(float, __builtin_amdgcn_mov_dpp(__builtin_bit_cast(int, x), CTRL, 0xf, 0xf, true));
}
constexpr int XOR1 = 0xB1, XOR2 = 0x4E, HMIR = 0x141, ROR8 = 0x128;
__device__ __forceinline__ f32x4 mfma(v4u a, v4u b, f32x4 c) {
  return __builtin_amdgcn_mfma_f32_16x16x32_bf16(__builtin_bit_cast(bf16x8, a), __builtin_bit_cast(bf16x8, b), c, 0, 0, 0);
}
__device__ __forceinline__ float xrow_max(float x) {
  auto s = __builtin_amdgcn_permlane16_swap(__float_as_uint(x), __float_as_uint(x), false, false);
  x = fmaxf(__uint_as_float(s[0]), __uint_as_float(s[1]));
  auto t = __builtin_amdgcn_permlane32_swap(__float_as_uint(x), __float_as_uint(x), false, false);
  return fmaxf(__uint_as_float(t[0]), __uint_as_float(t[1]));
}
__device__ __forceinline__ float xrow_sum(float x) {
  auto s = __builtin_amdgcn_permlane16_swap(__float_as_uint(x), __float_as_uint(x), false, false);
  x = __uint_as_float(s[0]) + __uint_as_float(s[1]);
  auto t = __builtin_amdgcn_permlane32_swap(__float_as_uint(x), __float_as_uint(x), false, false);
  return __uint_as_float(t[0]) + __uint_as_float(t[1]);
}
__device__ __forceinline__ unsigned pk_bf16(float lo, float hi) {
  bf16x2 v; v[0] = (__bf16)lo; v[1] = (__bf16)hi; return __builtin_bit_cast(unsigned, v);
}

__global__ void __launch_bounds__(256) k_pre(const unsigned short* __restrict__ x, const unsigned short* __restrict__ wq,
                                             const unsigned short* __restrict__ wg, const unsigned short* __restrict__ wu, unsigned char* ws) {
  __shared__ float xs[2][D];
  __shared__ float red[32][2][64 + 1];
  const int tid = threadIdx.x;
  for (int i = tid; i < 2 * D; i += 256) xs[i / D][i % D] = bf2f(x[i]);
  __syncthreads();
  const int ch = tid & 7, rs = tid >> 3;
  const bool isq = blockIdx.x < 64;
  const unsigned short* wp; int ld;
  if (isq) { wp = wq + blockIdx.x * 64 + ch * 8; ld = HQ * DH; }
  else { const int c0 = (blockIdx.x - 64) * 32; wp = (ch < 4 ? wg : wu) + c0 + (ch & 3) * 8; ld = F; }
  float acc[2][8];
#pragma unroll
  for (int j = 0; j < 8; ++j) { acc[0][j] = 0.f; acc[1][j] = 0.f; }
  v4u w[18];
#pragma unroll
  for (int i = 0; i < 18; ++i) w[i] = __builtin_nontemporal_load((const v4u*)(wp + (size_t)(rs + 32 * i) * ld));
#pragma unroll
  for (int i = 0; i < 18; ++i) {
    const float x0 = xs[0][rs + 32 * i], x1 = xs[1][rs + 32 * i];
#pragma unroll
    for (int k = 0; k < 4; ++k) {
      const float lo = bflo(w[i][k]), hi = bfhi(w[i][k]);
      acc[0][2 * k] = fmaf(x0, lo, acc[0][2 * k]); acc[0][2 * k + 1] = fmaf(x0, hi, acc[0][2 * k + 1]);
      acc[1][2 * k] = fmaf(x1, lo, acc[1][2 * k]); acc[1][2 * k + 1] = fmaf(x1, hi, acc[1][2 * k + 1]);
    }
  }
#pragma unroll
  for (int j = 0; j < 8; ++j) { red[rs][0][ch * 8 + j] = acc[0][j]; red[rs][1][ch * 8 + j] = acc[1][j]; }
  __syncthreads();
  if (tid < 128) {
    const int b = tid >> 6, c = tid & 63;
    float s = 0.f;
#pragma unroll 8
    for (int r = 0; r < 32; ++r) s += red[r][b][c];
    red[0][b][c] = s;
  }
  __syncthreads();
  if (isq) {
    if (tid < 128) { const int b = tid >> 6, c = tid & 63; ((unsigned short*)(ws + WS_Q))[b * 4096 + blockIdx.x * 64 + c] = f2bf(red[0][b][c]); }
  } else if (tid < 64) {
    const int b = tid >> 5, c = tid & 31;
    const float g = rbf(red[0][b][c]), u = rbf(red[0][b][32 + c]);
    const float inner = 0.7978845608028654f * (g + 0.044715f * g * g * g);
    const float t = tanhf(inner);
    const float ge = rbf(0.5f * g * (1.f + t));
    ((unsigned short*)(ws + WS_H))[b * F + (blockIdx.x - 64) * 32 + c] = f2bf(ge * u);
  }
}

__global__ void __launch_bounds__(NTHR) k_attn(const v4u* __restrict__ Kc, const v4u* __restrict__ Vc, unsigned char* ws) {
  __shared__ __attribute__((aligned(16))) float mrg[NW][2][PREC];
  const int tid = threadIdx.x, lane = tid & 63, w = __builtin_amdgcn_readfirstlane(tid >> 6);
  const int grp = blockIdx.x >> 3, sp = blockIdx.x & 7;
  const int b = grp >> 4, kvh = grp & 15;
  const int g = lane >> 4, c = lane & 15;
  const size_t base = ((size_t)grp * SK + (size_t)sp * KEYS_PER_WG + (size_t)w * 16) * 16 + lane;
  const v4u* kp = Kc + base;
  const v4u* vp = Vc + base;
  constexpr size_t TSTR = (size_t)NW * 16 * 16;
  v4u Kb[2][4], Vb[2][4];
#pragma unroll
  for (int t = 0; t < 4; ++t) Kb[0][t] = __builtin_nontemporal_load(kp + t * 64);
#pragma unroll
  for (int t = 0; t < 4; ++t) Vb[0][t] = __builtin_nontemporal_load(vp + t * 64);
  const v4u* qp = (const v4u*)(ws + WS_Q) + (size_t)(b * 4096 + kvh * 256) / 8 + c;
  const v4u q0 = qp[0], q1 = qp[16];
  v4u msk;
#pragma unroll
  for (int d = 0; d < 4; ++d) msk[d] = (((c & 7) >> 1) == d) ? ((c & 1) ? 0xffff0000u : 0x0000ffffu) : 0u;
  const bool hi8 = (c & 8) != 0;
  constexpr float C2 = 0.08838834764831845f * 1.4426950408889634f;
  float m = -INFINITY, l = 0.f;
  f32x4 acc = {0.f, 0.f, 0.f, 0.f};

#define PROCESS(KB, VB)                                                                              \
  {                                                                                                  \
    float s[4];                                                                                      \
    _Pragma("unroll") for (int t = 0; t < 4; ++t) {                                                  \
      float a0 = dot2(KB[t][0], q0[0], 0.f), a1 = dot2(KB[t][0], q1[0], 0.f);                        \
      _Pragma("unroll") for (int k = 1; k < 4; ++k) { a0 = dot2(KB[t][k], q0[k], a0); a1 = dot2(KB[t][k], q1[k], a1); } \
      const float keep = hi8 ? a1 : a0, send = hi8 ? a0 : a1;                                        \
      float v = keep + dpp<ROR8>(send);                                                              \
      v += dpp<HMIR>(v); v += dpp<XOR2>(v); v += dpp<XOR1>(v);                                       \
      s[t] = v;                                                                                      \
    }                                                                                                \
    float tm = fmaxf(fmaxf(s[0], s[1]), fmaxf(s[2], s[3]));                                          \
    tm = xrow_max(tm);                                                                               \
    const float mn = fmaxf(m, tm);                                                                   \
    const float alpha = __builtin_amdgcn_exp2f((m - mn) * C2);                                       \
    const float mc = mn * C2;                                                                        \
    m = mn;                                                                                          \
    float p[4];                                                                                      \
    _Pragma("unroll") for (int t = 0; t < 4; ++t) p[t] = __builtin_amdgcn_exp2f(fmaf(s[t], C2, -mc)); \
    l = fmaf(l, alpha, (p[0] + p[1]) + (p[2] + p[3]));                                               \
    acc *= alpha;                                                                                    \
    _Pragma("unroll") for (int t = 0; t < 4; ++t) {                                                  \
      const unsigned pp = pk_bf16(p[t], p[t]);                                                       \
      v4u bb; bb[0] = pp & msk[0]; bb[1] = pp & msk[1]; bb[2] = pp & msk[2]; bb[3] = pp & msk[3];    \
      acc = mfma(VB[t], bb, acc);                                                                    \
    }                                                                                                \
  }
#define ISSUE(KB, VB, i)                                                                             \
  {                                                                                                  \
    _Pragma("unroll") for (int t = 0; t < 4; ++t) KB[t] = __builtin_nontemporal_load(kp + (size_t)(i) * TSTR + t * 64); \
    _Pragma("unroll") for (int t = 0; t < 4; ++t) VB[t] = __builtin_nontemporal_load(vp + (size_t)(i) * TSTR + t * 64); \
  }
  for (int i = 0; i < TILES_PER_WAVE - 2; i += 2) {
    asm volatile("s_waitcnt vmcnt(0)" ::: "memory");
    ISSUE(Kb[1], Vb[1], i + 1);
    __builtin_amdgcn_sched_barrier(0);
    PROCESS(Kb[0], Vb[0]);
    __builtin_amdgcn_sched_barrier(0);
    asm volatile("s_waitcnt vmcnt(0)" ::: "memory");
    ISSUE(Kb[0], Vb[0], i + 2);
    __builtin_amdgcn_sched_barrier(0);
    PROCESS(Kb[1], Vb[1]);
    __builtin_amdgcn_sched_barrier(0);
  }
  ISSUE(Kb[1], Vb[1], TILES_PER_WAVE - 1);
  __builtin_amdgcn_sched_barrier(0);
  PROCESS(Kb[0], Vb[0]);
  PROCESS(Kb[1], Vb[1]);
  l = xrow_sum(l);
  const int h = c >> 3;
#pragma unroll
  for (int r = 0; r < 4; ++r) mrg[w][h][4 + 8 * (4 * g + r) + (c & 7)] = acc[r];
  if (g == 0 && (c & 7) == 0) { mrg[w][h][0] = m; mrg[w][h][1] = l; }
  __syncthreads();
  if (tid < 256) {
    const int hh = tid >> 7, d = tid & 127;
    float M = -INFINITY;
#pragma unroll
    for (int ww = 0; ww < NW; ++ww) M = fmaxf(M, mrg[ww][hh][0]);
    float L = 0.f, o = 0.f;
#pragma unroll
    for (int ww = 0; ww < NW; ++ww) {
      const float wt = __builtin_amdgcn_exp2f((mrg[ww][hh][0] - M) * C2);
      L = fmaf(wt, mrg[ww][hh][1], L); o = fmaf(wt, mrg[ww][hh][4 + d], o);
    }
    float* pr = (float*)(ws + WS_P) + ((size_t)(grp * NSPLIT + sp) * 2 + hh) * PREC;
    pr[4 + d] = o;
    if (d == 0) { pr[0] = M; pr[1] = L; }
  }
}

__global__ void __launch_bounds__(256) k_comb(unsigned char* ws) {
  const int grp = blockIdx.x, hh = threadIdx.x >> 7, d = threadIdx.x & 127;
  constexpr float C2 = 0.08838834764831845f * 1.4426950408889634f;
  const float* pr = (const float*)(ws + WS_P) + ((size_t)grp * NSPLIT * 2 + hh) * PREC;
  float M = -INFINITY;
#pragma unroll
  for (int s = 0; s < NSPLIT; ++s) M = fmaxf(M, pr[s * 2 * PREC]);
  float L = 0.f, o = 0.f;
#pragma unroll
  for (int s = 0; s < NSPLIT; ++s) {
    const float wt = __builtin_amdgcn_exp2f((pr[s * 2 * PREC] - M) * C2);
    L = fmaf(wt, pr[s * 2 * PREC + 1], L); o = fmaf(wt, pr[s * 2 * PREC + 4 + d], o);
  }
  const int b = grp >> 4, kvh = grp & 15;
  ((unsigned short*)(ws + WS_O))[b * 4096 + kvh * 256 + hh * 128 + d] = f2bf(o / L);
}

__global__ void __launch_bounds__(512) k_post(const unsigned short* __restrict__ x, const unsigned short* __restrict__ wo,
                                              const unsigned short* __restrict__ wd, const unsigned short* __restrict__ gw,
                                              const unsigned char* ws, unsigned short* out) {
  __shared__ float red[8][2][2][8];
  __shared__ float gsh[2];
  const int tid = threadIdx.x, lane = tid & 63, w = tid >> 6;
  const int c0 = blockIdx.x * 8;
  const unsigned short* o = (const unsigned short*)(ws + WS_O);
  const unsigned short* h = (const unsigned short*)(ws + WS_H);
  float aa[2][8], bb[2][8];
#pragma unroll
  for (int j = 0; j < 8; ++j) { aa[0][j] = aa[1][j] = bb[0][j] = bb[1][j] = 0.f; }
  v4u wa[8], wb[9];
#pragma unroll
  for (int i = 0; i < 8; ++i) wa[i] = *(const v4u*)(wo + (size_t)(tid + 512 * i) * D + c0);
#pragma unroll
  for (int i = 0; i < 9; ++i) wb[i] = *(const v4u*)(wd + (size_t)(tid + 512 * i) * D + c0);
#pragma unroll
  for (int i = 0; i < 8; ++i) {
    const float x0 = bf2f(o[tid + 512 * i]), x1 = bf2f(o[4096 + tid + 512 * i]);
#pragma unroll
    for (int k = 0; k < 4; ++k) {
      const float lo = bflo(wa[i][k]), hi = bfhi(wa[i][k]);
      aa[0][2 * k] = fmaf(x0, lo, aa[0][2 * k]); aa[0][2 * k + 1] = fmaf(x0, hi, aa[0][2 * k + 1]);
      aa[1][2 * k] = fmaf(x1, lo, aa[1][2 * k]); aa[1][2 * k + 1] = fmaf(x1, hi, aa[1][2 * k + 1]);
    }
  }
#pragma unroll
  for (int i = 0; i < 9; ++i) {
    const float x0 = bf2f(h[tid + 512 * i]), x1 = bf2f(h[F + tid + 512 * i]);
#pragma unroll
    for (int k = 0; k < 4; ++k) {
      const float lo = bflo(wb[i][k]), hi = bfhi(wb[i][k]);
      bb[0][2 * k] = fmaf(x0, lo, bb[0][2 * k]); bb[0][2 * k + 1] = fmaf(x0, hi, bb[0][2 * k + 1]);
      bb[1][2 * k] = fmaf(x1, lo, bb[1][2 * k]); bb[1][2 * k + 1] = fmaf(x1, hi, bb[1][2 * k + 1]);
    }
  }
#pragma unroll
  for (int bI = 0; bI < 2; ++bI)
#pragma unroll
    for (int j = 0; j < 8; ++j) {
      float va = aa[bI][j], vb = bb[bI][j];
#pragma unroll
      for (int off = 32; off >= 1; off >>= 1) { va += __shfl_xor(va, off); vb += __shfl_xor(vb, off); }
      if (lane == 0) { red[w][0][bI][j] = va; red[w][1][bI][j] = vb; }
    }
  if (w < 2) {
    float s = 0.f;
    for (int i = lane; i < D; i += 64) s += rbf(bf2f(x[w * D + i]) * bf2f(gw[i]));
#pragma unroll
    for (int off = 32; off >= 1; off >>= 1) s += __shfl_xor(s, off);
    if (lane == 0) { const float z = rbf(s); gsh[w] = rbf(1.f / (1.f + __expf(-z))); }
  }
  __syncthreads();
  if (tid < 16) {
    const int bI = tid >> 3, j = tid & 7;
    float a = 0.f, bv = 0.f;
#pragma unroll
    for (int ww = 0; ww < 8; ++ww) { a += red[ww][0][bI][j]; bv += red[ww][1][bI][j]; }
    const float g = gsh[bI];
    const float r = rbf(g * rbf(a)) + rbf(rbf(1.f - g) * rbf(bv));
    out[bI * D + c0 + j] = f2bf(r);
  }
}

extern "C" void kernel_launch(void* const* d_in, const int* in_sizes, int n_in, void* d_out, int out_size, void* d_ws, size_t ws_size,
                              hipStream_t stream) {
  const unsigned short* x = (const unsigned short*)d_in[0];
  const v4u* Kc = (const v4u*)d_in[1];
  const v4u* Vc = (const v4u*)d_in[2];
  const unsigned short* wq = (const unsigned short*)d_in[3];
  const unsigned short* wo = (const unsigned short*)d_in[4];
  const unsigned short* wg = (const unsigned short*)d_in[6];
  const unsigned short* wu = (const unsigned short*)d_in[7];
  const unsigned short* wd = (const unsigned short*)d_in[8];
  const unsigned short* gw = (const unsigned short*)d_in[9];
  unsigned char* ws = (unsigned char*)d_ws;
  k_pre<<<64 + 144, 256, 0, stream>>>(x, wq, wg, wu, ws);
  k_attn<<<256, NTHR, 0, stream>>>(Kc, Vc, ws);
  k_comb<<<32, 256, 0, stream>>>(ws);
  k_post<<<72, 512, 0, stream>>>(x, wo, wd, gw, ws, (unsigned short*)d_out);
}
```

```cpp
#include <hip/hip_runtime.h>
#include <hip/hip_bf16.h>
#include <stdint.h>

typedef unsigned v4u __attribute__((ext_vector_type(4)));
typedef unsigned v2u __attribute__((ext_vector_type(2)));
typedef float f32x4 __attribute__((ext_vector_type(4)));
typedef float f32x2 __attribute__((ext_vector_type(2)));
typedef short bf16x8 __attribute__((ext_vector_type(8)));
typedef __bf16 bf16x2 __attribute__((ext_vector_type(2)));
typedef __amdgpu_buffer_rsrc_t rsrc_t;
#define LAS __attribute__((address_space(3)))

constexpr int D = 576, SK = 32768, F = 4608;
constexpr int NW = 8, NTHR = NW * 64;
constexpr unsigned NT = 2048;
#define NSW_V 200
constexpr unsigned NSW = NSW_V;
constexpr unsigned DB = 8 * NSW;
#define NSTR 7
#define HJOIN 1
#define DRYRUN 1
#define H1MODE 1
constexpr unsigned RSV = NSTR;
constexpr unsigned NPRE = 7;
constexpr int AUX_NT = 2, AUX_SC1 = 16;
constexpr int OOB = 0x40000000;
constexpr unsigned SPIN_CAP = 400000u;
#define SLEEP0 4
#define SLEEPW 12

constexpr int WS_E = 0;
constexpr int WS_CNT = 4096;
constexpr int WS_QG = 16384;
constexpr int WS_H1 = WS_QG + 131072;
constexpr int H1_STRIDE = 264 * 8;
constexpr int WS_H2 = WS_H1 + 256 * H1_STRIDE;
constexpr int WS_H2B = WS_H2 + 256 * 512;
constexpr int WS_FF = WS_H2 + 256 * 1024;
constexpr int WS_FP = WS_FF + 144 * 64;
constexpr int WS_END = WS_FP + 144 * 4608;

constexpr int L_XS = 0;
constexpr int L_QP = 2304;
constexpr int L_QS = L_QP + 2048;
constexpr int L_MRG = L_QS + 512;
constexpr int L_RED = L_MRG + 8448;
constexpr int L_GU = L_RED + 4608;
constexpr int L_HS = L_GU + 1536;
constexpr int L_FP = L_HS + 256;
constexpr int L_FG = L_FP + 4608;
constexpr int L_FFN = L_FG + 864;
constexpr int L_TICK = L_FFN + 304;
constexpr int L_END = L_TICK + 16;
constexpr int LDS_BYTES = 96 * 1024;
static_assert(L_END <= LDS_BYTES, "lds");

__device__ __forceinline__ float bflo(unsigned u) { return __builtin_bit_cast(float, u << 16); }
__device__ __forceinline__ float bfhi(unsigned u) { return __builtin_bit_cast(float, u & 0xffff0000u); }
__device__ __forceinline__ unsigned pk_bf16(float lo, float hi) {
  bf16x2 v; v[0] = (__bf16)lo; v[1] = (__bf16)hi; return __builtin_bit_cast(unsigned, v);
}
__device__ __forceinline__ float rbf(float f) { return bflo(pk_bf16(f, f)); }
__device__ __forceinline__ float dot2(unsigned a, unsigned b, float c) {
  return __builtin_amdgcn_fdot2_f32_bf16(__builtin_bit_cast(bf16x2, a), __builtin_bit_cast(bf16x2, b), c, false);
}
template <int CTRL> __device__ __forceinline__ float dpp(float x) {
  return __builtin_bit_cast(float, __builtin_amdgcn_mov_dpp(__builtin_bit_cast(int, x), CTRL, 0xf, 0xf, true));
}
constexpr int XOR1 = 0xB1, XOR2 = 0x4E, HMIR = 0x141, ROR4 = 0x124, ROR8 = 0x128;
__device__ __forceinline__ f32x4 mfma(v4u a, v4u b, f32x4 c) {
  return __builtin_amdgcn_mfma_f32_16x16x32_bf16(__builtin_bit_cast(bf16x8, a), __builtin_bit_cast(bf16x8, b), c, 0, 0, 0);
}
__device__ __forceinline__ float swap16_sum(float a, float b) {
  auto s = __builtin_amdgcn_permlane16_swap(__float_as_uint(a), __float_as_uint(b), false, false);
  return __uint_as_float(s[0]) + __uint_as_float(s[1]);
}
__device__ __forceinline__ float swap32_sum(float a, float b) {
  auto s = __builtin_amdgcn_permlane32_swap(__float_as_uint(a), __float_as_uint(b), false, false);
  return __uint_as_float(s[0]) + __uint_as_float(s[1]);
}
__device__ __forceinline__ float xrow_max(float x) {
  auto s = __builtin_amdgcn_permlane16_swap(__float_as_uint(x), __float_as_uint(x), false, false);
  x = fmaxf(__uint_as_float(s[0]), __uint_as_float(s[1]));
  auto t = __builtin_amdgcn_permlane32_swap(__float_as_uint(x), __float_as_uint(x), false, false);
  return fmaxf(__uint_as_float(t[0]), __uint_as_float(t[1]));
}
__device__ __forceinline__ float xrow_sum(float x) { x = swap16_sum(x, x); return swap32_sum(x, x); }
__device__ __forceinline__ rsrc_t mk_rsrc(const void* p, int bytes) { return __builtin_amdgcn_make_buffer_rsrc((void*)p, 0, bytes, 0x00020000); }
#define BAR() do { asm volatile("s_waitcnt lgkmcnt(0)" ::: "memory"); __builtin_amdgcn_s_barrier(); asm volatile("" ::: "memory"); } while (0)
#define DRAIN() asm volatile("s_waitcnt vmcnt(0)" ::: "memory")

#define STAMP(i) do {} while (0)
#define STAMP_FLUSH() do {} while (0)
constexpr float SCALE = 0.08838834764831845f, L2E = 1.4426950408889634f;

struct St { float m, l; f32x4 acc; };
__device__ __forceinline__ void pin(St& st) { asm volatile("" : "+v"(st.m), "+v"(st.l), "+v"(st.acc) :: "memory"); }

__device__ __forceinline__ void process(const v4u (&KB)[4], const v4u (&VB)[4], const v4u& q0, const v4u& q1, const v4u& msk, bool hi8, St& st) {
  float s[4];
#pragma unroll
  for (int t = 0; t < 4; ++t) {
    float a0 = dot2(KB[t][0], q0[0], 0.f), a1 = dot2(KB[t][0], q1[0], 0.f);
#pragma unroll
    for (int k = 1; k < 4; ++k) { a0 = dot2(KB[t][k], q0[k], a0); a1 = dot2(KB[t][k], q1[k], a1); }
    const float keep = hi8 ? a1 : a0, send = hi8 ? a0 : a1;
    float v = keep + dpp<ROR8>(send);
    v += dpp<HMIR>(v); v += dpp<XOR2>(v); v += dpp<XOR1>(v);
    s[t] = v;
  }
  {
    const unsigned u0 = pk_bf16(s[0], s[1]), u1 = pk_bf16(s[2], s[3]);
    const unsigned w0 = pk_bf16(bflo(u0) * SCALE, bfhi(u0) * SCALE), w1 = pk_bf16(bflo(u1) * SCALE, bfhi(u1) * SCALE);
    s[0] = bflo(w0); s[1] = bfhi(w0); s[2] = bflo(w1); s[3] = bfhi(w1);
  }
  float tm = fmaxf(fmaxf(s[0], s[1]), fmaxf(s[2], s[3]));
  tm = xrow_max(tm);
  const float mn = fmaxf(st.m, __builtin_ceilf(tm * L2E));
  const float alpha = __builtin_amdgcn_exp2f(st.m - mn);
  st.m = mn;
  float p[4];
#pragma unroll
  for (int t = 0; t < 4; ++t) p[t] = __builtin_amdgcn_exp2f(fmaf(s[t], L2E, -mn));
  st.l = fmaf(st.l, alpha, (p[0] + p[1]) + (p[2] + p[3]));
  st.acc *= alpha;
#pragma unroll
  for (int t = 0; t < 4; ++t) {
    const unsigned pp = pk_bf16(p[t], p[t]);
    v4u bb; bb[0] = pp & msk[0]; bb[1] = pp & msk[1]; bb[2] = pp & msk[2]; bb[3] = pp & msk[3];
    st.acc = mfma(VB[t], bb, st.acc);
  }
}
__device__ __forceinline__ void issue(v4u (&KB)[4], v4u (&VB)[4], rsrc_t kr, rsrc_t vr, int voff, unsigned tile) {
  const int so = (int)(tile * 4096u);
#pragma unroll
  for (int t = 0; t < 4; ++t) KB[t] = __builtin_amdgcn_raw_buffer_load_b128(kr, voff + t * 1024, so, AUX_NT);
#pragma unroll
  for (int t = 0; t < 4; ++t) VB[t] = __builtin_amdgcn_raw_buffer_load_b128(vr, voff + t * 1024, so, AUX_NT);
}
__device__ __forceinline__ void atomic_inc_cond(unsigned& a, const void* head, unsigned x, unsigned lim, unsigned vzero, unsigned vone) {
  unsigned long long keep;
  asm volatile("s_cmp_lt_u32 %5, %6\n\ts_cbranch_scc0 1f\n\ts_mov_b64 %1, exec\n\ts_mov_b64 exec, 1\n\tglobal_atomic_add %0, %2, %3, %4 sc0\n\ts_mov_b64 exec, %1\n1:"
               : "+v"(a), "=&s"(keep) : "v"(vzero), "v"(vone), "s"(head), "s"(x), "s"(lim) : "memory", "scc");
}
__device__ __forceinline__ unsigned atomic_inc_sync(const void* head, unsigned vzero, unsigned vone) {
  unsigned long long keep; unsigned r, s;
  asm volatile("s_mov_b64 %1, exec\n\ts_mov_b64 exec, 1\n\tglobal_atomic_add %0, %3, %4, %5 sc0\n\ts_mov_b64 exec, %1\n\ts_waitcnt vmcnt(0)\n\tv_readfirstlane_b32 %2, %0\n\ts_nop 1"
               : "=&v"(r), "=&s"(keep), "=s"(s) : "v"(vzero), "v"(vone), "s"(head) : "memory");
  return s;
}
__device__ __forceinline__ unsigned lds_ticket(unsigned lds_addr_v, unsigned vone) {
  unsigned long long keep; unsigned r, s;
  asm volatile("s_mov_b64 %1, exec\n\ts_mov_b64 exec, 1\n\tds_add_rtn_u32 %0, %3, %4\n\ts_mov_b64 exec, %1\n\ts_waitcnt lgkmcnt(0)\n\tv_readfirstlane_b32 %2, %0\n\ts_nop 1"
               : "=&v"(r), "=&s"(keep), "=s"(s) : "v"(lds_addr_v), "v"(vone) : "memory");
  return s;
}

__device__ __forceinline__ void fma8(float (&acc)[8], float xv, const v4u& w) {
#pragma unroll
  for (int k = 0; k < 4; ++k) { acc[2 * k] = fmaf(xv, bflo(w[k]), acc[2 * k]); acc[2 * k + 1] = fmaf(xv, bfhi(w[k]), acc[2 * k + 1]); }
}
template <bool BIT2> __device__ __forceinline__ f32x2 red8(const float (&acc)[8]) {
  float a[4];
#pragma unroll
  for (int j = 0; j < 4; ++j) a[j] = swap32_sum(acc[j], acc[j + 4]);
  float b0 = swap16_sum(a[0], a[2]), b1 = swap16_sum(a[1], a[3]);
  if (BIT2) { b0 += dpp<ROR4>(b0); b1 += dpp<ROR4>(b1); }
  b0 += dpp<ROR8>(b0); b1 += dpp<ROR8>(b1);
  f32x2 r; r[0] = b0; r[1] = b1; return r;
}

__global__ void __launch_bounds__(NTHR, 2)
k_fused(const unsigned short* __restrict__ x, const unsigned char* Kc, const unsigned char* Vc,
        const unsigned char* __restrict__ wq, const unsigned char* wo, const unsigned char* wg,
        const unsigned char* wu, const unsigned char* __restrict__ wd, const unsigned short* __restrict__ gw,
        unsigned char* ws, unsigned* out) {
  __shared__ __attribute__((aligned(16))) unsigned char smem[LDS_BYTES];
  const int tid = threadIdx.x, lane = tid & 63, w = __builtin_amdgcn_readfirstlane(tid >> 6);
  const int bid = blockIdx.x, grp = bid >> 3, sp = bid & 7, b = grp >> 4, kvh = grp & 15;
  const int g = lane >> 4, c = lane & 15;
  unsigned tsv = 0xffffffffu; (void)tsv;
  STAMP(0);
  const rsrc_t wsr = mk_rsrc(ws, WS_END);
  const int voff = lane * 16;
  if (w == 7) {
    const v4u z = {0u, 0u, 0u, 0u};
    __builtin_amdgcn_raw_buffer_store_b128(z, wsr, WS_H1 + bid * H1_STRIDE + voff, 0, AUX_SC1);
    __builtin_amdgcn_raw_buffer_store_b128(z, wsr, WS_H1 + bid * H1_STRIDE + 1024 + voff, 0, AUX_SC1);
    __builtin_amdgcn_raw_buffer_store_b128(z, wsr, lane < 32 ? WS_H2 + bid * 512 + voff : WS_H2B + ((b * 8 + ((lane - 32) >> 2)) * 128 + (bid & 127)) * 64 + (lane & 3) * 16, 0, AUX_SC1);
    int off = OOB;
    if (lane < 4) off = WS_H1 + bid * H1_STRIDE + 2048 + voff;
    else if (lane == 4 && sp == 0) off = WS_CNT + grp * 256;
    __builtin_amdgcn_raw_buffer_store_b128(z, wsr, off, 0, AUX_SC1);
  }
  unsigned short xq[5];
  v4u wqv[5];
  {
    const int r = lane >> 3, row0 = 288 * (sp & 1) + 36 * w;
    const rsrc_t qr = mk_rsrc(wq, D * 8192);
    const int base = (row0 + r) * 8192 + (kvh * 256 + (sp >> 1) * 64) * 2 + (lane & 7) * 16;
#pragma unroll
    for (int i = 0; i < 4; ++i) wqv[i] = __builtin_amdgcn_raw_buffer_load_b128(qr, base, i * 8 * 8192, AUX_NT);
    wqv[4] = __builtin_amdgcn_raw_buffer_load_b128(qr, r < 4 ? base : OOB, 4 * 8 * 8192, AUX_NT);
#pragma unroll
    for (int i = 0; i < 5; ++i) xq[i] = x[b * D + row0 + 8 * i + (i < 4 ? r : (r & 3))];
  }
  const int xi = tid < 288 ? tid : 287;
  const unsigned xa = ((const unsigned*)x)[xi], xb = ((const unsigned*)x)[288 + xi];
  __builtin_amdgcn_sched_barrier(0);
  const rsrc_t kr = mk_rsrc(Kc + (size_t)grp * (SK * 256), SK * 256);
  const rsrc_t vr = mk_rsrc(Vc + (size_t)grp * (SK * 256), SK * 256);
  const unsigned sbase = (unsigned)sp * NSW;
  unsigned long long ev;
  v4u Kb[2][4], Vb[2][4];
  auto qpart = [&]() {
    asm volatile("" : "+v"(wqv[0]), "+v"(wqv[1]), "+v"(wqv[2]), "+v"(wqv[3]), "+v"(wqv[4]));
    float acc[8];
#pragma unroll
    for (int j = 0; j < 8; ++j) acc[j] = 0.f;
#pragma unroll
    for (int i = 0; i < 5; ++i) fma8(acc, bflo(xq[i]), wqv[i]);
    STAMP(12);
    const f32x2 r2 = red8<false>(acc);
    if ((lane & 8) == 0) *(LAS f32x2*)(smem + L_QP + (w * 64 + (lane & 7) * 8 + 4 * (lane >> 5) + 2 * ((lane >> 4) & 1)) * 4) = r2;
    if (tid < 288) {
      v2u xs2; xs2[0] = (xa & 0xffffu) | (xb << 16); xs2[1] = (xa >> 16) | (xb & 0xffff0000u);
      *(LAS v2u*)(smem + L_XS + tid * 8) = xs2;
    }
    if (tid == 0) { v4u t4 = {2 * NPRE, 0u, 0u, 0u}; *(LAS v4u*)(smem + L_TICK) = t4; }
    BAR();
    STAMP(2);
  };
  unsigned long long tag64;
  unsigned tag_hi, tag_lo8;
  auto mktag = [&]() {
    asm volatile("" : "+s"(ev));
    tag64 = ev * 6364136223846793005ull + 1442695040888963407ull;
    tag_hi = (unsigned)(tag64 >> 32) | 1u;
    tag_lo8 = ((unsigned)tag64 >> 24) & 0xffu;
  };

  const int ridx = (grp + 4 * sp) & 31;
  const bool has_task = ridx < 18;
  const int task = sp * 18 + ridx;
  const bool is_final = ridx >= 30;
  const int fs = (kvh >> 2) * 2 + (kvh & 1);

  v4u WO[8];
  const rsrc_t wor = mk_rsrc(wo, 4096 * D * 2);
  const int wo_off = (c < 8 || w == sp) ? (kvh * 256 + 32 * w + g) * 1152 + (c < 8 ? sp * 128 + c * 16 : 1024 + (c - 8) * 16) : OOB;
  v4u msk;
#pragma unroll
  for (int d = 0; d < 4; ++d) msk[d] = (((c & 7) >> 1) == d) ? ((c & 1) ? 0xffff0000u : 0x0000ffffu) : 0u;
  const bool hi8 = (c & 8) != 0;
  St st; st.m = -INFINITY; st.l = 0.f; st.acc = f32x4{0.f, 0.f, 0.f, 0.f};
  const unsigned char* head = ws + WS_CNT + grp * 256;
  unsigned vzero = 0u, vone = 1u, a = 0u;
  unsigned taddr = (unsigned)(size_t)(LAS unsigned char*)(smem + L_TICK);
  asm volatile("" : "+v"(vzero), "+v"(vone), "+v"(taddr), "+v"(a));
  unsigned lastt = 0;
  v4u q0, q1;

#define NEXT(nx)                                                                                                   \
  {                                                                                                                \
    unsigned cmpv;                                                                                                 \
    if (lastt + RSV < NSW) {                                                                                       \
      const unsigned t = lds_ticket(taddr, vone);                                                                  \
      lastt = t;                                                                                                   \
      if (t < NSW) { nx = sbase + t; cmpv = (t + RSV >= NSW) ? 0u : NT; }                                          \
      else { nx = atomic_inc_sync(head, vzero, vone) + DB; if (nx > NT) nx = NT; cmpv = nx; }                      \
    } else {                                                                                                       \
      nx = __builtin_amdgcn_readfirstlane(a) + DB; if (nx > NT) nx = NT;                                           \
      cmpv = nx;                                                                                                   \
    }                                                                                                              \
    atomic_inc_cond(a, head, cmpv, NT, vzero, vone);                                                               \
  }

  unsigned first = 0;
  asm volatile("" ::: "memory");
  __builtin_amdgcn_s_barrier();
  asm volatile("s_load_dwordx2 %0, %1, 0x0" : "=s"(ev) : "s"(ws) : "memory");
  STAMP(1);
  if (w == 7) {
    qpart();
    mktag();
    {
      float s = 0.f;
#pragma unroll
      for (int ww = 0; ww < NW; ++ww) s += *(const LAS float*)(smem + L_QP + (ww * 64 + lane) * 4);
      DRAIN();
      v2u gr; gr[0] = ((__float_as_uint(s) + 0x80u) & 0xffffff00u) | tag_lo8; gr[1] = tag_hi;
      __builtin_amdgcn_raw_buffer_store_b64(gr, wsr, WS_QG + ((grp * 2 + (sp & 1)) * 256 + (sp >> 1) * 64 + lane) * 8, 0, AUX_SC1);
    }
    STAMP(13);
    for (unsigned spins = 0;; ++spins) {
      v4u p[4];
#pragma unroll
      for (int i = 0; i < 4; ++i) p[i] = __builtin_amdgcn_raw_buffer_load_b128(wsr, WS_QG + grp * 4096 + lane * 32 + (i & 1) * 16 + (i >> 1) * 2048, 0, AUX_SC1);
      bool ok = true;
#pragma unroll
      for (int i = 0; i < 4; ++i) ok = ok && p[i][1] == tag_hi && p[i][3] == tag_hi && (p[i][0] & 0xffu) == tag_lo8 && (p[i][2] & 0xffu) == tag_lo8;
      if (__all(ok) || spins > SPIN_CAP) {
        float qv[4];
#pragma unroll
        for (int i = 0; i < 4; ++i)
          qv[i] = __uint_as_float(p[i >> 1][(i & 1) * 2] & 0xffffff00u) + __uint_as_float(p[2 + (i >> 1)][(i & 1) * 2] & 0xffffff00u);
        v2u qq; qq[0] = pk_bf16(qv[0], qv[1]); qq[1] = pk_bf16(qv[2], qv[3]);
        *(LAS v2u*)(smem + L_QS + lane * 8) = qq;
        break;
      }
      __builtin_amdgcn_s_sleep(2);
    }
    asm volatile("s_waitcnt lgkmcnt(0)" ::: "memory");
    if (lane == 0) *(volatile LAS unsigned*)(smem + L_TICK + 8) = 1u;
    STAMP(3);
#pragma unroll
    for (int i = 0; i < 8; ++i) WO[i] = __builtin_amdgcn_raw_buffer_load_b128(wor, wo_off, i * 4608, AUX_NT);
    q0 = *(const LAS v4u*)(smem + L_QS + c * 16); q1 = *(const LAS v4u*)(smem + L_QS + 256 + c * 16);
    if (has_task) {
      const rsrc_t dr = mk_rsrc(wd, F * D * 2);
      const int dbase = (task * 32 + g) * 1152 + c * 16;
      f32x4 da[5];
#pragma unroll
      for (int cb = 0; cb < 5; ++cb) da[cb] = f32x4{0.f, 0.f, 0.f, 0.f};
#pragma unroll
      for (int hf = 0; hf < 2; ++hf) {
        v4u wv[20];
#pragma unroll
        for (int i = 0; i < 4; ++i)
#pragma unroll
          for (int cb = 0; cb < 5; ++cb)
            wv[i * 5 + cb] = __builtin_amdgcn_raw_buffer_load_b128(dr, (cb == 4 && c >= 8) ? OOB : dbase + cb * 256, (hf * 4 + i) * 4608, AUX_NT);
        __builtin_amdgcn_sched_barrier(0);
        if (hf == 0) {
          for (unsigned spins = 0; spins < 64u * SPIN_CAP; ++spins) {
            if (*(volatile LAS unsigned*)(smem + L_TICK + 4) >= 6u) break;
            __builtin_amdgcn_s_sleep(4);
          }
          asm volatile("" ::: "memory");
          const LAS float* gu = (const LAS float*)(smem + L_GU) + lane;
          const float gv = rbf(gu[0] + gu[64] + gu[128]), uv = rbf(gu[192] + gu[256] + gu[320]);
          const float inner = 0.7978845608028654f * (gv + 0.044715f * gv * gv * gv);
          const float ge = rbf(0.5f * gv * (1.f + tanhf(inner)));
          *(LAS float*)(smem + L_HS + lane * 4) = rbf(ge * uv);
          asm volatile("s_waitcnt lgkmcnt(0)" ::: "memory");
        }
#pragma unroll
        for (int i = 0; i < 4; ++i) {
          const float hv = *(const LAS float*)(smem + L_HS + ((c >> 3) * 32 + (hf * 4 + i) * 4 + g) * 4);
          const unsigned pp = pk_bf16(hv, hv);
          v4u bb; bb[0] = pp & msk[0]; bb[1] = pp & msk[1]; bb[2] = pp & msk[2]; bb[3] = pp & msk[3];
#pragma unroll
          for (int cb = 0; cb < 5; ++cb) da[cb] = mfma(wv[i * 5 + cb], bb, da[cb]);
        }
        __builtin_amdgcn_sched_barrier(0);
      }
#pragma unroll
      for (int cb = 0; cb < 5; ++cb)
#pragma unroll
        for (int rr = 0; rr < 4; ++rr) {
          const int col = cb * 128 + (4 * g + rr) * 8 + (c & 7);
          if (col < D) *(LAS float*)(smem + L_FP + ((c >> 3) * D + col) * 4) = da[cb][rr];
        }
      asm volatile("s_waitcnt lgkmcnt(0)" ::: "memory");
#pragma unroll
      for (int i = 0; i < 5; ++i) {
        const int o = (i * 64 + lane) * 16;
        if (o < 4608) {
          const v4u v = *(const LAS v4u*)(smem + L_FP + o);
          __builtin_amdgcn_raw_buffer_store_b128(v, wsr, WS_FP + task * 4608 + o, 0, AUX_SC1);
        }
      }
      DRAIN();
      if (lane == 0) {
        v2u fl; fl[0] = (unsigned)tag64; fl[1] = tag_hi;
        __builtin_amdgcn_raw_buffer_store_b64(fl, wsr, WS_FF + task * 64, 0, AUX_SC1);
      }
    }
    STAMP(4);
    if (is_final) {
      for (unsigned spins = 0;; ++spins) {
        bool ok = true;
#pragma unroll
        for (int i = 0; i < 4; ++i) {
          const v2u p = __builtin_amdgcn_raw_buffer_load_b64(wsr, WS_QG + (i * 64 + lane) * 512, 0, AUX_SC1);
          ok = ok && p[1] == tag_hi && (p[0] & 0xffu) == tag_lo8;
        }
        if (__all(ok) || spins > SPIN_CAP) break;
        __builtin_amdgcn_s_sleep(16);
      }
      if (grp == 2 && lane == 0) {
        v2u e; e[0] = (unsigned)tag64; e[1] = (unsigned)(tag64 >> 32);
        __builtin_amdgcn_raw_buffer_store_b64(e, wsr, WS_E, 0, AUX_SC1);
      }
      {
        float z = 0.f;
#pragma unroll
        for (int i = 0; i < 9; ++i) {
          const unsigned xv = *(const LAS unsigned*)(smem + L_XS + (i * 64 + lane) * 4);
          z += rbf((b ? bfhi(xv) : bflo(xv)) * bflo(gw[i * 64 + lane]));
        }
        z += dpp<XOR1>(z); z += dpp<XOR2>(z); z += dpp<ROR4>(z); z += dpp<ROR8>(z);
        z = rbf(xrow_sum(z));
        if (lane == 0) *(LAS float*)(smem + L_FFN + 72 * 4) = rbf(1.f / (1.f + __expf(-z)));
      }
      for (unsigned spins = 0;; ++spins) {
        bool ok = true;
#pragma unroll
        for (int i = 0; i < 3; ++i) {
          const int tk = i * 64 + lane;
          const v2u p = __builtin_amdgcn_raw_buffer_load_b64(wsr, WS_FF + (tk < 144 ? tk : 0) * 64, 0, AUX_SC1);
          ok = ok && p[1] == tag_hi && p[0] == (unsigned)tag64;
        }
        if (__all(ok) || spins > SPIN_CAP) break;
        __builtin_amdgcn_s_sleep(64);
      }
      __builtin_amdgcn_fence(__ATOMIC_ACQUIRE, "wavefront");
      {
        const int ts = lane / 18, q4 = lane - ts * 18;
        const int fcol = q4 < 16 ? fs * 64 + q4 * 4 : 512 + fs * 8 + (q4 - 16) * 4;
        const int fo = ts < 3 ? WS_FP + ts * 4608 + (b * D + fcol) * 4 : OOB;
        f32x4 fa = {0.f, 0.f, 0.f, 0.f};
#pragma unroll 1
        for (int bt = 0; bt < 2; ++bt) {
          v4u pv[24];
#pragma unroll
          for (int i = 0; i < 24; ++i) pv[i] = __builtin_amdgcn_raw_buffer_load_b128(wsr, fo, (bt * 24 + i) * 3 * 4608, AUX_SC1);
          __builtin_amdgcn_sched_barrier(0);
#pragma unroll
          for (int i = 0; i < 24; ++i) fa += __builtin_bit_cast(f32x4, pv[i]);
        }
        if (ts < 3) *(LAS f32x4*)(smem + L_FG + (ts * 72 + q4 * 4) * 4) = fa;
        asm volatile("s_waitcnt lgkmcnt(0)" ::: "memory");
#pragma unroll
        for (int i = 0; i < 2; ++i) {
          const int col = i * 64 + lane;
          if (col < 72)
            *(LAS float*)(smem + L_FFN + col * 4) = *(const LAS float*)(smem + L_FG + col * 4) + *(const LAS float*)(smem + L_FG + (72 + col) * 4) +
                                                    *(const LAS float*)(smem + L_FG + (144 + col) * 4);
        }
      }
    }
    STAMP(5);
    DRAIN();
    if (HJOIN) { NEXT(first); } else first = NT;
    issue(Kb[1], Vb[1], kr, vr, voff, first);
  } else {
    unsigned qrdy = 0;
#define PACE()                                                                                              \
    {                                                                                                       \
      __builtin_amdgcn_sched_barrier(0);                                                                    \
      if (qrdy == 0) {                                                                                      \
        DRAIN();                                                                                            \
        qrdy = __builtin_amdgcn_readfirstlane(*(volatile LAS unsigned*)(smem + L_TICK + 8));                \
      }                                                                                                     \
      __builtin_amdgcn_sched_barrier(0);                                                                    \
    }
    const int so0 = (int)((sbase + w) * 4096u), so1 = (int)((sbase + NPRE + w) * 4096u);
#pragma unroll
    for (int t = 0; t < 4; ++t) Kb[0][t] = __builtin_amdgcn_raw_buffer_load_b128(kr, voff + t * 1024, so0, AUX_NT);
    __builtin_amdgcn_sched_barrier(0);
    qpart();
    mktag();
    PACE();
#pragma unroll
    for (int t = 0; t < 4; ++t) Vb[0][t] = __builtin_amdgcn_raw_buffer_load_b128(vr, voff + t * 1024, so0, AUX_NT);
    PACE();
#pragma unroll
    for (int t = 0; t < 4; ++t) Kb[1][t] = __builtin_amdgcn_raw_buffer_load_b128(kr, voff + t * 1024, so1, AUX_NT);
    PACE();
#pragma unroll
    for (int t = 0; t < 4; ++t) Vb[1][t] = __builtin_amdgcn_raw_buffer_load_b128(vr, voff + t * 1024, so1, AUX_NT);
    PACE();
#pragma unroll
    for (int i = 0; i < 8; ++i) WO[i] = __builtin_amdgcn_raw_buffer_load_b128(wor, wo_off, i * 4608, AUX_NT);
    const bool gu_wave = has_task && w < 6;
    const int r = lane >> 2, mat = w >= 3 ? 1 : 0, third = w - 3 * mat;
    v4u wv[12];
    if (gu_wave) {
      const rsrc_t gr = mk_rsrc(mat ? wu : wg, D * F * 2);
      const int gbase = (third * 192 + r) * (F * 2) + task * 64 + (lane & 3) * 16;
#pragma unroll
      for (int pc = 0; pc < 3; ++pc) {
        PACE();
#pragma unroll
        for (int i = 4 * pc; i < 4 * pc + 4; ++i) wv[i] = __builtin_amdgcn_raw_buffer_load_b128(gr, gbase, i * 16 * (F * 2), AUX_NT);
      }
    }
    __builtin_amdgcn_sched_barrier(0);
    for (unsigned spins = 0; qrdy == 0 && spins < 64u * SPIN_CAP; ++spins) {
      qrdy = __builtin_amdgcn_readfirstlane(*(volatile LAS unsigned*)(smem + L_TICK + 8));
      if (qrdy == 0) __builtin_amdgcn_s_sleep(1);
    }
    asm volatile("" ::: "memory");
#undef PACE
    STAMP(3);
    q0 = *(const LAS v4u*)(smem + L_QS + c * 16); q1 = *(const LAS v4u*)(smem + L_QS + 256 + c * 16);
    process(Kb[0], Vb[0], q0, q1, msk, hi8, st); pin(st);
    if (gu_wave) {
      float a0[8], a1[8];
#pragma unroll
      for (int j = 0; j < 8; ++j) { a0[j] = 0.f; a1[j] = 0.f; }
#pragma unroll
      for (int i = 0; i < 12; ++i) {
        const unsigned xv = *(const LAS unsigned*)(smem + L_XS + (third * 192 + i * 16 + r) * 4);
        fma8(a0, bflo(xv), wv[i]); fma8(a1, bfhi(xv), wv[i]);
      }
      const f32x2 r0 = red8<true>(a0), r1 = red8<true>(a1);
      if ((lane & 12) == 0) {
        const int col = (lane & 3) * 8 + 4 * (lane >> 5) + 2 * ((lane >> 4) & 1);
        *(LAS f32x2*)(smem + L_GU + ((w * 2 + 0) * 32 + col) * 4) = r0;
        *(LAS f32x2*)(smem + L_GU + ((w * 2 + 1) * 32 + col) * 4) = r1;
      }
      asm volatile("s_waitcnt lgkmcnt(0)" ::: "memory");
      (void)lds_ticket(taddr + 4, vone);
    }
    if (NSTR < 7 && w >= NSTR) {
      process(Kb[1], Vb[1], q0, q1, msk, hi8, st); pin(st);
      first = NT;
    }
  }
  __builtin_amdgcn_sched_barrier(0);
  unsigned dry = DRYRUN ? (unsigned)(w + 1) >> 3 : 0u;
#pragma clang loop unroll(disable)
  for (;;) {
  asm volatile("" : "+s"(dry));
  if (dry == 0 && first < NT) {
    while (true) {
      unsigned nx;
      asm volatile("s_waitcnt vmcnt(0)" : "+v"(a) :: "memory");
      NEXT(nx);
      issue(Kb[0], Vb[0], kr, vr, voff, nx);
      __builtin_amdgcn_sched_barrier(0);
      process(Kb[1], Vb[1], q0, q1, msk, hi8, st); pin(st);
      __builtin_amdgcn_sched_barrier(0);
      if (nx >= NT) break;
      asm volatile("s_waitcnt vmcnt(0)" : "+v"(a) :: "memory");
      NEXT(nx);
      issue(Kb[1], Vb[1], kr, vr, voff, nx);
      __builtin_amdgcn_sched_barrier(0);
      process(Kb[0], Vb[0], q0, q1, msk, hi8, st); pin(st);
      __builtin_amdgcn_sched_barrier(0);
      if (nx >= NT) break;
    }
  }
  const rsrc_t tr = mk_rsrc(ws, dry ? 0 : WS_END);
  const rsrc_t outr = mk_rsrc(out, dry ? 0 : 2 * D * 2);
  LAS unsigned char* sm = (LAS unsigned char*)smem + (dry ? 49152 : 0);
  const int vt = dry ? lane : tid;
  const unsigned cap = dry ? 0u : SPIN_CAP;
#define TBAR() do { asm volatile("s_waitcnt lgkmcnt(0)" ::: "memory"); if (dry == 0) __builtin_amdgcn_s_barrier(); asm volatile("" ::: "memory"); } while (0)
  STAMP(6);
  {
    const float l = xrow_sum(st.l);
    const int h = c >> 3;
    LAS float* mrg = (LAS float*)(sm + L_MRG) + (w * 2 + h) * 132;
#pragma unroll
    for (int r = 0; r < 4; ++r) mrg[4 + 8 * (4 * g + r) + (c & 7)] = st.acc[r];
    if (g == 0 && (c & 7) == 0) { mrg[0] = st.m; mrg[1] = l; }
  }
  TBAR();
  STAMP(7);
  if (vt < 256) {
    const int hh = vt >> 7, pos = vt & 127;
    const int d = (pos & 96) + 4 * (pos & 7) + ((pos >> 3) & 3);
    const LAS float* mrg = (const LAS float*)(sm + L_MRG) + hh * 132;
    float M = -INFINITY;
#pragma unroll
    for (int ww = 0; ww < NW; ++ww) M = fmaxf(M, mrg[ww * 264]);
    float L = 0.f, o = 0.f;
#pragma unroll
    for (int ww = 0; ww < NW; ++ww) {
      const float wt = __builtin_amdgcn_exp2f(mrg[ww * 264] - M);
      L = fmaf(wt, mrg[ww * 264 + 1], L); o = fmaf(wt, mrg[ww * 264 + 4 + d], o);
    }
    v2u gr; gr[0] = __float_as_uint(o); gr[1] = tag_hi;
    __builtin_amdgcn_raw_buffer_store_b64(gr, tr, WS_H1 + bid * H1_STRIDE + vt * 8, 0, AUX_SC1);
    if (pos < 2) {
      gr[0] = __float_as_uint(pos ? L : M);
      __builtin_amdgcn_raw_buffer_store_b64(gr, tr, WS_H1 + bid * H1_STRIDE + (256 + 2 * hh + pos) * 8, 0, AUX_SC1);
    }
  }
  STAMP(14);
  {
    const int src = WS_H1 + (grp * 8 + (c & 7)) * H1_STRIDE;
    const int o_acc = src + (32 * w + 8 * g + 4 * (c >> 3)) * 8, o_ml = src + (256 + 2 * (w >> 2)) * 8;
    v4u p0, p1, pm;
    if (H1MODE == 1) {
      if (w == 7) {
        const int o_w = WS_H1 + (grp * 8 + (lane & 7)) * H1_STRIDE + (256 + 2 * ((lane >> 3) & 1)) * 8;
        for (unsigned spins = 0;; ++spins) {
          const v4u t = __builtin_amdgcn_raw_buffer_load_b128(tr, o_w, 0, AUX_SC1);
          if (__all(t[1] == tag_hi && t[3] == tag_hi) || spins >= cap) break;
          __builtin_amdgcn_s_sleep(1);
        }
      }
      TBAR();
      STAMP(15);
    } else if (H1MODE == 2) {
      for (unsigned spins = 0;; ++spins) {
        const v4u t = __builtin_amdgcn_raw_buffer_load_b128(tr, o_ml, 0, AUX_SC1);
        if (__all(t[1] == tag_hi && t[3] == tag_hi) || spins >= cap) break;
        __builtin_amdgcn_s_sleep(2);
      }
    }
    for (unsigned spins = 0;; ++spins) {
      p0 = __builtin_amdgcn_raw_buffer_load_b128(tr, o_acc, 0, AUX_SC1);
      p1 = __builtin_amdgcn_raw_buffer_load_b128(tr, o_acc + 16, 0, AUX_SC1);
      pm = __builtin_amdgcn_raw_buffer_load_b128(tr, o_ml, 0, AUX_SC1);
      const bool ok = p0[1] == tag_hi && p0[3] == tag_hi && p1[1] == tag_hi && p1[3] == tag_hi && pm[1] == tag_hi && pm[3] == tag_hi;
      if (__all(ok) || spins >= cap) break;
      __builtin_amdgcn_s_sleep(2);
    }
    STAMP(8);
    const float Ms = __uint_as_float(pm[0]), Ls = __uint_as_float(pm[2]);
    float Mx = fmaxf(Ms, dpp<XOR1>(Ms)); Mx = fmaxf(Mx, dpp<XOR2>(Mx)); Mx = fmaxf(Mx, dpp<HMIR>(Mx));
    const float wt = __builtin_amdgcn_exp2f(Ms - Mx);
    float v[5] = {wt * __uint_as_float(p0[0]), wt * __uint_as_float(p0[2]), wt * __uint_as_float(p1[0]), wt * __uint_as_float(p1[2]), wt * Ls};
#pragma unroll
    for (int i = 0; i < 5; ++i) { v[i] += dpp<XOR1>(v[i]); v[i] += dpp<XOR2>(v[i]); v[i] += dpp<HMIR>(v[i]); }
    const float inv = 1.f / v[4];
    f32x4 y = {0.f, 0.f, 0.f, 0.f};
#pragma unroll
    for (int i = 0; i < 8; ++i) {
      const float ov = v[i & 3] * inv;
      const unsigned pp = ((i >> 2) != 0) == hi8 ? pk_bf16(ov, ov) : 0u;
      v4u bb; bb[0] = pp & msk[0]; bb[1] = pp & msk[1]; bb[2] = pp & msk[2]; bb[3] = pp & msk[3];
      y = mfma(WO[i], bb, y);
    }
    if (g < 2 || w == sp) {
      const int rowi = g < 2 ? w * 2 + (c >> 3) : 16 + (c >> 3);
#pragma unroll
      for (int r = 0; r < 4; ++r) *(LAS float*)(sm + L_RED + (rowi * 64 + ((4 * g + r) & 7) * 8 + (c & 7)) * 4) = y[r];
    }
  }
  TBAR();
  STAMP(9);
  if (vt < 128) {
    float s;
    if (vt < 64) {
      s = 0.f;
#pragma unroll
      for (int i = 0; i < 16; ++i) s += *(const LAS float*)(sm + L_RED + (i * 64 + vt) * 4);
    } else {
      s = *(const LAS float*)(sm + L_RED + (16 * 64 + vt - 64) * 4) + *(const LAS float*)(sm + L_RED + (17 * 64 + vt - 64) * 4);
    }
    v2u gr; gr[0] = __float_as_uint(s); gr[1] = tag_hi;
    __builtin_amdgcn_raw_buffer_store_b64(gr, tr, vt < 64 ? WS_H2 + bid * 512 + vt * 8 : WS_H2B + ((b * 8 + ((vt - 64) >> 3)) * 128 + (bid & 127)) * 64 + (vt & 7) * 8, 0, AUX_SC1);
  }
  if (is_final) {
    const float gt = *(const LAS float*)(sm + L_FFN + 72 * 4), og = rbf(1.f - gt);
    const int srcA = WS_H2 + ((b * 16 + c) * 8 + fs) * 512 + (4 * w + g) * 16;
    const int srcB = WS_H2B + (b * 8 + fs) * 8192 + w * 1024 + voff;
    v4u pa, pb;
    for (unsigned spins = 0;; ++spins) {
      pa = __builtin_amdgcn_raw_buffer_load_b128(tr, srcA, 0, AUX_SC1);
      pb = __builtin_amdgcn_raw_buffer_load_b128(tr, srcB, 0, AUX_SC1);
      const bool ok = pa[1] == tag_hi && pa[3] == tag_hi && pb[1] == tag_hi && pb[3] == tag_hi;
      if (__all(ok) || spins >= cap) break;
      __builtin_amdgcn_s_sleep(1);
    }
    STAMP(10);
    {
      float b0 = __uint_as_float(pb[0]), b1 = __uint_as_float(pb[2]);
      b0 += dpp<ROR4>(b0); b1 += dpp<ROR4>(b1); b0 += dpp<ROR8>(b0); b1 += dpp<ROR8>(b1);
      b0 = xrow_sum(b0); b1 = xrow_sum(b1);
      if (lane < 4) { f32x2 t; t[0] = b0; t[1] = b1; *(LAS f32x2*)(sm + L_FG + (w * 4 + lane) * 8) = t; }
      float a0 = __uint_as_float(pa[0]), a1 = __uint_as_float(pa[2]);
      a0 += dpp<XOR1>(a0); a1 += dpp<XOR1>(a1); a0 += dpp<XOR2>(a0); a1 += dpp<XOR2>(a1);
      a0 += dpp<ROR4>(a0); a1 += dpp<ROR4>(a1); a0 += dpp<ROR8>(a0); a1 += dpp<ROR8>(a1);
      const int cp = 4 * w + g;
      const f32x2 fv = *(const LAS f32x2*)(sm + L_FFN + cp * 8);
      const float r0 = rbf(gt * rbf(a0)) + rbf(og * rbf(fv[0])), r1 = rbf(gt * rbf(a1)) + rbf(og * rbf(fv[1]));
      if (c == 0) __builtin_amdgcn_raw_buffer_store_b32(pk_bf16(r0, r1), outr, (b * D + fs * 64) * 2 + cp * 4, 0, 0);
    }
    TBAR();
    if (vt < 4) {
      float b0 = 0.f, b1 = 0.f;
#pragma unroll
      for (int i = 0; i < 8; ++i) { const f32x2 t = *(const LAS f32x2*)(sm + L_FG + (i * 4 + vt) * 8); b0 += t[0]; b1 += t[1]; }
      const f32x2 fv = *(const LAS f32x2*)(sm + L_FFN + (32 + vt) * 8);
      const float r0 = rbf(gt * rbf(b0)) + rbf(og * rbf(fv[0])), r1 = rbf(gt * rbf(b1)) + rbf(og * rbf(fv[1]));
      __builtin_amdgcn_raw_buffer_store_b32(pk_bf16(r0, r1), outr, (b * D + 512 + fs * 8) * 2 + vt * 4, 0, 0);
    }
  }
  if (dry == 0) break;
  dry = 0;
  }
#undef NEXT
  STAMP(11);
  STAMP_FLUSH();
}

extern "C" void kernel_launch(void* const* d_in, const int* in_sizes, int n_in, void* d_out, int out_size, void* d_ws, size_t ws_size,
                              hipStream_t stream) {
  k_fused<<<256, NTHR, 0, stream>>>((const unsigned short*)d_in[0], (const unsigned char*)d_in[1], (const unsigned char*)d_in[2],
                                    (const unsigned char*)d_in[3], (const unsigned char*)d_in[4], (const unsigned char*)d_in[6],
                                    (const unsigned char*)d_in[7], (const unsigned char*)d_in[8], (const unsigned short*)d_in[9],
                                    (unsigned char*)d_ws, (unsigned*)d_out);
}
```

```cpp
#define VLO 0
#define VHI 8
#include <hip/hip_runtime.h>
#include <hip/hip_bf16.h>
#include <stdint.h>

typedef unsigned v4u __attribute__((ext_vector_type(4)));
typedef unsigned v2u __attribute__((ext_vector_type(2)));
typedef float f32x4 __attribute__((ext_vector_type(4)));
typedef float f32x2 __attribute__((ext_vector_type(2)));
typedef short bf16x8 __attribute__((ext_vector_type(8)));
typedef __bf16 bf16x2 __attribute__((ext_vector_type(2)));
typedef __amdgpu_buffer_rsrc_t rsrc_t;
#define LAS __attribute__((address_space(3)))

constexpr int D = 576, SK = 32768, F = 4608;
constexpr int NW = 8, NTHR = NW * 64;
constexpr unsigned NT = 2048;
#define NSW_V 200
constexpr unsigned NSW = NSW_V;
constexpr unsigned DB = 8 * NSW;
#define KLO 32
#define KHI 32
#define AUXW 2
#define NSTR 7
#define HJOIN 1
#define DRYRUN 1
#define H1MODE 1
constexpr unsigned RSV = NSTR;
constexpr unsigned NPRE = 7;
constexpr int AUX_NT = 2, AUX_SC1 = 16;
constexpr int OOB = 0x40000000;
constexpr unsigned SPIN_CAP = 400000u;
#define SLEEP0 4
#define SLEEPW 12

constexpr int WS_E = 0;
constexpr int WS_CNT = 4096;
constexpr int WS_QG = 16384;
constexpr int WS_H1 = WS_QG + 131072;
constexpr int H1_STRIDE = 264 * 8;
constexpr int WS_H2 = WS_H1 + 256 * H1_STRIDE;
constexpr int WS_H2B = WS_H2 + 256 * 512;
constexpr int WS_FF = WS_H2 + 256 * 1024;
constexpr int WS_FP = WS_FF + 144 * 64;
constexpr int WS_END = WS_FP + 144 * 4608;

constexpr int L_XS = 0;
constexpr int L_QP = 2304;
constexpr int L_QS = L_QP + 2048;
constexpr int L_MRG = L_QS + 512;
constexpr int L_RED = L_MRG + 8448;
constexpr int L_GU = L_RED + 4608;
constexpr int L_HS = L_GU + 1536;
constexpr int L_FP = L_HS + 256;
constexpr int L_FG = L_FP + 4608;
constexpr int L_FFN = L_FG + 864;
constexpr int L_TICK = L_FFN + 304;
constexpr int L_END = L_TICK + 16;
constexpr int LDS_BYTES = 96 * 1024;
static_assert(L_END <= LDS_BYTES, "lds");

__device__ __forceinline__ float bflo(unsigned u) { return __builtin_bit_cast(float, u << 16); }
__device__ __forceinline__ float bfhi(unsigned u) { return __builtin_bit_cast(float, u & 0xffff0000u); }
__device__ __forceinline__ unsigned pk_bf16(float lo, float hi) {
  bf16x2 v; v[0] = (__bf16)lo; v[1] = (__bf16)hi; return __builtin_bit_cast(unsigned, v);
}
__device__ __forceinline__ float rbf(float f) { return bflo(pk_bf16(f, f)); }
__device__ __forceinline__ float dot2(unsigned a, unsigned b, float c) {
  return __builtin_amdgcn_fdot2_f32_bf16(__builtin_bit_cast(bf16x2, a), __builtin_bit_cast(bf16x2, b), c, false);
}
template <int CTRL> __device__ __forceinline__ float dpp(float x) {
  return __builtin_bit_cast(float, __builtin_amdgcn_mov_dpp(__builtin_bit_cast(int, x), CTRL, 0xf, 0xf, true));
}
constexpr int XOR1 = 0xB1, XOR2 = 0x4E, HMIR = 0x141, ROR4 = 0x124, ROR8 = 0x128;
__device__ __forceinline__ f32x4 mfma(v4u a, v4u b, f32x4 c) {
  return __builtin_amdgcn_mfma_f32_16x16x32_bf16(__builtin_bit_cast(bf16x8, a), __builtin_bit_cast(bf16x8, b), c, 0, 0, 0);
}
__device__ __forceinline__ float swap16_sum(float a, float b) {
  auto s = __builtin_amdgcn_permlane16_swap(__float_as_uint(a), __float_as_uint(b), false, false);
  return __uint_as_float(s[0]) + __uint_as_float(s[1]);
}
__device__ __forceinline__ float swap32_sum(float a, float b) {
  auto s = __builtin_amdgcn_permlane32_swap(__float_as_uint(a), __float_as_uint(b), false, false);
  return __uint_as_float(s[0]) + __uint_as_float(s[1]);
}
__device__ __forceinline__ float xrow_max(float x) {
  auto s = __builtin_amdgcn_permlane16_swap(__float_as_uint(x), __float_as_uint(x), false, false);
  x = fmaxf(__uint_as_float(s[0]), __uint_as_float(s[1]));
  auto t = __builtin_amdgcn_permlane32_swap(__float_as_uint(x), __float_as_uint(x), false, false);
  return fmaxf(__uint_as_float(t[0]), __uint_as_float(t[1]));
}
__device__ __forceinline__ float xrow_sum(float x) { x = swap16_sum(x, x); return swap32_sum(x, x); }
__device__ __forceinline__ rsrc_t mk_rsrc(const void* p, int bytes) { return __builtin_amdgcn_make_buffer_rsrc((void*)p, 0, bytes, 0x00020000); }
#define BAR() do { asm volatile("s_waitcnt lgkmcnt(0)" ::: "memory"); __builtin_amdgcn_s_barrier(); asm volatile("" ::: "memory"); } while (0)
#define DRAIN() asm volatile("s_waitcnt vmcnt(0)" ::: "memory")

#define STAMP(i) do {} while (0)
#define STAMP_FLUSH() do {} while (0)
constexpr float SCALE = 0.08838834764831845f, L2E = 1.4426950408889634f;

struct St { float m, l; f32x4 acc; };
__device__ __forceinline__ void pin(St& st) { asm volatile("" : "+v"(st.m), "+v"(st.l), "+v"(st.acc) :: "memory"); }

__device__ __forceinline__ void process(const v4u (&KB)[4], const v4u (&VB)[4], const v4u& q0, const v4u& q1, const v4u& msk, bool hi8, St& st) {
  float s[4];
#pragma unroll
  for (int t = 0; t < 4; ++t) {
    float a0 = dot2(KB[t][0], q0[0], 0.f), a1 = dot2(KB[t][0], q1[0], 0.f);
#pragma unroll
    for (int k = 1; k < 4; ++k) { a0 = dot2(KB[t][k], q0[k], a0); a1 = dot2(KB[t][k], q1[k], a1); }
    const float keep = hi8 ? a1 : a0, send = hi8 ? a0 : a1;
    float v = keep + dpp<ROR8>(send);
    v += dpp<HMIR>(v); v += dpp<XOR2>(v); v += dpp<XOR1>(v);
    s[t] = v;
  }
  {
    const unsigned u0 = pk_bf16(s[0], s[1]), u1 = pk_bf16(s[2], s[3]);
    const unsigned w0 = pk_bf16(bflo(u0) * SCALE, bfhi(u0) * SCALE), w1 = pk_bf16(bflo(u1) * SCALE, bfhi(u1) * SCALE);
    s[0] = bflo(w0); s[1] = bfhi(w0); s[2] = bflo(w1); s[3] = bfhi(w1);
  }
  float tm = fmaxf(fmaxf(s[0], s[1]), fmaxf(s[2], s[3]));
  tm = xrow_max(tm);
  const float mn = fmaxf(st.m, __builtin_ceilf(tm * L2E));
  const float alpha = __builtin_amdgcn_exp2f(st.m - mn);
  st.m = mn;
  float p[4];
#pragma unroll
  for (int t = 0; t < 4; ++t) p[t] = __builtin_amdgcn_exp2f(fmaf(s[t], L2E, -mn));
  st.l = fmaf(st.l, alpha, (p[0] + p[1]) + (p[2] + p[3]));
  st.acc *= alpha;
#pragma unroll
  for (int t = 0; t < 4; ++t) {
    const unsigned pp = pk_bf16(p[t], p[t]);
    v4u bb; bb[0] = pp & msk[0]; bb[1] = pp & msk[1]; bb[2] = pp & msk[2]; bb[3] = pp & msk[3];
    st.acc = mfma(VB[t], bb, st.acc);
  }
}
__device__ __forceinline__ void issue(v4u (&KB)[4], v4u (&VB)[4], rsrc_t kr, rsrc_t vr, int voff, unsigned tile, bool kdef, bool vdef) {
  const int so = (int)(tile * 4096u);
  if (kdef) {
#pragma unroll
    for (int t = 0; t < 4; ++t) KB[t] = __builtin_amdgcn_raw_buffer_load_b128(kr, voff + t * 1024, so, 0);
  } else {
#pragma unroll
    for (int t = 0; t < 4; ++t) KB[t] = __builtin_amdgcn_raw_buffer_load_b128(kr, voff + t * 1024, so, AUX_NT);
  }
  if (vdef) {
#pragma unroll
    for (int t = 0; t < 4; ++t) VB[t] = __builtin_amdgcn_raw_buffer_load_b128(vr, voff + t * 1024, so, 0);
  } else {
#pragma unroll
    for (int t = 0; t < 4; ++t) VB[t] = __builtin_amdgcn_raw_buffer_load_b128(vr, voff + t * 1024, so, AUX_NT);
  }
}
__device__ __forceinline__ void atomic_inc_cond(unsigned& a, const void* head, unsigned x, unsigned lim, unsigned vzero, unsigned vone) {
  unsigned long long keep;
  asm volatile("s_cmp_lt_u32 %5, %6\n\ts_cbranch_scc0 1f\n\ts_mov_b64 %1, exec\n\ts_mov_b64 exec, 1\n\tglobal_atomic_add %0, %2, %3, %4 sc0\n\ts_mov_b64 exec, %1\n1:"
               : "+v"(a), "=&s"(keep) : "v"(vzero), "v"(vone), "s"(head), "s"(x), "s"(lim) : "memory", "scc");
}
__device__ __forceinline__ unsigned atomic_inc_sync(const void* head, unsigned vzero, unsigned vone) {
  unsigned long long keep; unsigned r, s;
  asm volatile("s_mov_b64 %1, exec\n\ts_mov_b64 exec, 1\n\tglobal_atomic_add %0, %3, %4, %5 sc0\n\ts_mov_b64 exec, %1\n\ts_waitcnt vmcnt(0)\n\tv_readfirstlane_b32 %2, %0\n\ts_nop 1"
               : "=&v"(r), "=&s"(keep), "=s"(s) : "v"(vzero), "v"(vone), "s"(head) : "memory");
  return s;
}
__device__ __forceinline__ unsigned lds_ticket(unsigned lds_addr_v, unsigned vone) {
  unsigned long long keep; unsigned r, s;
  asm volatile("s_mov_b64 %1, exec\n\ts_mov_b64 exec, 1\n\tds_add_rtn_u32 %0, %3, %4\n\ts_mov_b64 exec, %1\n\ts_waitcnt lgkmcnt(0)\n\tv_readfirstlane_b32 %2, %0\n\ts_nop 1"
               : "=&v"(r), "=&s"(keep), "=s"(s) : "v"(lds_addr_v), "v"(vone) : "memory");
  return s;
}

__device__ __forceinline__ void fma8(float (&acc)[8], float xv, const v4u& w) {
#pragma unroll
  for (int k = 0; k < 4; ++k) { acc[2 * k] = fmaf(xv, bflo(w[k]), acc[2 * k]); acc[2 * k + 1] = fmaf(xv, bfhi(w[k]), acc[2 * k + 1]); }
}
template <bool BIT2> __device__ __forceinline__ f32x2 red8(const float (&acc)[8]) {
  float a[4];
#pragma unroll
  for (int j = 0; j < 4; ++j) a[j] = swap32_sum(acc[j], acc[j + 4]);
  float b0 = swap16_sum(a[0], a[2]), b1 = swap16_sum(a[1], a[3]);
  if (BIT2) { b0 += dpp<ROR4>(b0); b1 += dpp<ROR4>(b1); }
  b0 += dpp<ROR8>(b0); b1 += dpp<ROR8>(b1);
  f32x2 r; r[0] = b0; r[1] = b1; return r;
}

__global__ void __launch_bounds__(NTHR, 2)
k_fused(const unsigned short* __restrict__ x, const unsigned char* Kc, const unsigned char* Vc,
        const unsigned char* __restrict__ wq, const unsigned char* wo, const unsigned char* wg,
        const unsigned char* wu, const unsigned char* __restrict__ wd, const unsigned short* __restrict__ gw,
        unsigned char* ws, unsigned* out) {
  __shared__ __attribute__((aligned(16))) unsigned char smem[LDS_BYTES];
  const int tid = threadIdx.x, lane = tid & 63, w = __builtin_amdgcn_readfirstlane(tid >> 6);
  const int bid = blockIdx.x, grp = bid >> 3, sp = bid & 7, b = grp >> 4, kvh = grp & 15;
  const int g = lane >> 4, c = lane & 15;
  unsigned tsv = 0xffffffffu; (void)tsv;
  STAMP(0);
  const rsrc_t wsr = mk_rsrc(ws, WS_END);
  const int voff = lane * 16;
  if (w == 7) {
    const v4u z = {0u, 0u, 0u, 0u};
    __builtin_amdgcn_raw_buffer_store_b128(z, wsr, WS_H1 + bid * H1_STRIDE + voff, 0, AUX_SC1);
    __builtin_amdgcn_raw_buffer_store_b128(z, wsr, WS_H1 + bid * H1_STRIDE + 1024 + voff, 0, AUX_SC1);
    __builtin_amdgcn_raw_buffer_store_b128(z, wsr, lane < 32 ? WS_H2 + bid * 512 + voff : WS_H2B + ((b * 8 + ((lane - 32) >> 2)) * 128 + (bid & 127)) * 64 + (lane & 3) * 16, 0, AUX_SC1);
    int off = OOB;
    if (lane < 4) off = WS_H1 + bid * H1_STRIDE + 2048 + voff;
    else if (lane == 4 && sp == 0) off = WS_CNT + grp * 256;
    __builtin_amdgcn_raw_buffer_store_b128(z, wsr, off, 0, AUX_SC1);
  }
  unsigned short xq[5];
  v4u wqv[5];
  {
    const int r = lane >> 3, row0 = 288 * (sp & 1) + 36 * w;
    const rsrc_t qr = mk_rsrc(wq, D * 8192);
    const int base = (row0 + r) * 8192 + (kvh * 256 + (sp >> 1) * 64) * 2 + (lane & 7) * 16;
#pragma unroll
    for (int i = 0; i < 4; ++i) wqv[i] = __builtin_amdgcn_raw_buffer_load_b128(qr, base, i * 8 * 8192, AUXW);
    wqv[4] = __builtin_amdgcn_raw_buffer_load_b128(qr, r < 4 ? base : OOB, 4 * 8 * 8192, AUXW);
#pragma unroll
    for (int i = 0; i < 5; ++i) xq[i] = x[b * D + row0 + 8 * i + (i < 4 ? r : (r & 3))];
  }
  const int xi = tid < 288 ? tid : 287;
  const unsigned xa = ((const unsigned*)x)[xi], xb = ((const unsigned*)x)[288 + xi];
  __builtin_amdgcn_sched_barrier(0);
  const rsrc_t kr = mk_rsrc(Kc + (size_t)grp * (SK * 256), SK * 256);
  const rsrc_t vr = mk_rsrc(Vc + (size_t)grp * (SK * 256), SK * 256);
  const unsigned sbase = (unsigned)sp * NSW;
  const bool kdef = grp >= KLO && grp < KHI, vdef = grp >= VLO && grp < VHI;
  unsigned long long ev;
  v4u Kb[2][4], Vb[2][4];
  auto qpart = [&]() {
    asm volatile("" : "+v"(wqv[0]), "+v"(wqv[1]), "+v"(wqv[2]), "+v"(wqv[3]), "+v"(wqv[4]));
    float acc[8];
#pragma unroll
    for (int j = 0; j < 8; ++j) acc[j] = 0.f;
#pragma unroll
    for (int i = 0; i < 5; ++i) fma8(acc, bflo(xq[i]), wqv[i]);
    STAMP(12);
    const f32x2 r2 = red8<false>(acc);
    if ((lane & 8) == 0) *(LAS f32x2*)(smem + L_QP + (w * 64 + (lane & 7) * 8 + 4 * (lane >> 5) + 2 * ((lane >> 4) & 1)) * 4) = r2;
    if (tid < 288) {
      v2u xs2; xs2[0] = (xa & 0xffffu) | (xb << 16); xs2[1] = (xa >> 16) | (xb & 0xffff0000u);
      *(LAS v2u*)(smem + L_XS + tid * 8) = xs2;
    }
    if (tid == 0) { v4u t4 = {2 * NPRE, 0u, 0u, 0u}; *(LAS v4u*)(smem + L_TICK) = t4; }
    BAR();
    STAMP(2);
  };
  unsigned long long tag64;
  unsigned tag_hi, tag_lo8;
  auto mktag = [&]() {
    asm volatile("" : "+s"(ev));
    tag64 = ev * 6364136223846793005ull + 1442695040888963407ull;
    tag_hi = (unsigned)(tag64 >> 32) | 1u;
    tag_lo8 = ((unsigned)tag64 >> 24) & 0xffu;
  };

  const int ridx = (grp + 4 * sp) & 31;
  const bool has_task = ridx < 18;
  const int task = sp * 18 + ridx;
  const bool is_final = ridx >= 30;
  const int fs = (kvh >> 2) * 2 + (kvh & 1);

  v4u WO[8];
  const rsrc_t wor = mk_rsrc(wo, 4096 * D * 2);
  const int wo_off = (c < 8 || w == sp) ? (kvh * 256 + 32 * w + g) * 1152 + (c < 8 ? sp * 128 + c * 16 : 1024 + (c - 8) * 16) : OOB;
  v4u msk;
#pragma unroll
  for (int d = 0; d < 4; ++d) msk[d] = (((c & 7) >> 1) == d) ? ((c & 1) ? 0xffff0000u : 0x0000ffffu) : 0u;
  const bool hi8 = (c & 8) != 0;
  St st; st.m = -INFINITY; st.l = 0.f; st.acc = f32x4{0.f, 0.f, 0.f, 0.f};
  const unsigned char* head = ws + WS_CNT + grp * 256;
  unsigned vzero = 0u, vone = 1u, a = 0u;
  unsigned taddr = (unsigned)(size_t)(LAS unsigned char*)(smem + L_TICK);
  asm volatile("" : "+v"(vzero), "+v"(vone), "+v"(taddr), "+v"(a));
  unsigned lastt = 0;
  v4u q0, q1;

#define NEXT(nx)                                                                                                   \
  {                                                                                                                \
    unsigned cmpv;                                                                                                 \
    if (lastt + RSV < NSW) {                                                                                       \
      const unsigned t = lds_ticket(taddr, vone);                                                                  \
      lastt = t;                                                                                                   \
      if (t < NSW) { nx = sbase + t; cmpv = (t + RSV >= NSW) ? 0u : NT; }                                          \
      else { nx = atomic_inc_sync(head, vzero, vone) + DB; if (nx > NT) nx = NT; cmpv = nx; }                      \
    } else {                                                                                                       \
      nx = __builtin_amdgcn_readfirstlane(a) + DB; if (nx > NT) nx = NT;                                           \
      cmpv = nx;                                                                                                   \
    }                                                                                                              \
    atomic_inc_cond(a, head, cmpv, NT, vzero, vone);                                                               \
  }

  unsigned first = 0;
  asm volatile("" ::: "memory");
  __builtin_amdgcn_s_barrier();
  asm volatile("s_load_dwordx2 %0, %1, 0x0" : "=s"(ev) : "s"(ws) : "memory");
  STAMP(1);
  if (w == 7) {
    qpart();
    mktag();
    {
      float s = 0.f;
#pragma unroll
      for (int ww = 0; ww < NW; ++ww) s += *(const LAS float*)(smem + L_QP + (ww * 64 + lane) * 4);
      DRAIN();
      v2u gr; gr[0] = ((__float_as_uint(s) + 0x80u) & 0xffffff00u) | tag_lo8; gr[1] = tag_hi;
      __builtin_amdgcn_raw_buffer_store_b64(gr, wsr, WS_QG + ((grp * 2 + (sp & 1)) * 256 + (sp >> 1) * 64 + lane) * 8, 0, AUX_SC1);
    }
    STAMP(13);
    for (unsigned spins = 0;; ++spins) {
      v4u p[4];
#pragma unroll
      for (int i = 0; i < 4; ++i) p[i] = __builtin_amdgcn_raw_buffer_load_b128(wsr, WS_QG + grp * 4096 + lane * 32 + (i & 1) * 16 + (i >> 1) * 2048, 0, AUX_SC1);
      bool ok = true;
#pragma unroll
      for (int i = 0; i < 4; ++i) ok = ok && p[i][1] == tag_hi && p[i][3] == tag_hi && (p[i][0] & 0xffu) == tag_lo8 && (p[i][2] & 0xffu) == tag_lo8;
      if (__all(ok) || spins > SPIN_CAP) {
        float qv[4];
#pragma unroll
        for (int i = 0; i < 4; ++i)
          qv[i] = __uint_as_float(p[i >> 1][(i & 1) * 2] & 0xffffff00u) + __uint_as_float(p[2 + (i >> 1)][(i & 1) * 2] & 0xffffff00u);
        v2u qq; qq[0] = pk_bf16(qv[0], qv[1]); qq[1] = pk_bf16(qv[2], qv[3]);
        *(LAS v2u*)(smem + L_QS + lane * 8) = qq;
        break;
      }
      __builtin_amdgcn_s_sleep(2);
    }
    asm volatile("s_waitcnt lgkmcnt(0)" ::: "memory");
    if (lane == 0) *(volatile LAS unsigned*)(smem + L_TICK + 8) = 1u;
    STAMP(3);
#pragma unroll
    for (int i = 0; i < 8; ++i) WO[i] = __builtin_amdgcn_raw_buffer_load_b128(wor, wo_off, i * 4608, AUXW);
    q0 = *(const LAS v4u*)(smem + L_QS + c * 16); q1 = *(const LAS v4u*)(smem + L_QS + 256 + c * 16);
    if (has_task) {
      const rsrc_t dr = mk_rsrc(wd, F * D * 2);
      const int dbase = (task * 32 + g) * 1152 + c * 16;
      f32x4 da[5];
#pragma unroll
      for (int cb = 0; cb < 5; ++cb) da[cb] = f32x4{0.f, 0.f, 0.f, 0.f};
#pragma unroll
      for (int hf = 0; hf < 2; ++hf) {
        v4u wv[20];
#pragma unroll
        for (int i = 0; i < 4; ++i)
#pragma unroll
          for (int cb = 0; cb < 5; ++cb)
            wv[i * 5 + cb] = __builtin_amdgcn_raw_buffer_load_b128(dr, (cb == 4 && c >= 8) ? OOB : dbase + cb * 256, (hf * 4 + i) * 4608, AUXW);
        __builtin_amdgcn_sched_barrier(0);
        if (hf == 0) {
          for (unsigned spins = 0; spins < 64u * SPIN_CAP; ++spins) {
            if (*(volatile LAS unsigned*)(smem + L_TICK + 4) >= 6u) break;
            __builtin_amdgcn_s_sleep(4);
          }
          asm volatile("" ::: "memory");
          const LAS float* gu = (const LAS float*)(smem + L_GU) + lane;
          const float gv = rbf(gu[0] + gu[64] + gu[128]), uv = rbf(gu[192] + gu[256] + gu[320]);
          const float inner = 0.7978845608028654f * (gv + 0.044715f * gv * gv * gv);
          const float ge = rbf(0.5f * gv * (1.f + tanhf(inner)));
          *(LAS float*)(smem + L_HS + lane * 4) = rbf(ge * uv);
          asm volatile("s_waitcnt lgkmcnt(0)" ::: "memory");
        }
#pragma unroll
        for (int i = 0; i < 4; ++i) {
          const float hv = *(const LAS float*)(smem + L_HS + ((c >> 3) * 32 + (hf * 4 + i) * 4 + g) * 4);
          const unsigned pp = pk_bf16(hv, hv);
          v4u bb; bb[0] = pp & msk[0]; bb[1] = pp & msk[1]; bb[2] = pp & msk[2]; bb[3] = pp & msk[3];
#pragma unroll
          for (int cb = 0; cb < 5; ++cb) da[cb] = mfma(wv[i * 5 + cb], bb, da[cb]);
        }
        __builtin_amdgcn_sched_barrier(0);
      }
#pragma unroll
      for (int cb = 0; cb < 5; ++cb)
#pragma unroll
        for (int rr = 0; rr < 4; ++rr) {
          const int col = cb * 128 + (4 * g + rr) * 8 + (c & 7);
          if (col < D) *(LAS float*)(smem + L_FP + ((c >> 3) * D + col) * 4) = da[cb][rr];
        }
      asm volatile("s_waitcnt lgkmcnt(0)" ::: "memory");
#pragma unroll
      for (int i = 0; i < 5; ++i) {
        const int o = (i * 64 + lane) * 16;
        if (o < 4608) {
          const v4u v = *(const LAS v4u*)(smem + L_FP + o);
          __builtin_amdgcn_raw_buffer_store_b128(v, wsr, WS_FP + task * 4608 + o, 0, AUX_SC1);
        }
      }
      DRAIN();
      if (lane == 0) {
        v2u fl; fl[0] = (unsigned)tag64; fl[1] = tag_hi;
        __builtin_amdgcn_raw_buffer_store_b64(fl, wsr, WS_FF + task * 64, 0, AUX_SC1);
      }
    }
    STAMP(4);
    if (is_final) {
      for (unsigned spins = 0;; ++spins) {
        bool ok = true;
#pragma unroll
        for (int i = 0; i < 4; ++i) {
          const v2u p = __builtin_amdgcn_raw_buffer_load_b64(wsr, WS_QG + (i * 64 + lane) * 512, 0, AUX_SC1);
          ok = ok && p[1] == tag_hi && (p[0] & 0xffu) == tag_lo8;
        }
        if (__all(ok) || spins > SPIN_CAP) break;
        __builtin_amdgcn_s_sleep(16);
      }
      if (grp == 2 && lane == 0) {
        v2u e; e[0] = (unsigned)tag64; e[1] = (unsigned)(tag64 >> 32);
        __builtin_amdgcn_raw_buffer_store_b64(e, wsr, WS_E, 0, AUX_SC1);
      }
      {
        float z = 0.f;
#pragma unroll
        for (int i = 0; i < 9; ++i) {
          const unsigned xv = *(const LAS unsigned*)(smem + L_XS + (i * 64 + lane) * 4);
          z += rbf((b ? bfhi(xv) : bflo(xv)) * bflo(gw[i * 64 + lane]));
        }
        z += dpp<XOR1>(z); z += dpp<XOR2>(z); z += dpp<ROR4>(z); z += dpp<ROR8>(z);
        z = rbf(xrow_sum(z));
        if (lane == 0) *(LAS float*)(smem + L_FFN + 72 * 4) = rbf(1.f / (1.f + __expf(-z)));
      }
      for (unsigned spins = 0;; ++spins) {
        bool ok = true;
#pragma unroll
        for (int i = 0; i < 3; ++i) {
          const int tk = i * 64 + lane;
          const v2u p = __builtin_amdgcn_raw_buffer_load_b64(wsr, WS_FF + (tk < 144 ? tk : 0) * 64, 0, AUX_SC1);
          ok = ok && p[1] == tag_hi && p[0] == (unsigned)tag64;
        }
        if (__all(ok) || spins > SPIN_CAP) break;
        __builtin_amdgcn_s_sleep(64);
      }
      __builtin_amdgcn_fence(__ATOMIC_ACQUIRE, "wavefront");
      {
        const int ts = lane / 18, q4 = lane - ts * 18;
        const int fcol = q4 < 16 ? fs * 64 + q4 * 4 : 512 + fs * 8 + (q4 - 16) * 4;
        const int fo = ts < 3 ? WS_FP + ts * 4608 + (b * D + fcol) * 4 : OOB;
        f32x4 fa = {0.f, 0.f, 0.f, 0.f};
#pragma unroll 1
        for (int bt = 0; bt < 2; ++bt) {
          v4u pv[24];
#pragma unroll
          for (int i = 0; i < 24; ++i) pv[i] = __builtin_amdgcn_raw_buffer_load_b128(wsr, fo, (bt * 24 + i) * 3 * 4608, AUX_SC1);
          __builtin_amdgcn_sched_barrier(0);
#pragma unroll
          for (int i = 0; i < 24; ++i) fa += __builtin_bit_cast(f32x4, pv[i]);
        }
        if (ts < 3) *(LAS f32x4*)(smem + L_FG + (ts * 72 + q4 * 4) * 4) = fa;
        asm volatile("s_waitcnt lgkmcnt(0)" ::: "memory");
#pragma unroll
        for (int i = 0; i < 2; ++i) {
          const int col = i * 64 + lane;
          if (col < 72)
            *(LAS float*)(smem + L_FFN + col * 4) = *(const LAS float*)(smem + L_FG + col * 4) + *(const LAS float*)(smem + L_FG + (72 + col) * 4) +
                                                    *(const LAS float*)(smem + L_FG + (144 + col) * 4);
        }
      }
    }
    STAMP(5);
    DRAIN();
    if (HJOIN) { NEXT(first); } else first = NT;
    issue(Kb[1], Vb[1], kr, vr, voff, first, kdef, vdef);
  } else {
    unsigned qrdy = 0;
#define PACE()                                                                                              \
    {                                                                                                       \
      __builtin_amdgcn_sched_barrier(0);                                                                    \
      if (qrdy == 0) {                                                                                      \
        DRAIN();                                                                                            \
        qrdy = __builtin_amdgcn_readfirstlane(*(volatile LAS unsigned*)(smem + L_TICK + 8));                \
      }                                                                                                     \
      __builtin_amdgcn_sched_barrier(0);                                                                    \
    }
    const int so0 = (int)((sbase + w) * 4096u), so1 = (int)((sbase + NPRE + w) * 4096u);
#pragma unroll
    for (int t = 0; t < 4; ++t) Kb[0][t] = __builtin_amdgcn_raw_buffer_load_b128(kr, voff + t * 1024, so0, AUX_NT);
    __builtin_amdgcn_sched_barrier(0);
    qpart();
    mktag();
    PACE();
#pragma unroll
    for (int t = 0; t < 4; ++t) Vb[0][t] = __builtin_amdgcn_raw_buffer_load_b128(vr, voff + t * 1024, so0, AUX_NT);
    PACE();
#pragma unroll
    for (int t = 0; t < 4; ++t) Kb[1][t] = __builtin_amdgcn_raw_buffer_load_b128(kr, voff + t * 1024, so1, AUX_NT);
    PACE();
#pragma unroll
    for (int t = 0; t < 4; ++t) Vb[1][t] = __builtin_amdgcn_raw_buffer_load_b128(vr, voff + t * 1024, so1, AUX_NT);
    PACE();
#pragma unroll
    for (int i = 0; i < 8; ++i) WO[i] = __builtin_amdgcn_raw_buffer_load_b128(wor, wo_off, i * 4608, AUXW);
    const bool gu_wave = has_task && w < 6;
    const int r = lane >> 2, mat = w >= 3 ? 1 : 0, third = w - 3 * mat;
    v4u wv[12];
    if (gu_wave) {
      const rsrc_t gr = mk_rsrc(mat ? wu : wg, D * F * 2);
      const int gbase = (third * 192 + r) * (F * 2) + task * 64 + (lane & 3) * 16;
#pragma unroll
      for (int pc = 0; pc < 3; ++pc) {
        PACE();
#pragma unroll
        for (int i = 4 * pc; i < 4 * pc + 4; ++i) wv[i] = __builtin_amdgcn_raw_buffer_load_b128(gr, gbase, i * 16 * (F * 2), AUXW);
      }
    }
    __builtin_amdgcn_sched_barrier(0);
    for (unsigned spins = 0; qrdy == 0 && spins < 64u * SPIN_CAP; ++spins) {
      qrdy = __builtin_amdgcn_readfirstlane(*(volatile LAS unsigned*)(smem + L_TICK + 8));
      if (qrdy == 0) __builtin_amdgcn_s_sleep(1);
    }
    asm volatile("" ::: "memory");
#undef PACE
    STAMP(3);
    q0 = *(const LAS v4u*)(smem + L_QS + c * 16); q1 = *(const LAS v4u*)(smem + L_QS + 256 + c * 16);
    process(Kb[0], Vb[0], q0, q1, msk, hi8, st); pin(st);
    if (gu_wave) {
      float a0[8], a1[8];
#pragma unroll
      for (int j = 0; j < 8; ++j) { a0[j] = 0.f; a1[j] = 0.f; }
#pragma unroll
      for (int i = 0; i < 12; ++i) {
        const unsigned xv = *(const LAS unsigned*)(smem + L_XS + (third * 192 + i * 16 + r) * 4);
        fma8(a0, bflo(xv), wv[i]); fma8(a1, bfhi(xv), wv[i]);
      }
      const f32x2 r0 = red8<true>(a0), r1 = red8<true>(a1);
      if ((lane & 12) == 0) {
        const int col = (lane & 3) * 8 + 4 * (lane >> 5) + 2 * ((lane >> 4) & 1);
        *(LAS f32x2*)(smem + L_GU + ((w * 2 + 0) * 32 + col) * 4) = r0;
        *(LAS f32x2*)(smem + L_GU + ((w * 2 + 1) * 32 + col) * 4) = r1;
      }
      asm volatile("s_waitcnt lgkmcnt(0)" ::: "memory");
      (void)lds_ticket(taddr + 4, vone);
    }
    if (NSTR < 7 && w >= NSTR) {
      process(Kb[1], Vb[1], q0, q1, msk, hi8, st); pin(st);
      first = NT;
    }
  }
  __builtin_amdgcn_sched_barrier(0);
  unsigned dry = DRYRUN ? (unsigned)(w + 1) >> 3 : 0u;
#pragma clang loop unroll(disable)
  for (;;) {
  asm volatile("" : "+s"(dry));
  if (dry == 0 && first < NT) {
    while (true) {
      unsigned nx;
      __builtin_amdgcn_s_waitcnt(0x0F70);
      asm volatile("" : "+v"(a) :: "memory");
      NEXT(nx);
      issue(Kb[0], Vb[0], kr, vr, voff, nx, kdef, vdef);
      __builtin_amdgcn_sched_barrier(0);
      process(Kb[1], Vb[1], q0, q1, msk, hi8, st); pin(st);
      __builtin_amdgcn_sched_barrier(0);
      if (nx >= NT) break;
      __builtin_amdgcn_s_waitcnt(0x0F70);
      asm volatile("" : "+v"(a) :: "memory");
      NEXT(nx);
      issue(Kb[1], Vb[1], kr, vr, voff, nx, kdef, vdef);
      __builtin_amdgcn_sched_barrier(0);
      process(Kb[0], Vb[0], q0, q1, msk, hi8, st); pin(st);
      __builtin_amdgcn_sched_barrier(0);
      if (nx >= NT) break;
    }
  }
  const rsrc_t tr = mk_rsrc(ws, dry ? 0 : WS_END);
  const rsrc_t outr = mk_rsrc(out, dry ? 0 : 2 * D * 2);
  LAS unsigned char* sm = (LAS unsigned char*)smem + (dry ? 49152 : 0);
  const int vt = dry ? lane : tid;
  const unsigned cap = dry ? 0u : SPIN_CAP;
#define TBAR() do { asm volatile("s_waitcnt lgkmcnt(0)" ::: "memory"); if (dry == 0) __builtin_amdgcn_s_barrier(); asm volatile("" ::: "memory"); } while (0)
  STAMP(6);
  {
    const float l = xrow_sum(st.l);
    const int h = c >> 3;
    LAS float* mrg = (LAS float*)(sm + L_MRG) + (w * 2 + h) * 132;
#pragma unroll
    for (int r = 0; r < 4; ++r) mrg[4 + 8 * (4 * g + r) + (c & 7)] = st.acc[r];
    if (g == 0 && (c & 7) == 0) { mrg[0] = st.m; mrg[1] = l; }
  }
  TBAR();
  STAMP(7);
  if (vt < 256) {
    const int hh = vt >> 7, pos = vt & 127;
    const int d = (pos & 96) + 4 * (pos & 7) + ((pos >> 3) & 3);
    const LAS float* mrg = (const LAS float*)(sm + L_MRG) + hh * 132;
    float M = -INFINITY;
#pragma unroll
    for (int ww = 0; ww < NW; ++ww) M = fmaxf(M, mrg[ww * 264]);
    float L = 0.f, o = 0.f;
#pragma unroll
    for (int ww = 0; ww < NW; ++ww) {
      const float wt = __builtin_amdgcn_exp2f(mrg[ww * 264] - M);
      L = fmaf(wt, mrg[ww * 264 + 1], L); o = fmaf(wt, mrg[ww * 264 + 4 + d], o);
    }
    v2u gr; gr[0] = __float_as_uint(o); gr[1] = tag_hi;
    __builtin_amdgcn_raw_buffer_store_b64(gr, tr, WS_H1 + bid * H1_STRIDE + vt * 8, 0, AUX_SC1);
    if (pos < 2) {
      gr[0] = __float_as_uint(pos ? L : M);
      __builtin_amdgcn_raw_buffer_store_b64(gr, tr, WS_H1 + bid * H1_STRIDE + (256 + 2 * hh + pos) * 8, 0, AUX_SC1);
    }
  }
  STAMP(14);
  {
    const int src = WS_H1 + (grp * 8 + (c & 7)) * H1_STRIDE;
    const int o_acc = src + (32 * w + 8 * g + 4 * (c >> 3)) * 8, o_ml = src + (256 + 2 * (w >> 2)) * 8;
    v4u p0, p1, pm;
    if (H1MODE == 1) {
      if (w == 7) {
        const int o_w = WS_H1 + (grp * 8 + (lane & 7)) * H1_STRIDE + (256 + 2 * ((lane >> 3) & 1)) * 8;
        for (unsigned spins = 0;; ++spins) {
          const v4u t = __builtin_amdgcn_raw_buffer_load_b128(tr, o_w, 0, AUX_SC1);
          if (__all(t[1] == tag_hi && t[3] == tag_hi) || spins >= cap) break;
          __builtin_amdgcn_s_sleep(1);
        }
      }
      TBAR();
      STAMP(15);
    } else if (H1MODE == 2) {
      for (unsigned spins = 0;; ++spins) {
        const v4u t = __builtin_amdgcn_raw_buffer_load_b128(tr, o_ml, 0, AUX_SC1);
        if (__all(t[1] == tag_hi && t[3] == tag_hi) || spins >= cap) break;
        __builtin_amdgcn_s_sleep(2);
      }
    }
    for (unsigned spins = 0;; ++spins) {
      p0 = __builtin_amdgcn_raw_buffer_load_b128(tr, o_acc, 0, AUX_SC1);
      p1 = __builtin_amdgcn_raw_buffer_load_b128(tr, o_acc + 16, 0, AUX_SC1);
      pm = __builtin_amdgcn_raw_buffer_load_b128(tr, o_ml, 0, AUX_SC1);
      const bool ok = p0[1] == tag_hi && p0[3] == tag_hi && p1[1] == tag_hi && p1[3] == tag_hi && pm[1] == tag_hi && pm[3] == tag_hi;
      if (__all(ok) || spins >= cap) break;
      __builtin_amdgcn_s_sleep(2);
    }
    STAMP(8);
    const float Ms = __uint_as_float(pm[0]), Ls = __uint_as_float(pm[2]);
    float Mx = fmaxf(Ms, dpp<XOR1>(Ms)); Mx = fmaxf(Mx, dpp<XOR2>(Mx)); Mx = fmaxf(Mx, dpp<HMIR>(Mx));
    const float wt = __builtin_amdgcn_exp2f(Ms - Mx);
    float v[5] = {wt * __uint_as_float(p0[0]), wt * __uint_as_float(p0[2]), wt * __uint_as_float(p1[0]), wt * __uint_as_float(p1[2]), wt * Ls};
#pragma unroll
    for (int i = 0; i < 5; ++i) { v[i] += dpp<XOR1>(v[i]); v[i] += dpp<XOR2>(v[i]); v[i] += dpp<HMIR>(v[i]); }
    const float inv = 1.f / v[4];
    f32x4 y = {0.f, 0.f, 0.f, 0.f};
#pragma unroll
    for (int i = 0; i < 8; ++i) {
      const float ov = v[i & 3] * inv;
      const unsigned pp = ((i >> 2) != 0) == hi8 ? pk_bf16(ov, ov) : 0u;
      v4u bb; bb[0] = pp & msk[0]; bb[1] = pp & msk[1]; bb[2] = pp & msk[2]; bb[3] = pp & msk[3];
      y = mfma(WO[i], bb, y);
    }
    if (g < 2 || w == sp) {
      const int rowi = g < 2 ? w * 2 + (c >> 3) : 16 + (c >> 3);
#pragma unroll
      for (int r = 0; r < 4; ++r) *(LAS float*)(sm + L_RED + (rowi * 64 + ((4 * g + r) & 7) * 8 + (c & 7)) * 4) = y[r];
    }
  }
  TBAR();
  STAMP(9);
  if (vt < 128) {
    float s;
    if (vt < 64) {
      s = 0.f;
#pragma unroll
      for (int i = 0; i < 16; ++i) s += *(const LAS float*)(sm + L_RED + (i * 64 + vt) * 4);
    } else {
      s = *(const LAS float*)(sm + L_RED + (16 * 64 + vt - 64) * 4) + *(const LAS float*)(sm + L_RED + (17 * 64 + vt - 64) * 4);
    }
    v2u gr; gr[0] = __float_as_uint(s); gr[1] = tag_hi;
    __builtin_amdgcn_raw_buffer_store_b64(gr, tr, vt < 64 ? WS_H2 + bid * 512 + vt * 8 : WS_H2B + ((b * 8 + ((vt - 64) >> 3)) * 128 + (bid & 127)) * 64 + (vt & 7) * 8, 0, AUX_SC1);
  }
  if (is_final) {
    const float gt = *(const LAS float*)(sm + L_FFN + 72 * 4), og = rbf(1.f - gt);
    const int srcA = WS_H2 + ((b * 16 + c) * 8 + fs) * 512 + (4 * w + g) * 16;
    const int srcB = WS_H2B + (b * 8 + fs) * 8192 + w * 1024 + voff;
    v4u pa, pb;
    for (unsigned spins = 0;; ++spins) {
      pa = __builtin_amdgcn_raw_buffer_load_b128(tr, srcA, 0, AUX_SC1);
      pb = __builtin_amdgcn_raw_buffer_load_b128(tr, srcB, 0, AUX_SC1);
      const bool ok = pa[1] == tag_hi && pa[3] == tag_hi && pb[1] == tag_hi && pb[3] == tag_hi;
      if (__all(ok) || spins >= cap) break;
      __builtin_amdgcn_s_sleep(1);
    }
    STAMP(10);
    {
      float b0 = __uint_as_float(pb[0]), b1 = __uint_as_float(pb[2]);
      b0 += dpp<ROR4>(b0); b1 += dpp<ROR4>(b1); b0 += dpp<ROR8>(b0); b1 += dpp<ROR8>(b1);
      b0 = xrow_sum(b0); b1 = xrow_sum(b1);
      if (lane < 4) { f32x2 t; t[0] = b0; t[1] = b1; *(LAS f32x2*)(sm + L_FG + (w * 4 + lane) * 8) = t; }
      float a0 = __uint_as_float(pa[0]), a1 = __uint_as_float(pa[2]);
      a0 += dpp<XOR1>(a0); a1 += dpp<XOR1>(a1); a0 += dpp<XOR2>(a0); a1 += dpp<XOR2>(a1);
      a0 += dpp<ROR4>(a0); a1 += dpp<ROR4>(a1); a0 += dpp<ROR8>(a0); a1 += dpp<ROR8>(a1);
      const int cp = 4 * w + g;
      const f32x2 fv = *(const LAS f32x2*)(sm + L_FFN + cp * 8);
      const float r0 = rbf(gt * rbf(a0)) + rbf(og * rbf(fv[0])), r1 = rbf(gt * rbf(a1)) + rbf(og * rbf(fv[1]));
      if (c == 0) __builtin_amdgcn_raw_buffer_store_b32(pk_bf16(r0, r1), outr, (b * D + fs * 64) * 2 + cp * 4, 0, 0);
    }
    TBAR();
    if (vt < 4) {
      float b0 = 0.f, b1 = 0.f;
#pragma unroll
      for (int i = 0; i < 8; ++i) { const f32x2 t = *(const LAS f32x2*)(sm + L_FG + (i * 4 + vt) * 8); b0 += t[0]; b1 += t[1]; }
      const f32x2 fv = *(const LAS f32x2*)(sm + L_FFN + (32 + vt) * 8);
      const float r0 = rbf(gt * rbf(b0)) + rbf(og * rbf(fv[0])), r1 = rbf(gt * rbf(b1)) + rbf(og * rbf(fv[1]));
      __builtin_amdgcn_raw_buffer_store_b32(pk_bf16(r0, r1), outr, (b * D + 512 + fs * 8) * 2 + vt * 4, 0, 0);
    }
  }
  if (dry == 0) break;
  dry = 0;
  }
#undef NEXT
  STAMP(11);
  STAMP_FLUSH();
}

extern "C" void kernel_launch(void* const* d_in, const int* in_sizes, int n_in, void* d_out, int out_size, void* d_ws, size_t ws_size,
                              hipStream_t stream) {
  k_fused<<<256, NTHR, 0, stream>>>((const unsigned short*)d_in[0], (const unsigned char*)d_in[1], (const unsigned char*)d_in[2],
                                    (const unsigned char*)d_in[3], (const unsigned char*)d_in[4], (const unsigned char*)d_in[6],
                                    (const unsigned char*)d_in[7], (const unsigned char*)d_in[8], (const unsigned short*)d_in[9],
                                    (unsigned char*)d_ws, (unsigned*)d_out);
}
```

```cpp
#include <hip/hip_runtime.h>
#include <hip/hip_bf16.h>
#include <stdint.h>

typedef unsigned v4u __attribute__((ext_vector_type(4)));
typedef unsigned v2u __attribute__((ext_vector_type(2)));
typedef float f32x4 __attribute__((ext_vector_type(4)));
typedef float f32x2 __attribute__((ext_vector_type(2)));
typedef short bf16x8 __attribute__((ext_vector_type(8)));
typedef __bf16 bf16x2 __attribute__((ext_vector_type(2)));
typedef __amdgpu_buffer_rsrc_t rsrc_t;
#define LAS __attribute__((address_space(3)))

constexpr int D = 576, SK = 32768, F = 4608;
constexpr int NW = 8, NTHR = NW * 64;
constexpr unsigned NT = 2048;
#define NSW_V 200
constexpr unsigned NSW = NSW_V;
constexpr unsigned DB = 8 * NSW;
#define NSTR 7
#define HJOIN 1
#define DRYRUN 1
#define H1MODE 1
constexpr unsigned RSV = NSTR;
constexpr unsigned NPRE = 7;
constexpr int AUX_NT = 2, AUX_SC1 = 16;
constexpr int OOB = 0x40000000;
constexpr unsigned SPIN_CAP = 400000u;
#define SLEEP0 4
#define SLEEPW 12

constexpr int WS_E = 0;
constexpr int WS_CNT = 4096;
constexpr int WS_QG = 16384;
constexpr int WS_H1 = WS_QG + 131072;
constexpr int H1_STRIDE = 264 * 8;
constexpr int WS_H2 = WS_H1 + 256 * H1_STRIDE;
constexpr int WS_H2B = WS_H2 + 256 * 512;
constexpr int WS_FF = WS_H2 + 256 * 1024;
constexpr int WS_FP = WS_FF + 144 * 64;
constexpr int WS_END = WS_FP + 144 * 4608;

constexpr int L_XS = 0;
constexpr int L_QP = 2304;
constexpr int L_QS = L_QP + 2048;
constexpr int L_MRG = L_QS + 512;
constexpr int L_RED = L_MRG + 8448;
constexpr int L_GU = L_RED + 4608;
constexpr int L_HS = L_GU + 1536;
constexpr int L_FP = L_HS + 256;
constexpr int L_FG = L_FP + 4608;
constexpr int L_FFN = L_FG + 864;
constexpr int L_TICK = L_FFN + 304;
constexpr int L_END = L_TICK + 16;
constexpr int LDS_BYTES = 96 * 1024;
static_assert(L_END <= LDS_BYTES, "lds");

__device__ __forceinline__ float bflo(unsigned u) { return __builtin_bit_cast(float, u << 16); }
__device__ __forceinline__ float bfhi(unsigned u) { return __builtin_bit_cast(float, u & 0xffff0000u); }
__device__ __forceinline__ unsigned pk_bf16(float lo, float hi) {
  bf16x2 v; v[0] = (__bf16)lo; v[1] = (__bf16)hi; return __builtin_bit_cast(unsigned, v);
}
__device__ __forceinline__ float rbf(float f) { return bflo(pk_bf16(f, f)); }
__device__ __forceinline__ float dot2(unsigned a, unsigned b, float c) {
  return __builtin_amdgcn_fdot2_f32_bf16(__builtin_bit_cast(bf16x2, a), __builtin_bit_cast(bf16x2, b), c, false);
}
template <int CTRL> __device__ __forceinline__ float dpp(float x) {
  return __builtin_bit_cast(float, __builtin_amdgcn_mov_dpp(__builtin_bit_cast(int, x), CTRL, 0xf, 0xf, true));
}
constexpr int XOR1 = 0xB1, XOR2 = 0x4E, HMIR = 0x141, ROR4 = 0x124, ROR8 = 0x128;
__device__ __forceinline__ f32x4 mfma(v4u a, v4u b, f32x4 c) {
  return __builtin_amdgcn_mfma_f32_16x16x32_bf16(__builtin_bit_cast(bf16x8, a), __builtin_bit_cast(bf16x8, b), c, 0, 0, 0);
}
__device__ __forceinline__ float swap16_sum(float a, float b) {
  auto s = __builtin_amdgcn_permlane16_swap(__float_as_uint(a), __float_as_uint(b), false, false);
  return __uint_as_float(s[0]) + __uint_as_float(s[1]);
}
__device__ __forceinline__ float swap32_sum(float a, float b) {
  auto s = __builtin_amdgcn_permlane32_swap(__float_as_uint(a), __float_as_uint(b), false, false);
  return __uint_as_float(s[0]) + __uint_as_float(s[1]);
}
__device__ __forceinline__ float xrow_max(float x) {
  auto s = __builtin_amdgcn_permlane16_swap(__float_as_uint(x), __float_as_uint(x), false, false);
  x = fmaxf(__uint_as_float(s[0]), __uint_as_float(s[1]));
  auto t = __builtin_amdgcn_permlane32_swap(__float_as_uint(x), __float_as_uint(x), false, false);
  return fmaxf(__uint_as_float(t[0]), __uint_as_float(t[1]));
}
__device__ __forceinline__ float xrow_sum(float x) { x = swap16_sum(x, x); return swap32_sum(x, x); }
__device__ __forceinline__ rsrc_t mk_rsrc(const void* p, int bytes) { return __builtin_amdgcn_make_buffer_rsrc((void*)p, 0, bytes, 0x00020000); }
#define BAR() do { asm volatile("s_waitcnt lgkmcnt(0)" ::: "memory"); __builtin_amdgcn_s_barrier(); asm volatile("" ::: "memory"); } while (0)
#define DRAIN() asm volatile("s_waitcnt vmcnt(0)" ::: "memory")

#define STAMP(i) do {} while (0)
#define STAMP_FLUSH() do {} while (0)
constexpr float SCALE = 0.08838834764831845f, L2E = 1.4426950408889634f;

struct St { float m, l; f32x4 acc; };
__device__ __forceinline__ void pin(St& st) { asm volatile("" : "+v"(st.m), "+v"(st.l), "+v"(st.acc) :: "memory"); }

__device__ __forceinline__ void process(const v4u (&KB)[4], const v4u (&VB)[4], const v4u& q0, const v4u& q1, const v4u& msk, bool hi8, St& st) {
  float s[4];
#pragma unroll
  for (int t = 0; t < 4; ++t) {
    float a0 = dot2(KB[t][0], q0[0], 0.f), a1 = dot2(KB[t][0], q1[0], 0.f);
#pragma unroll
    for (int k = 1; k < 4; ++k) { a0 = dot2(KB[t][k], q0[k], a0); a1 = dot2(KB[t][k], q1[k], a1); }
    const float keep = hi8 ? a1 : a0, send = hi8 ? a0 : a1;
    float v = keep + dpp<ROR8>(send);
    v += dpp<HMIR>(v); v += dpp<XOR2>(v); v += dpp<XOR1>(v);
    s[t] = v;
  }
  {
    const unsigned u0 = pk_bf16(s[0], s[1]), u1 = pk_bf16(s[2], s[3]);
    const unsigned w0 = pk_bf16(bflo(u0) * SCALE, bfhi(u0) * SCALE), w1 = pk_bf16(bflo(u1) * SCALE, bfhi(u1) * SCALE);
    s[0] = bflo(w0); s[1] = bfhi(w0); s[2] = bflo(w1); s[3] = bfhi(w1);
  }
  float tm = fmaxf(fmaxf(s[0], s[1]), fmaxf(s[2], s[3]));
  tm = xrow_max(tm);
  const float mn = fmaxf(st.m, __builtin_ceilf(tm * L2E));
  const float alpha = __builtin_amdgcn_exp2f(st.m - mn);
  st.m = mn;
  float p[4];
#pragma unroll
  for (int t = 0; t < 4; ++t) p[t] = __builtin_amdgcn_exp2f(fmaf(s[t], L2E, -mn));
  st.l = fmaf(st.l, alpha, (p[0] + p[1]) + (p[2] + p[3]));
  st.acc *= alpha;
#pragma unroll
  for (int t = 0; t < 4; ++t) {
    const unsigned pp = pk_bf16(p[t], p[t]);
    v4u bb; bb[0] = pp & msk[0]; bb[1] = pp & msk[1]; bb[2] = pp & msk[2]; bb[3] = pp & msk[3];
    st.acc = mfma(VB[t], bb, st.acc);
  }
}
__device__ __forceinline__ void issue(v4u (&KB)[4], v4u (&VB)[4], rsrc_t kr, rsrc_t vr, int voff, unsigned tile) {
  const int so = (int)(tile * 4096u);
#pragma unroll
  for (int t = 0; t < 4; ++t) KB[t] = __builtin_amdgcn_raw_buffer_load_b128(kr, voff + t * 1024, so, AUX_NT);
#pragma unroll
  for (int t = 0; t < 4; ++t) VB[t] = __builtin_amdgcn_raw_buffer_load_b128(vr, voff + t * 1024, so, AUX_NT);
}
__device__ __forceinline__ void atomic_inc_cond(unsigned& a, const void* head, unsigned x, unsigned lim, unsigned vzero, unsigned vone) {
  unsigned long long keep;
  asm volatile("s_cmp_lt_u32 %5, %6\n\ts_cbranch_scc0 1f\n\ts_mov_b64 %1, exec\n\ts_mov_b64 exec, 1\n\tglobal_atomic_add %0, %2, %3, %4 sc0\n\ts_mov_b64 exec, %1\n1:"
               : "+v"(a), "=&s"(keep) : "v"(vzero), "v"(vone), "s"(head), "s"(x), "s"(lim) : "memory", "scc");
}
__device__ __forceinline__ unsigned atomic_inc_sync(const void* head, unsigned vzero, unsigned vone) {
  unsigned long long keep; unsigned r, s;
  asm volatile("s_mov_b64 %1, exec\n\ts_mov_b64 exec, 1\n\tglobal_atomic_add %0, %3, %4, %5 sc0\n\ts_mov_b64 exec, %1\n\ts_waitcnt vmcnt(0)\n\tv_readfirstlane_b32 %2, %0\n\ts_nop 1"
               : "=&v"(r), "=&s"(keep), "=s"(s) : "v"(vzero), "v"(vone), "s"(head) : "memory");
  return s;
}
__device__ __forceinline__ unsigned lds_ticket(unsigned lds_addr_v, unsigned vone) {
  unsigned long long keep; unsigned r, s;
  asm volatile("s_mov_b64 %1, exec\n\ts_mov_b64 exec, 1\n\tds_add_rtn_u32 %0, %3, %4\n\ts_mov_b64 exec, %1\n\ts_waitcnt lgkmcnt(0)\n\tv_readfirstlane_b32 %2, %0\n\ts_nop 1"
               : "=&v"(r), "=&s"(keep), "=s"(s) : "v"(lds_addr_v), "v"(vone) : "memory");
  return s;
}

__device__ __forceinline__ void fma8(float (&acc)[8], float xv, const v4u& w) {
#pragma unroll
  for (int k = 0; k < 4; ++k) { acc[2 * k] = fmaf(xv, bflo(w[k]), acc[2 * k]); acc[2 * k + 1] = fmaf(xv, bfhi(w[k]), acc[2 * k + 1]); }
}
template <bool BIT2> __device__ __forceinline__ f32x2 red8(const float (&acc)[8]) {
  float a[4];
#pragma unroll
  for (int j = 0; j < 4; ++j) a[j] = swap32_sum(acc[j], acc[j + 4]);
  float b0 = swap16_sum(a[0], a[2]), b1 = swap16_sum(a[1], a[3]);
  if (BIT2) { b0 += dpp<ROR4>(b0); b1 += dpp<ROR4>(b1); }
  b0 += dpp<ROR8>(b0); b1 += dpp<ROR8>(b1);
  f32x2 r; r[0] = b0; r[1] = b1; return r;
}

__global__ void __launch_bounds__(NTHR, 2)
k_fused(const unsigned short* __restrict__ x, const unsigned char* Kc, const unsigned char* Vc,
        const unsigned char* __restrict__ wq, const unsigned char* wo, const unsigned char* wg,
        const unsigned char* wu, const unsigned char* __restrict__ wd, const unsigned short* __restrict__ gw,
        unsigned char* ws, unsigned* out) {
  __shared__ __attribute__((aligned(16))) unsigned char smem[LDS_BYTES];
  const int tid = threadIdx.x, lane = tid & 63, w = __builtin_amdgcn_readfirstlane(tid >> 6);
  const int bid = blockIdx.x, grp = bid >> 3, sp = bid & 7, b = grp >> 4, kvh = grp & 15;
  const int g = lane >> 4, c = lane & 15;
  unsigned tsv = 0xffffffffu; (void)tsv;
  const unsigned long long tstart = __builtin_amdgcn_s_memrealtime();
  STAMP(0);
  const rsrc_t wsr = mk_rsrc(ws, WS_END);
  const int voff = lane * 16;
  if (w == 7) {
    const v4u z = {0u, 0u, 0u, 0u};
    __builtin_amdgcn_raw_buffer_store_b128(z, wsr, WS_H1 + bid * H1_STRIDE + voff, 0, AUX_SC1);
    __builtin_amdgcn_raw_buffer_store_b128(z, wsr, WS_H1 + bid * H1_STRIDE + 1024 + voff, 0, AUX_SC1);
    __builtin_amdgcn_raw_buffer_store_b128(z, wsr, lane < 32 ? WS_H2 + bid * 512 + voff : WS_H2B + ((b * 8 + ((lane - 32) >> 2)) * 128 + (bid & 127)) * 64 + (lane & 3) * 16, 0, AUX_SC1);
    int off = OOB;
    if (lane < 4) off = WS_H1 + bid * H1_STRIDE + 2048 + voff;
    else if (lane == 4 && sp == 0) off = WS_CNT + grp * 256;
    __builtin_amdgcn_raw_buffer_store_b128(z, wsr, off, 0, AUX_SC1);
  }
  unsigned short xq[5];
  v4u wqv[5];
  {
    const int r = lane >> 3, row0 = 288 * (sp & 1) + 36 * w;
    const rsrc_t qr = mk_rsrc(wq, D * 8192);
    const int base = (row0 + r) * 8192 + (kvh * 256 + (sp >> 1) * 64) * 2 + (lane & 7) * 16;
#pragma unroll
    for (int i = 0; i < 4; ++i) wqv[i] = __builtin_amdgcn_raw_buffer_load_b128(qr, base, i * 8 * 8192, AUX_NT);
    wqv[4] = __builtin_amdgcn_raw_buffer_load_b128(qr, r < 4 ? base : OOB, 4 * 8 * 8192, AUX_NT);
#pragma unroll
    for (int i = 0; i < 5; ++i) xq[i] = x[b * D + row0 + 8 * i + (i < 4 ? r : (r & 3))];
  }
  const int xi = tid < 288 ? tid : 287;
  const unsigned xa = ((const unsigned*)x)[xi], xb = ((const unsigned*)x)[288 + xi];
  __builtin_amdgcn_sched_barrier(0);
  const rsrc_t kr = mk_rsrc(Kc + (size_t)grp * (SK * 256), SK * 256);
  const rsrc_t vr = mk_rsrc(Vc + (size_t)grp * (SK * 256), SK * 256);
  const unsigned sbase = (unsigned)sp * NSW;
  unsigned long long ev;
  v4u Kb[2][4], Vb[2][4];
  auto qpart = [&]() {
    asm volatile("" : "+v"(wqv[0]), "+v"(wqv[1]), "+v"(wqv[2]), "+v"(wqv[3]), "+v"(wqv[4]));
    float acc[8];
#pragma unroll
    for (int j = 0; j < 8; ++j) acc[j] = 0.f;
#pragma unroll
    for (int i = 0; i < 5; ++i) fma8(acc, bflo(xq[i]), wqv[i]);
    STAMP(12);
    const f32x2 r2 = red8<false>(acc);
    if ((lane & 8) == 0) *(LAS f32x2*)(smem + L_QP + (w * 64 + (lane & 7) * 8 + 4 * (lane >> 5) + 2 * ((lane >> 4) & 1)) * 4) = r2;
    if (tid < 288) {
      v2u xs2; xs2[0] = (xa & 0xffffu) | (xb << 16); xs2[1] = (xa >> 16) | (xb & 0xffff0000u);
      *(LAS v2u*)(smem + L_XS + tid * 8) = xs2;
    }
    if (tid == 0) { v4u t4 = {2 * NPRE, 0u, 0u, 0u}; *(LAS v4u*)(smem + L_TICK) = t4; }
    BAR();
    STAMP(2);
  };
  unsigned long long tag64;
  unsigned tag_hi, tag_lo8;
  auto mktag = [&]() {
    asm volatile("" : "+s"(ev));
    tag64 = ev * 6364136223846793005ull + 1442695040888963407ull;
    tag_hi = (unsigned)(tag64 >> 32) | 1u;
    tag_lo8 = ((unsigned)tag64 >> 24) & 0xffu;
  };

  const int ridx = (grp + 4 * sp) & 31;
  const bool has_task = ridx < 18;
  const int task = sp * 18 + ridx;
  const bool is_final = ridx >= 30;
  const int fs = (kvh >> 2) * 2 + (kvh & 1);

  v4u WO[8];
  const rsrc_t wor = mk_rsrc(wo, 4096 * D * 2);
  const int wo_off = (c < 8 || w == sp) ? (kvh * 256 + 32 * w + g) * 1152 + (c < 8 ? sp * 128 + c * 16 : 1024 + (c - 8) * 16) : OOB;
  v4u msk;
#pragma unroll
  for (int d = 0; d < 4; ++d) msk[d] = (((c & 7) >> 1) == d) ? ((c & 1) ? 0xffff0000u : 0x0000ffffu) : 0u;
  const bool hi8 = (c & 8) != 0;
  St st; st.m = -INFINITY; st.l = 0.f; st.acc = f32x4{0.f, 0.f, 0.f, 0.f};
  const unsigned char* head = ws + WS_CNT + grp * 256;
  unsigned vzero = 0u, vone = 1u, a = 0u;
  unsigned taddr = (unsigned)(size_t)(LAS unsigned char*)(smem + L_TICK);
  asm volatile("" : "+v"(vzero), "+v"(vone), "+v"(taddr), "+v"(a));
  unsigned lastt = 0;
  v4u q0, q1;

#define NEXT(nx)                                                                                                   \
  {                                                                                                                \
    unsigned cmpv;                                                                                                 \
    if (lastt + RSV < NSW) {                                                                                       \
      const unsigned t = lds_ticket(taddr, vone);                                                                  \
      lastt = t;                                                                                                   \
      if (t < NSW) { nx = sbase + t; cmpv = (t + RSV >= NSW) ? 0u : NT; }                                          \
      else { nx = atomic_inc_sync(head, vzero, vone) + DB; if (nx > NT) nx = NT; cmpv = nx; }                      \
    } else {                                                                                                       \
      nx = __builtin_amdgcn_readfirstlane(a) + DB; if (nx > NT) nx = NT;                                           \
      cmpv = nx;                                                                                                   \
    }                                                                                                              \
    atomic_inc_cond(a, head, cmpv, NT, vzero, vone);                                                               \
  }

  unsigned first = 0;
  asm volatile("" ::: "memory");
  __builtin_amdgcn_s_barrier();
  asm volatile("s_load_dwordx2 %0, %1, 0x0" : "=s"(ev) : "s"(ws) : "memory");
  STAMP(1);
  if (w == 7) {
    qpart();
    mktag();
    {
      float s = 0.f;
#pragma unroll
      for (int ww = 0; ww < NW; ++ww) s += *(const LAS float*)(smem + L_QP + (ww * 64 + lane) * 4);
      DRAIN();
      v2u gr; gr[0] = ((__float_as_uint(s) + 0x80u) & 0xffffff00u) | tag_lo8; gr[1] = tag_hi;
      __builtin_amdgcn_raw_buffer_store_b64(gr, wsr, WS_QG + ((grp * 2 + (sp & 1)) * 256 + (sp >> 1) * 64 + lane) * 8, 0, AUX_SC1);
    }
    STAMP(13);
    for (unsigned spins = 0;; ++spins) {
      v4u p[4];
#pragma unroll
      for (int i = 0; i < 4; ++i) p[i] = __builtin_amdgcn_raw_buffer_load_b128(wsr, WS_QG + grp * 4096 + lane * 32 + (i & 1) * 16 + (i >> 1) * 2048, 0, AUX_SC1);
      bool ok = true;
#pragma unroll
      for (int i = 0; i < 4; ++i) ok = ok && p[i][1] == tag_hi && p[i][3] == tag_hi && (p[i][0] & 0xffu) == tag_lo8 && (p[i][2] & 0xffu) == tag_lo8;
      if (__all(ok) || spins > SPIN_CAP) {
        float qv[4];
#pragma unroll
        for (int i = 0; i < 4; ++i)
          qv[i] = __uint_as_float(p[i >> 1][(i & 1) * 2] & 0xffffff00u) + __uint_as_float(p[2 + (i >> 1)][(i & 1) * 2] & 0xffffff00u);
        v2u qq; qq[0] = pk_bf16(qv[0], qv[1]); qq[1] = pk_bf16(qv[2], qv[3]);
        *(LAS v2u*)(smem + L_QS + lane * 8) = qq;
        break;
      }
      __builtin_amdgcn_s_sleep(2);
    }
    asm volatile("s_waitcnt lgkmcnt(0)" ::: "memory");
    if (lane == 0) *(volatile LAS unsigned*)(smem + L_TICK + 8) = 1u;
    STAMP(3);
#pragma unroll
    for (int i = 0; i < 8; ++i) WO[i] = __builtin_amdgcn_raw_buffer_load_b128(wor, wo_off, i * 4608, AUX_NT);
    q0 = *(const LAS v4u*)(smem + L_QS + c * 16); q1 = *(const LAS v4u*)(smem + L_QS + 256 + c * 16);
    if (has_task) {
      const rsrc_t dr = mk_rsrc(wd, F * D * 2);
      const int dbase = (task * 32 + g) * 1152 + c * 16;
      f32x4 da[5];
#pragma unroll
      for (int cb = 0; cb < 5; ++cb) da[cb] = f32x4{0.f, 0.f, 0.f, 0.f};
#pragma unroll
      for (int hf = 0; hf < 2; ++hf) {
        v4u wv[20];
#pragma unroll
        for (int i = 0; i < 4; ++i)
#pragma unroll
          for (int cb = 0; cb < 5; ++cb)
            wv[i * 5 + cb] = __builtin_amdgcn_raw_buffer_load_b128(dr, (cb == 4 && c >= 8) ? OOB : dbase + cb * 256, (hf * 4 + i) * 4608, AUX_NT);
        __builtin_amdgcn_sched_barrier(0);
        if (hf == 0) {
          for (unsigned spins = 0; spins < 64u * SPIN_CAP; ++spins) {
            if (*(volatile LAS unsigned*)(smem + L_TICK + 4) >= 6u) break;
            __builtin_amdgcn_s_sleep(4);
          }
          asm volatile("" ::: "memory");
          const LAS float* gu = (const LAS float*)(smem + L_GU) + lane;
          const float gv = rbf(gu[0] + gu[64] + gu[128]), uv = rbf(gu[192] + gu[256] + gu[320]);
          const float inner = 0.7978845608028654f * (gv + 0.044715f * gv * gv * gv);
          const float ge = rbf(0.5f * gv * (1.f + tanhf(inner)));
          *(LAS float*)(smem + L_HS + lane * 4) = rbf(ge * uv);
          asm volatile("s_waitcnt lgkmcnt(0)" ::: "memory");
        }
#pragma unroll
        for (int i = 0; i < 4; ++i) {
          const float hv = *(const LAS float*)(smem + L_HS + ((c >> 3) * 32 + (hf * 4 + i) * 4 + g) * 4);
          const unsigned pp = pk_bf16(hv, hv);
          v4u bb; bb[0] = pp & msk[0]; bb[1] = pp & msk[1]; bb[2] = pp & msk[2]; bb[3] = pp & msk[3];
#pragma unroll
          for (int cb = 0; cb < 5; ++cb) da[cb] = mfma(wv[i * 5 + cb], bb, da[cb]);
        }
        __builtin_amdgcn_sched_barrier(0);
      }
#pragma unroll
      for (int cb = 0; cb < 5; ++cb)
#pragma unroll
        for (int rr = 0; rr < 4; ++rr) {
          const int col = cb * 128 + (4 * g + rr) * 8 + (c & 7);
          if (col < D) *(LAS float*)(smem + L_FP + ((c >> 3) * D + col) * 4) = da[cb][rr];
        }
      asm volatile("s_waitcnt lgkmcnt(0)" ::: "memory");
#pragma unroll
      for (int i = 0; i < 5; ++i) {
        const int o = (i * 64 + lane) * 16;
        if (o < 4608) {
          const v4u v = *(const LAS v4u*)(smem + L_FP + o);
          __builtin_amdgcn_raw_buffer_store_b128(v, wsr, WS_FP + task * 4608 + o, 0, AUX_SC1);
        }
      }
      DRAIN();
      if (lane == 0) {
        v2u fl; fl[0] = (unsigned)tag64; fl[1] = tag_hi;
        __builtin_amdgcn_raw_buffer_store_b64(fl, wsr, WS_FF + task * 64, 0, AUX_SC1);
      }
    }
    STAMP(4);
    if (is_final) {
      for (unsigned spins = 0;; ++spins) {
        bool ok = true;
#pragma unroll
        for (int i = 0; i < 4; ++i) {
          const v2u p = __builtin_amdgcn_raw_buffer_load_b64(wsr, WS_QG + (i * 64 + lane) * 512, 0, AUX_SC1);
          ok = ok && p[1] == tag_hi && (p[0] & 0xffu) == tag_lo8;
        }
        if (__all(ok) || spins > SPIN_CAP) break;
        __builtin_amdgcn_s_sleep(16);
      }
      if (grp == 2 && lane == 0) {
        v2u e; e[0] = (unsigned)tag64; e[1] = (unsigned)(tag64 >> 32);
        __builtin_amdgcn_raw_buffer_store_b64(e, wsr, WS_E, 0, AUX_SC1);
      }
      {
        float z = 0.f;
#pragma unroll
        for (int i = 0; i < 9; ++i) {
          const unsigned xv = *(const LAS unsigned*)(smem + L_XS + (i * 64 + lane) * 4);
          z += rbf((b ? bfhi(xv) : bflo(xv)) * bflo(gw[i * 64 + lane]));
        }
        z += dpp<XOR1>(z); z += dpp<XOR2>(z); z += dpp<ROR4>(z); z += dpp<ROR8>(z);
        z = rbf(xrow_sum(z));
        if (lane == 0) *(LAS float*)(smem + L_FFN + 72 * 4) = rbf(1.f / (1.f + __expf(-z)));
      }
      for (unsigned spins = 0;; ++spins) {
        bool ok = true;
#pragma unroll
        for (int i = 0; i < 3; ++i) {
          const int tk = i * 64 + lane;
          const v2u p = __builtin_amdgcn_raw_buffer_load_b64(wsr, WS_FF + (tk < 144 ? tk : 0) * 64, 0, AUX_SC1);
          ok = ok && p[1] == tag_hi && p[0] == (unsigned)tag64;
        }
        if (__all(ok) || spins > SPIN_CAP) break;
        __builtin_amdgcn_s_sleep(64);
      }
      __builtin_amdgcn_fence(__ATOMIC_ACQUIRE, "wavefront");
      {
        const int ts = lane / 18, q4 = lane - ts * 18;
        const int fcol = q4 < 16 ? fs * 64 + q4 * 4 : 512 + fs * 8 + (q4 - 16) * 4;
        const int fo = ts < 3 ? WS_FP + ts * 4608 + (b * D + fcol) * 4 : OOB;
        f32x4 fa = {0.f, 0.f, 0.f, 0.f};
#pragma unroll 1
        for (int bt = 0; bt < 2; ++bt) {
          v4u pv[24];
#pragma unroll
          for (int i = 0; i < 24; ++i) pv[i] = __builtin_amdgcn_raw_buffer_load_b128(wsr, fo, (bt * 24 + i) * 3 * 4608, AUX_SC1);
          __builtin_amdgcn_sched_barrier(0);
#pragma unroll
          for (int i = 0; i < 24; ++i) fa += __builtin_bit_cast(f32x4, pv[i]);
        }
        if (ts < 3) *(LAS f32x4*)(smem + L_FG + (ts * 72 + q4 * 4) * 4) = fa;
        asm volatile("s_waitcnt lgkmcnt(0)" ::: "memory");
#pragma unroll
        for (int i = 0; i < 2; ++i) {
          const int col = i * 64 + lane;
          if (col < 72)
            *(LAS float*)(smem + L_FFN + col * 4) = *(const LAS float*)(smem + L_FG + col * 4) + *(const LAS float*)(smem + L_FG + (72 + col) * 4) +
                                                    *(const LAS float*)(smem + L_FG + (144 + col) * 4);
        }
      }
    }
    STAMP(5);
    DRAIN();
    if (HJOIN) { NEXT(first); } else first = NT;
    issue(Kb[1], Vb[1], kr, vr, voff, first);
  } else {
    unsigned qrdy = 0;
#define PACE()                                                                                              \
    {                                                                                                       \
      __builtin_amdgcn_sched_barrier(0);                                                                    \
      if (qrdy == 0) {                                                                                      \
        DRAIN();                                                                                            \
        qrdy = __builtin_amdgcn_readfirstlane(*(volatile LAS unsigned*)(smem + L_TICK + 8));                \
      }                                                                                                     \
      __builtin_amdgcn_sched_barrier(0);                                                                    \
    }
    const int so0 = (int)((sbase + w) * 4096u), so1 = (int)((sbase + NPRE + w) * 4096u);
#pragma unroll
    for (int t = 0; t < 4; ++t) Kb[0][t] = __builtin_amdgcn_raw_buffer_load_b128(kr, voff + t * 1024, so0, AUX_NT);
    __builtin_amdgcn_sched_barrier(0);
    qpart();
    mktag();
    PACE();
#pragma unroll
    for (int t = 0; t < 4; ++t) Vb[0][t] = __builtin_amdgcn_raw_buffer_load_b128(vr, voff + t * 1024, so0, AUX_NT);
    PACE();
#pragma unroll
    for (int t = 0; t < 4; ++t) Kb[1][t] = __builtin_amdgcn_raw_buffer_load_b128(kr, voff + t * 1024, so1, AUX_NT);
    PACE();
#pragma unroll
    for (int t = 0; t < 4; ++t) Vb[1][t] = __builtin_amdgcn_raw_buffer_load_b128(vr, voff + t * 1024, so1, AUX_NT);
    PACE();
#pragma unroll
    for (int i = 0; i < 8; ++i) WO[i] = __builtin_amdgcn_raw_buffer_load_b128(wor, wo_off, i * 4608, AUX_NT);
    const bool gu_wave = has_task && w < 6;
    const int r = lane >> 2, mat = w >= 3 ? 1 : 0, third = w - 3 * mat;
    v4u wv[12];
    if (gu_wave) {
      const rsrc_t gr = mk_rsrc(mat ? wu : wg, D * F * 2);
      const int gbase = (third * 192 + r) * (F * 2) + task * 64 + (lane & 3) * 16;
#pragma unroll
      for (int pc = 0; pc < 3; ++pc) {
        PACE();
#pragma unroll
        for (int i = 4 * pc; i < 4 * pc + 4; ++i) wv[i] = __builtin_amdgcn_raw_buffer_load_b128(gr, gbase, i * 16 * (F * 2), AUX_NT);
      }
    }
    __builtin_amdgcn_sched_barrier(0);
    for (unsigned spins = 0; qrdy == 0 && spins < 64u * SPIN_CAP; ++spins) {
      qrdy = __builtin_amdgcn_readfirstlane(*(volatile LAS unsigned*)(smem + L_TICK + 8));
      if (qrdy == 0) __builtin_amdgcn_s_sleep(1);
    }
    asm volatile("" ::: "memory");
#undef PACE
    STAMP(3);
    q0 = *(const LAS v4u*)(smem + L_QS + c * 16); q1 = *(const LAS v4u*)(smem + L_QS + 256 + c * 16);
    process(Kb[0], Vb[0], q0, q1, msk, hi8, st); pin(st);
    if (gu_wave) {
      float a0[8], a1[8];
#pragma unroll
      for (int j = 0; j < 8; ++j) { a0[j] = 0.f; a1[j] = 0.f; }
#pragma unroll
      for (int i = 0; i < 12; ++i) {
        const unsigned xv = *(const LAS unsigned*)(smem + L_XS + (third * 192 + i * 16 + r) * 4);
        fma8(a0, bflo(xv), wv[i]); fma8(a1, bfhi(xv), wv[i]);
      }
      const f32x2 r0 = red8<true>(a0), r1 = red8<true>(a1);
      if ((lane & 12) == 0) {
        const int col = (lane & 3) * 8 + 4 * (lane >> 5) + 2 * ((lane >> 4) & 1);
        *(LAS f32x2*)(smem + L_GU + ((w * 2 + 0) * 32 + col) * 4) = r0;
        *(LAS f32x2*)(smem + L_GU + ((w * 2 + 1) * 32 + col) * 4) = r1;
      }
      asm volatile("s_waitcnt lgkmcnt(0)" ::: "memory");
      (void)lds_ticket(taddr + 4, vone);
    }
    if (NSTR < 7 && w >= NSTR) {
      process(Kb[1], Vb[1], q0, q1, msk, hi8, st); pin(st);
      first = NT;
    }
  }
  __builtin_amdgcn_sched_barrier(0);
  unsigned dry = DRYRUN ? (unsigned)(w + 1) >> 3 : 0u;
#pragma clang loop unroll(disable)
  for (;;) {
  asm volatile("" : "+s"(dry));
  if (dry == 0 && first < NT) {
    while (true) {
      unsigned nx;
      asm volatile("s_waitcnt vmcnt(0)" : "+v"(a) :: "memory");
      NEXT(nx);
      issue(Kb[0], Vb[0], kr, vr, voff, nx);
      __builtin_amdgcn_sched_barrier(0);
      process(Kb[1], Vb[1], q0, q1, msk, hi8, st); pin(st);
      __builtin_amdgcn_sched_barrier(0);
      if (nx >= NT) break;
      asm volatile("s_waitcnt vmcnt(0)" : "+v"(a) :: "memory");
      NEXT(nx);
      issue(Kb[1], Vb[1], kr, vr, voff, nx);
      __builtin_amdgcn_sched_barrier(0);
      process(Kb[0], Vb[0], q0, q1, msk, hi8, st); pin(st);
      __builtin_amdgcn_sched_barrier(0);
      if (nx >= NT) break;
    }
  }
  const rsrc_t tr = mk_rsrc(ws, dry ? 0 : WS_END);
  const rsrc_t outr = mk_rsrc(out, dry ? 0 : 2 * D * 2);
  LAS unsigned char* sm = (LAS unsigned char*)smem + (dry ? 49152 : 0);
  const int vt = dry ? lane : tid;
  const unsigned cap = dry ? 0u : SPIN_CAP;
#define TBAR() do { asm volatile("s_waitcnt lgkmcnt(0)" ::: "memory"); if (dry == 0) __builtin_amdgcn_s_barrier(); asm volatile("" ::: "memory"); } while (0)
  STAMP(6);
  {
    const float l = xrow_sum(st.l);
    const int h = c >> 3;
    LAS float* mrg = (LAS float*)(sm + L_MRG) + (w * 2 + h) * 132;
#pragma unroll
    for (int r = 0; r < 4; ++r) mrg[4 + 8 * (4 * g + r) + (c & 7)] = st.acc[r];
    if (g == 0 && (c & 7) == 0) { mrg[0] = st.m; mrg[1] = l; }
  }
  TBAR();
  STAMP(7);
  if (vt < 256) {
    const int hh = vt >> 7, pos = vt & 127;
    const int d = (pos & 96) + 4 * (pos & 7) + ((pos >> 3) & 3);
    const LAS float* mrg = (const LAS float*)(sm + L_MRG) + hh * 132;
    float M = -INFINITY;
#pragma unroll
    for (int ww = 0; ww < NW; ++ww) M = fmaxf(M, mrg[ww * 264]);
    float L = 0.f, o = 0.f;
#pragma unroll
    for (int ww = 0; ww < NW; ++ww) {
      const float wt = __builtin_amdgcn_exp2f(mrg[ww * 264] - M);
      L = fmaf(wt, mrg[ww * 264 + 1], L); o = fmaf(wt, mrg[ww * 264 + 4 + d], o);
    }
    v2u gr; gr[0] = __float_as_uint(o); gr[1] = tag_hi;
    __builtin_amdgcn_raw_buffer_store_b64(gr, tr, WS_H1 + bid * H1_STRIDE + vt * 8, 0, AUX_SC1);
    if (pos < 2) {
      gr[0] = __float_as_uint(pos ? L : M);
      __builtin_amdgcn_raw_buffer_store_b64(gr, tr, WS_H1 + bid * H1_STRIDE + (256 + 2 * hh + pos) * 8, 0, AUX_SC1);
    }
  }
  STAMP(14);
  {
    const int src = WS_H1 + (grp * 8 + (c & 7)) * H1_STRIDE;
    const int o_acc = src + (32 * w + 8 * g + 4 * (c >> 3)) * 8, o_ml = src + (256 + 2 * (w >> 2)) * 8;
    v4u p0, p1, pm;
    if (H1MODE == 1) {
      if (w == 7) {
        const int o_w = WS_H1 + (grp * 8 + (lane & 7)) * H1_STRIDE + (256 + 2 * ((lane >> 3) & 1)) * 8;
        for (unsigned spins = 0;; ++spins) {
          const v4u t = __builtin_amdgcn_raw_buffer_load_b128(tr, o_w, 0, AUX_SC1);
          if (__all(t[1] == tag_hi && t[3] == tag_hi) || spins >= cap) break;
          __builtin_amdgcn_s_sleep(1);
        }
      }
      TBAR();
      STAMP(15);
    } else if (H1MODE == 2) {
      for (unsigned spins = 0;; ++spins) {
        const v4u t = __builtin_amdgcn_raw_buffer_load_b128(tr, o_ml, 0, AUX_SC1);
        if (__all(t[1] == tag_hi && t[3] == tag_hi) || spins >= cap) break;
        __builtin_amdgcn_s_sleep(2);
      }
    }
    for (unsigned spins = 0;; ++spins) {
      p0 = __builtin_amdgcn_raw_buffer_load_b128(tr, o_acc, 0, AUX_SC1);
      p1 = __builtin_amdgcn_raw_buffer_load_b128(tr, o_acc + 16, 0, AUX_SC1);
      pm = __builtin_amdgcn_raw_buffer_load_b128(tr, o_ml, 0, AUX_SC1);
      const bool ok = p0[1] == tag_hi && p0[3] == tag_hi && p1[1] == tag_hi && p1[3] == tag_hi && pm[1] == tag_hi && pm[3] == tag_hi;
      if (__all(ok) || spins >= cap) break;
      __builtin_amdgcn_s_sleep(2);
    }
    STAMP(8);
    const float Ms = __uint_as_float(pm[0]), Ls = __uint_as_float(pm[2]);
    float Mx = fmaxf(Ms, dpp<XOR1>(Ms)); Mx = fmaxf(Mx, dpp<XOR2>(Mx)); Mx = fmaxf(Mx, dpp<HMIR>(Mx));
    const float wt = __builtin_amdgcn_exp2f(Ms - Mx);
    float v[5] = {wt * __uint_as_float(p0[0]), wt * __uint_as_float(p0[2]), wt * __uint_as_float(p1[0]), wt * __uint_as_float(p1[2]), wt * Ls};
#pragma unroll
    for (int i = 0; i < 5; ++i) { v[i] += dpp<XOR1>(v[i]); v[i] += dpp<XOR2>(v[i]); v[i] += dpp<HMIR>(v[i]); }
    const float inv = 1.f / v[4];
    f32x4 y = {0.f, 0.f, 0.f, 0.f};
#pragma unroll
    for (int i = 0; i < 8; ++i) {
      const float ov = v[i & 3] * inv;
      const unsigned pp = ((i >> 2) != 0) == hi8 ? pk_bf16(ov, ov) : 0u;
      v4u bb; bb[0] = pp & msk[0]; bb[1] = pp & msk[1]; bb[2] = pp & msk[2]; bb[3] = pp & msk[3];
      y = mfma(WO[i], bb, y);
    }
    if (g < 2 || w == sp) {
      const int rowi = g < 2 ? w * 2 + (c >> 3) : 16 + (c >> 3);
#pragma unroll
      for (int r = 0; r < 4; ++r) *(LAS float*)(sm + L_RED + (rowi * 64 + ((4 * g + r) & 7) * 8 + (c & 7)) * 4) = y[r];
    }
  }
  TBAR();
  STAMP(9);
  if (vt < 128) {
    float s;
    if (vt < 64) {
      s = 0.f;
#pragma unroll
      for (int i = 0; i < 16; ++i) s += *(const LAS float*)(sm + L_RED + (i * 64 + vt) * 4);
    } else {
      s = *(const LAS float*)(sm + L_RED + (16 * 64 + vt - 64) * 4) + *(const LAS float*)(sm + L_RED + (17 * 64 + vt - 64) * 4);
    }
    v2u gr; gr[0] = __float_as_uint(s); gr[1] = tag_hi;
    __builtin_amdgcn_raw_buffer_store_b64(gr, tr, vt < 64 ? WS_H2 + bid * 512 + vt * 8 : WS_H2B + ((b * 8 + ((vt - 64) >> 3)) * 128 + (bid & 127)) * 64 + (vt & 7) * 8, 0, AUX_SC1);
  }
  if (is_final) {
    const float gt = *(const LAS float*)(sm + L_FFN + 72 * 4), og = rbf(1.f - gt);
    const int srcA = WS_H2 + ((b * 16 + c) * 8 + fs) * 512 + (4 * w + g) * 16;
    const int srcB = WS_H2B + (b * 8 + fs) * 8192 + w * 1024 + voff;
    v4u pa, pb;
    for (unsigned spins = 0;; ++spins) {
      pa = __builtin_amdgcn_raw_buffer_load_b128(tr, srcA, 0, AUX_SC1);
      pb = __builtin_amdgcn_raw_buffer_load_b128(tr, srcB, 0, AUX_SC1);
      const bool ok = pa[1] == tag_hi && pa[3] == tag_hi && pb[1] == tag_hi && pb[3] == tag_hi;
      if (__all(ok) || spins >= cap) break;
      __builtin_amdgcn_s_sleep(1);
    }
    STAMP(10);
    {
      float b0 = __uint_as_float(pb[0]), b1 = __uint_as_float(pb[2]);
      b0 += dpp<ROR4>(b0); b1 += dpp<ROR4>(b1); b0 += dpp<ROR8>(b0); b1 += dpp<ROR8>(b1);
      b0 = xrow_sum(b0); b1 = xrow_sum(b1);
      if (lane < 4) { f32x2 t; t[0] = b0; t[1] = b1; *(LAS f32x2*)(sm + L_FG + (w * 4 + lane) * 8) = t; }
      float a0 = __uint_as_float(pa[0]), a1 = __uint_as_float(pa[2]);
      a0 += dpp<XOR1>(a0); a1 += dpp<XOR1>(a1); a0 += dpp<XOR2>(a0); a1 += dpp<XOR2>(a1);
      a0 += dpp<ROR4>(a0); a1 += dpp<ROR4>(a1); a0 += dpp<ROR8>(a0); a1 += dpp<ROR8>(a1);
      const int cp = 4 * w + g;
      const f32x2 fv = *(const LAS f32x2*)(sm + L_FFN + cp * 8);
      const float r0 = rbf(gt * rbf(a0)) + rbf(og * rbf(fv[0])), r1 = rbf(gt * rbf(a1)) + rbf(og * rbf(fv[1]));
      if (c == 0) __builtin_amdgcn_raw_buffer_store_b32(pk_bf16(r0, r1), outr, (b * D + fs * 64) * 2 + cp * 4, 0, 0);
    }
    TBAR();
    if (vt < 4) {
      float b0 = 0.f, b1 = 0.f;
#pragma unroll
      for (int i = 0; i < 8; ++i) { const f32x2 t = *(const LAS f32x2*)(sm + L_FG + (i * 4 + vt) * 8); b0 += t[0]; b1 += t[1]; }
      const f32x2 fv = *(const LAS f32x2*)(sm + L_FFN + (32 + vt) * 8);
      const float r0 = rbf(gt * rbf(b0)) + rbf(og * rbf(fv[0])), r1 = rbf(gt * rbf(b1)) + rbf(og * rbf(fv[1]));
      __builtin_amdgcn_raw_buffer_store_b32(pk_bf16(r0, r1), outr, (b * D + 512 + fs * 8) * 2 + vt * 4, 0, 0);
    }
  }
  if (dry == 0) break;
  dry = 0;
  }
#undef NEXT
  if (bid == 0 && w == 0) while (__builtin_amdgcn_s_memrealtime() - tstart < 15000ull) __builtin_amdgcn_s_sleep(1);
  STAMP(11);
  STAMP_FLUSH();
}

extern "C" void kernel_launch(void* const* d_in, const int* in_sizes, int n_in, void* d_out, int out_size, void* d_ws, size_t ws_size,
                              hipStream_t stream) {
  k_fused<<<256, NTHR, 0, stream>>>((const unsigned short*)d_in[0], (const unsigned char*)d_in[1], (const unsigned char*)d_in[2],
                                    (const unsigned char*)d_in[3], (const unsigned char*)d_in[4], (const unsigned char*)d_in[6],
                                    (const unsigned char*)d_in[7], (const unsigned char*)d_in[8], (const unsigned short*)d_in[9],
                                    (unsigned char*)d_ws, (unsigned*)d_out);
}
```

```cpp
#include <hip/hip_runtime.h>
#include <hip/hip_bf16.h>
#include <stdint.h>

typedef unsigned v4u __attribute__((ext_vector_type(4)));
typedef unsigned v2u __attribute__((ext_vector_type(2)));
typedef float f32x4 __attribute__((ext_vector_type(4)));
typedef float f32x2 __attribute__((ext_vector_type(2)));
typedef short bf16x8 __attribute__((ext_vector_type(8)));
typedef __bf16 bf16x2 __attribute__((ext_vector_type(2)));
typedef __amdgpu_buffer_rsrc_t rsrc_t;
#define LAS __attribute__((address_space(3)))

constexpr int D = 576, SK = 32768, F = 4608;
constexpr int NW = 8, NTHR = NW * 64;
#define NSW_V 224
constexpr unsigned NSW = NSW_V;
constexpr unsigned ND = 8 * (256 - NSW);
constexpr unsigned NONE = 2048;
constexpr int AUX_NT = 2, AUX_SC1 = 16;
constexpr int OOB = 0x40000000;
constexpr unsigned SPIN_CAP = 400000u;
#define H1P 1
#define NE 2
#define STAG 4
#define H1MASK 1
#define NEARD 64
#define PSLEEP 2

constexpr int WS_E = 0;
constexpr int WS_CNT = 4096;
constexpr int WS_QG = 16384;
constexpr int WS_H1 = WS_QG + 131072;
constexpr int H1_STRIDE = 2176;
constexpr int WS_H2 = WS_H1 + 256 * H1_STRIDE;
constexpr int WS_FF = WS_H2 + 144 * 4096;
constexpr int WS_FP = WS_FF + 144 * 64;
constexpr int WS_END = WS_FP + 144 * 4608;

constexpr int L_XS = 0;
constexpr int L_QP = 2304;
constexpr int L_QS = L_QP + 2048;
constexpr int L_MRG = L_QS + 512;
constexpr int L_ML = L_MRG + 8192;
constexpr int L_OS = L_ML + 96;
constexpr int L_GU = L_OS + 256;
constexpr int L_HS = L_GU + 1536;
constexpr int L_FP = L_HS + 256;
constexpr int L_FFN = L_FP + 4608;
constexpr int L_FB = L_FFN + 80;
constexpr int L_SYNC = L_FB + 256;
constexpr int L_END = L_SYNC + 48;
constexpr int LDS_BYTES = 96 * 1024;
static_assert(L_END <= LDS_BYTES, "lds");

__device__ __forceinline__ float bflo(unsigned u) { return __builtin_bit_cast(float, u << 16); }
__device__ __forceinline__ float bfhi(unsigned u) { return __builtin_bit_cast(float, u & 0xffff0000u); }
__device__ __forceinline__ unsigned pk_bf16(float lo, float hi) {
  bf16x2 v; v[0] = (__bf16)lo; v[1] = (__bf16)hi; return __builtin_bit_cast(unsigned, v);
}
__device__ __forceinline__ float rbf(float f) { return bflo(pk_bf16(f, f)); }
__device__ __forceinline__ float dot2(unsigned a, unsigned b, float c) {
  return __builtin_amdgcn_fdot2_f32_bf16(__builtin_bit_cast(bf16x2, a), __builtin_bit_cast(bf16x2, b), c, false);
}
template <int CTRL> __device__ __forceinline__ float dpp(float x) {
  return __builtin_bit_cast(float, __builtin_amdgcn_mov_dpp(__builtin_bit_cast(int, x), CTRL, 0xf, 0xf, true));
}
constexpr int XOR1 = 0xB1, XOR2 = 0x4E, HMIR = 0x141, ROR4 = 0x124, ROR8 = 0x128;
__device__ __forceinline__ f32x4 mfma(v4u a, v4u b, f32x4 c) {
  return __builtin_amdgcn_mfma_f32_16x16x32_bf16(__builtin_bit_cast(bf16x8, a), __builtin_bit_cast(bf16x8, b), c, 0, 0, 0);
}
__device__ __forceinline__ float swap16_sum(float a, float b) {
  auto s = __builtin_amdgcn_permlane16_swap(__float_as_uint(a), __float_as_uint(b), false, false);
  return __uint_as_float(s[0]) + __uint_as_float(s[1]);
}
__device__ __forceinline__ float swap32_sum(float a, float b) {
  auto s = __builtin_amdgcn_permlane32_swap(__float_as_uint(a), __float_as_uint(b), false, false);
  return __uint_as_float(s[0]) + __uint_as_float(s[1]);
}
__device__ __forceinline__ float xrow_max(float x) {
  auto s = __builtin_amdgcn_permlane16_swap(__float_as_uint(x), __float_as_uint(x), false, false);
  x = fmaxf(__uint_as_float(s[0]), __uint_as_float(s[1]));
  auto t = __builtin_amdgcn_permlane32_swap(__float_as_uint(x), __float_as_uint(x), false, false);
  return fmaxf(__uint_as_float(t[0]), __uint_as_float(t[1]));
}
__device__ __forceinline__ float xrow_sum(float x) { x = swap16_sum(x, x); return swap32_sum(x, x); }
__device__ __forceinline__ rsrc_t mk_rsrc(const void* p, int bytes) { return __builtin_amdgcn_make_buffer_rsrc((void*)p, 0, bytes, 0x00020000); }
#define BAR() do { asm volatile("s_waitcnt lgkmcnt(0)" ::: "memory"); __builtin_amdgcn_s_barrier(); asm volatile("" ::: "memory"); } while (0)
#define DRAIN() do { __builtin_amdgcn_s_waitcnt(0x0F70); asm volatile("s_waitcnt vmcnt(0)" ::: "memory"); } while (0)
#define LDSW() asm volatile("s_waitcnt lgkmcnt(0)" ::: "memory")

#define STAMP(i) do {} while (0)
#define STAMP_FLUSH() do {} while (0)
constexpr float SCALE = 0.08838834764831845f, L2E = 1.4426950408889634f;

typedef double f64x4 __attribute__((ext_vector_type(4)));
struct St { float m; double l; f64x4 acc; };
__device__ __forceinline__ void pin(St& st) { asm volatile("" : "+v"(st.m), "+v"(st.l), "+v"(st.acc) :: "memory"); }
__device__ __forceinline__ v4u diag(float v, const v4u& msk) {
  const unsigned pp = pk_bf16(v, v);
  v4u bb; bb[0] = pp & msk[0]; bb[1] = pp & msk[1]; bb[2] = pp & msk[2]; bb[3] = pp & msk[3];
  return bb;
}

__device__ __forceinline__ void process(const v4u (&KB)[4], const v4u (&VB)[4], const v4u& q0, const v4u& q1, const v4u& msk, bool hi8, St& st) {
  float s[4];
#pragma unroll
  for (int t = 0; t < 4; ++t) {
    float a0 = dot2(KB[t][0], q0[0], 0.f), a1 = dot2(KB[t][0], q1[0], 0.f);
#pragma unroll
    for (int k = 1; k < 4; ++k) { a0 = dot2(KB[t][k], q0[k], a0); a1 = dot2(KB[t][k], q1[k], a1); }
    const float keep = hi8 ? a1 : a0, send = hi8 ? a0 : a1;
    float v = keep + dpp<ROR8>(send);
    v += dpp<HMIR>(v); v += dpp<XOR2>(v); v += dpp<XOR1>(v);
    s[t] = v;
  }
  {
    const unsigned u0 = pk_bf16(s[0], s[1]), u1 = pk_bf16(s[2], s[3]);
    const unsigned w0 = pk_bf16(bflo(u0) * SCALE, bfhi(u0) * SCALE), w1 = pk_bf16(bflo(u1) * SCALE, bfhi(u1) * SCALE);
    s[0] = bflo(w0); s[1] = bfhi(w0); s[2] = bflo(w1); s[3] = bfhi(w1);
  }
  float tm = fmaxf(fmaxf(s[0], s[1]), fmaxf(s[2], s[3]));
  tm = xrow_max(tm);
  const float mt = __builtin_ceilf(tm * L2E);
  const float mn = fmaxf(st.m, mt);
  const double alpha = (double)__builtin_amdgcn_exp2f(st.m - mn);
  const float beta = __builtin_amdgcn_exp2f(mt - mn);
  st.m = mn;
  float p[4];
#pragma unroll
  for (int t = 0; t < 4; ++t) p[t] = __builtin_amdgcn_exp2f(fmaf(s[t], L2E, -mt));
  f32x4 ta = {0.f, 0.f, 0.f, 0.f};
#pragma unroll
  for (int t = 0; t < 4; ++t) ta = mfma(VB[t], diag(p[t], msk), ta);
  st.l = __builtin_fma(st.l, alpha, (double)(((p[0] + p[1]) + (p[2] + p[3])) * beta));
#pragma unroll
  for (int r = 0; r < 4; ++r) st.acc[r] = __builtin_fma(st.acc[r], alpha, (double)(ta[r] * beta));
}
__device__ __forceinline__ void issue(v4u (&KB)[4], v4u (&VB)[4], rsrc_t kr, rsrc_t vr, int voff, int so) {
#pragma unroll
  for (int t = 0; t < 4; ++t) KB[t] = __builtin_amdgcn_raw_buffer_load_b128(kr, voff + t * 1024, so, AUX_NT);
#pragma unroll
  for (int t = 0; t < 4; ++t) VB[t] = __builtin_amdgcn_raw_buffer_load_b128(vr, voff + t * 1024, so, AUX_NT);
}
__device__ __forceinline__ void atomic_inc_cond(unsigned& a, const void* head, unsigned x, unsigned lim, unsigned vzero, unsigned vone) {
  unsigned long long keep;
  asm volatile("s_cmp_lt_u32 %5, %6\n\ts_cbranch_scc0 1f\n\ts_mov_b64 %1, exec\n\ts_mov_b64 exec, 1\n\tglobal_atomic_add %0, %2, %3, %4 sc0\n\ts_mov_b64 exec, %1\n1:"
               : "+v"(a), "=&s"(keep) : "v"(vzero), "v"(vone), "s"(head), "s"(x), "s"(lim) : "memory", "scc");
}
__device__ __forceinline__ unsigned lds_ticket(unsigned lds_addr_v, unsigned vone) {
  unsigned long long keep; unsigned r, s;
  asm volatile("s_mov_b64 %1, exec\n\ts_mov_b64 exec, 1\n\tds_add_rtn_u32 %0, %3, %4\n\ts_mov_b64 exec, %1\n\ts_waitcnt lgkmcnt(0)\n\tv_readfirstlane_b32 %2, %0\n\ts_nop 1"
               : "=&v"(r), "=&s"(keep), "=s"(s) : "v"(lds_addr_v), "v"(vone) : "memory");
  return s;
}
template <int CTRL> __device__ __forceinline__ unsigned dppu(unsigned x) { return (unsigned)__builtin_amdgcn_mov_dpp((int)x, CTRL, 0xf, 0xf, true); }
template <int CTRL> __device__ __forceinline__ double dppd(double x) {
  const v2u u = __builtin_bit_cast(v2u, x);
  v2u r; r[0] = dppu<CTRL>(u[0]); r[1] = dppu<CTRL>(u[1]);
  return __builtin_bit_cast(double, r);
}
__device__ __forceinline__ double xrow_sumd(double x) {
  v2u u = __builtin_bit_cast(v2u, x), a, b2;
  { auto s0 = __builtin_amdgcn_permlane16_swap(u[0], u[0], false, false); auto s1 = __builtin_amdgcn_permlane16_swap(u[1], u[1], false, false);
    a[0] = s0[0]; a[1] = s1[0]; b2[0] = s0[1]; b2[1] = s1[1]; }
  x = __builtin_bit_cast(double, a) + __builtin_bit_cast(double, b2);
  u = __builtin_bit_cast(v2u, x);
  { auto s0 = __builtin_amdgcn_permlane32_swap(u[0], u[0], false, false); auto s1 = __builtin_amdgcn_permlane32_swap(u[1], u[1], false, false);
    a[0] = s0[0]; a[1] = s1[0]; b2[0] = s0[1]; b2[1] = s1[1]; }
  return __builtin_bit_cast(double, a) + __builtin_bit_cast(double, b2);
}
__device__ __forceinline__ void lds_inc(unsigned char* smem, int idx, int lane) {
  LDSW();
  if (lane == 0) __hip_atomic_fetch_add((LAS unsigned*)(LAS unsigned char*)smem + (L_SYNC / 4 + idx), 1u, __ATOMIC_RELAXED, __HIP_MEMORY_SCOPE_WORKGROUP);
}
template <int SLEEP> __device__ __forceinline__ void lds_wait(unsigned char* smem, int idx, unsigned want) {
#pragma unroll 1
  for (unsigned spins = 0; spins < 64u * SPIN_CAP; ++spins) {
    const unsigned v = __builtin_amdgcn_readfirstlane(*((volatile LAS unsigned*)(LAS unsigned char*)smem + (L_SYNC / 4 + idx)));
    if (v >= want) break;
    if (SLEEP) __builtin_amdgcn_s_sleep(SLEEP);
  }
  asm volatile("" ::: "memory");
}

__device__ __forceinline__ void fma8(float (&acc)[8], float xv, const v4u& w) {
#pragma unroll
  for (int k = 0; k < 4; ++k) { acc[2 * k] = fmaf(xv, bflo(w[k]), acc[2 * k]); acc[2 * k + 1] = fmaf(xv, bfhi(w[k]), acc[2 * k + 1]); }
}
template <bool BIT2> __device__ __forceinline__ f32x2 red8(const float (&acc)[8]) {
  float a[4];
#pragma unroll
  for (int j = 0; j < 4; ++j) a[j] = swap32_sum(acc[j], acc[j + 4]);
  float b0 = swap16_sum(a[0], a[2]), b1 = swap16_sum(a[1], a[3]);
  if (BIT2) { b0 += dpp<ROR4>(b0); b1 += dpp<ROR4>(b1); }
  b0 += dpp<ROR8>(b0); b1 += dpp<ROR8>(b1);
  f32x2 r; r[0] = b0; r[1] = b1; return r;
}
__device__ __forceinline__ float val(unsigned u) { return __uint_as_float(u & ~1u); }
__device__ __forceinline__ bool all4(const v4u& p) { return (p[0] & p[1] & p[2] & p[3] & 1u) != 0u; }

__global__ void __launch_bounds__(NTHR, 2)
k_fused(const unsigned short* __restrict__ x, const unsigned char* Kc, const unsigned char* Vc,
        const unsigned char* __restrict__ wq, const unsigned char* wo, const unsigned char* wg,
        const unsigned char* wu, const unsigned char* wd, const unsigned short* __restrict__ gw,
        unsigned char* ws, unsigned* out) {
  __shared__ __attribute__((aligned(16))) unsigned char smem[LDS_BYTES];
  const int tid = threadIdx.x, lane = tid & 63, w = __builtin_amdgcn_readfirstlane(tid >> 6);
  const int bid = blockIdx.x, grp = bid >> 3, sp = bid & 7, b = grp >> 4, kvh = grp & 15;
  const int g = lane >> 4, c = lane & 15;
  unsigned tsv = 0xffffffffu; (void)tsv;
  STAMP(0);
  const int voff = lane * 16;
  const int ridx = (grp + 4 * sp) & 31;
  const bool has_task = ridx < 18;
  const int task = sp * 18 + ridx;
  const bool is_final = ridx >= 18 && ridx < 27;
  const int fid = sp * 9 + ridx - 18;
  const bool hi8 = (c & 8) != 0;
#define MAKE_MSK() v4u msk; _Pragma("unroll") for (int d = 0; d < 4; ++d) msk[d] = (((c & 7) >> 1) == d) ? ((c & 1) ? 0xffff0000u : 0x0000ffffu) : 0u;

  auto gather_o = [&](bool watch_flag) {
    const rsrc_t wsr = mk_rsrc(ws, WS_END);
    const int o_acc = WS_H1 + (grp * 8 + 4 * (c >> 3)) * H1_STRIDE + (32 * sp + 8 * g + (c & 7)) * 8;
    const int o_ml = WS_H1 + (grp * 8 + (c & 7)) * H1_STRIDE + 2048 + (sp >> 2) * 16;
    v2u pv[4];
    v4u pm = {0u, 0u, 0u, 0u};
#pragma unroll
    for (int i = 0; i < 4; ++i) pv[i] = v2u{0u, 0u};
    for (unsigned spins = 0;; ++spins) {
      if (H1MASK) {
        if (!(pv[0][0] & pv[1][0] & pv[2][0] & pv[3][0] & 1u)) {
#pragma unroll
          for (int i = 0; i < 4; ++i) pv[i] = __builtin_amdgcn_raw_buffer_load_b64(wsr, o_acc, i * H1_STRIDE, AUX_SC1);
        }
        if (!(pm[0] & pm[2] & 1u)) pm = __builtin_amdgcn_raw_buffer_load_b128(wsr, o_ml, 0, AUX_SC1);
      } else {
#pragma unroll
        for (int i = 0; i < 4; ++i) pv[i] = __builtin_amdgcn_raw_buffer_load_b64(wsr, o_acc, i * H1_STRIDE, AUX_SC1);
        pm = __builtin_amdgcn_raw_buffer_load_b128(wsr, o_ml, 0, AUX_SC1);
      }
      if (__all((pv[0][0] & pv[1][0] & pv[2][0] & pv[3][0] & pm[0] & pm[2] & 1u) != 0u) || spins > SPIN_CAP) break;
      if (watch_flag && __builtin_amdgcn_readfirstlane(*((volatile LAS unsigned*)(smem + L_SYNC) + 4)) != 0u) return;
      __builtin_amdgcn_s_sleep(PSLEEP);
    }
    STAMP(8);
    const float Ms = __uint_as_float(pm[1]);
    float Mx = fmaxf(Ms, dpp<XOR1>(Ms)); Mx = fmaxf(Mx, dpp<XOR2>(Mx)); Mx = fmaxf(Mx, dpp<HMIR>(Mx));
    const float wt = __builtin_amdgcn_exp2f(Ms - Mx);
    v2u lv; lv[0] = pm[2]; lv[1] = pm[3];
    double den = (double)wt * __builtin_bit_cast(double, lv);
    den += dppd<XOR1>(den); den += dppd<XOR2>(den); den += dppd<HMIR>(den);
    double num = 0.0;
#define ACC1(i) { const float wl_ = dpp<0x150 + (i)>(wt), wh_ = dpp<0x154 + (i)>(wt); num = __builtin_fma((double)(hi8 ? wh_ : wl_), __builtin_bit_cast(double, pv[i]), num); }
    ACC1(0) ACC1(1) ACC1(2) ACC1(3)
#undef ACC1
    num += dppd<ROR8>(num);
    const float ov = (float)num / (float)den;
    *(LAS unsigned*)(smem + L_OS + lane * 4) = pk_bf16(ov, ov);
    lds_inc(smem, 4, lane);
  };

  if (w >= 4) {
    const int sw = w - 4, stid = tid - 256;
    const rsrc_t kr = mk_rsrc(Kc + (size_t)grp * (SK * 256), SK * 256);
    const rsrc_t vr = mk_rsrc(Vc + (size_t)grp * (SK * 256), SK * 256);
    const unsigned sbase = (unsigned)sp * 256u;
    v4u Kb[2][4], Vb[2][4], Ke[NE][4], Ve[NE][4];
    issue(Ke[0], Ve[0], kr, vr, voff, (int)((sbase + sw) * 4096u));
    __builtin_amdgcn_sched_barrier(0);
    asm volatile("" ::: "memory");
    __builtin_amdgcn_s_barrier();
    asm volatile("" ::: "memory");
    STAMP(1);
    MAKE_MSK();
#pragma unroll
    for (int e = 1; e < NE; ++e) {
      DRAIN();
      issue(Ke[e], Ve[e], kr, vr, voff, (int)((sbase + 4 * e + sw) * 4096u));
      __builtin_amdgcn_sched_barrier(0);
    }
    DRAIN();
    issue(Kb[0], Vb[0], kr, vr, voff, (int)((sbase + 4 * NE + sw) * 4096u));
    __builtin_amdgcn_sched_barrier(0);
    DRAIN();
    issue(Kb[1], Vb[1], kr, vr, voff, (int)((sbase + 4 * NE + 4 + sw) * 4096u));
    __builtin_amdgcn_sched_barrier(0);
    STAMP(2);
    const unsigned char* head = ws + WS_CNT + grp * 256;
    unsigned vzero = 0u, vone = 1u, a = 0u;
    unsigned taddr = (unsigned)(size_t)(LAS unsigned char*)(smem + L_SYNC + 28);
    asm volatile("" : "+v"(vzero), "+v"(vone), "+v"(taddr), "+v"(a));
    unsigned lastt = 0;
#define NEXT(nx)                                                                                                   \
    {                                                                                                              \
      unsigned cmpv;                                                                                               \
      if (lastt + 4 < NSW) {                                                                                       \
        lastt = lds_ticket(taddr, vone);                                                                           \
        nx = sbase + lastt;                                                                                        \
        cmpv = (lastt + 4 >= NSW) ? 0u : ND;                                                                       \
      } else {                                                                                                     \
        cmpv = __builtin_amdgcn_readfirstlane(a);                                                                  \
        nx = cmpv < ND ? (cmpv & 7u) * 256u + NSW + (cmpv >> 3) : NONE;                                            \
        if (cmpv + NEARD >= ND) *((volatile LAS unsigned*)(smem + L_SYNC) + 2) = 1u;                               \
      }                                                                                                            \
      atomic_inc_cond(a, head, cmpv, ND, vzero, vone);                                                             \
    }
    lds_wait<1>(smem, 0, 1u);
    STAMP(3);
    const v4u q0 = *(const LAS v4u*)(smem + L_QS + c * 16), q1 = *(const LAS v4u*)(smem + L_QS + 256 + c * 16);
    St st; st.m = -INFINITY; st.l = 0.0; st.acc = f64x4{0.0, 0.0, 0.0, 0.0};
    process(Kb[0], Vb[0], q0, q1, msk, hi8, st); pin(st);
    __builtin_amdgcn_sched_barrier(0);
    {
      unsigned nx;
      NEXT(nx);
      issue(Kb[0], Vb[0], kr, vr, voff, (int)(nx * 4096u));
    }
    __builtin_amdgcn_sched_barrier(0);
#pragma unroll
    for (int e = 0; e < NE; ++e) {
      process(Ke[e], Ve[e], q0, q1, msk, hi8, st); pin(st);
      __builtin_amdgcn_sched_barrier(0);
    }
    process(Kb[1], Vb[1], q0, q1, msk, hi8, st); pin(st);
    __builtin_amdgcn_sched_barrier(0);
    while (true) {
      unsigned nx;
      __builtin_amdgcn_s_waitcnt(0x0F70);
      asm volatile("s_waitcnt vmcnt(0)" : "+v"(a) :: "memory");
      NEXT(nx);
      issue(Kb[1], Vb[1], kr, vr, voff, (int)(nx * 4096u));
      __builtin_amdgcn_sched_barrier(0);
      process(Kb[0], Vb[0], q0, q1, msk, hi8, st); pin(st);
      __builtin_amdgcn_sched_barrier(0);
      if (nx >= NONE) break;
      __builtin_amdgcn_s_waitcnt(0x0F70);
      asm volatile("s_waitcnt vmcnt(0)" : "+v"(a) :: "memory");
      NEXT(nx);
      issue(Kb[0], Vb[0], kr, vr, voff, (int)(nx * 4096u));
      __builtin_amdgcn_sched_barrier(0);
      process(Kb[1], Vb[1], q0, q1, msk, hi8, st); pin(st);
      __builtin_amdgcn_sched_barrier(0);
      if (nx >= NONE) break;
    }
#undef NEXT
    STAMP(6);
    const rsrc_t wsr = mk_rsrc(ws, WS_END);
    {
      const double l = xrow_sumd(st.l);
      const int h = c >> 3;
#pragma unroll
      for (int r = 0; r < 4; ++r) {
        const int dim = 8 * (4 * g + r) + (c & 7);
        *(LAS double*)(smem + L_MRG + (sw * 256 + h * 128 + (dim & 96) + (dim & 3) * 8 + ((dim >> 2) & 7)) * 8) = st.acc[r];
      }
      if (g == 0 && (c & 7) == 0) { *(LAS float*)(smem + L_ML + (h * 4 + sw) * 4) = st.m; *(LAS double*)(smem + L_ML + 32 + (h * 4 + sw) * 8) = l; }
    }
    lds_inc(smem, 3, lane);
    lds_wait<0>(smem, 3, 4u);
    STAMP(7);
    {
      const int hh = sw >> 1;
      const f32x4 m4 = *(const LAS f32x4*)(smem + L_ML + hh * 16);
      const f64x4 l4 = *(const LAS f64x4*)(smem + L_ML + 32 + hh * 32);
      double a4[4];
#pragma unroll
      for (int ww = 0; ww < 4; ++ww) a4[ww] = *(const LAS double*)(smem + L_MRG + (ww * 256 + stid) * 8);
      const float M = fmaxf(fmaxf(m4[0], m4[1]), fmaxf(m4[2], m4[3]));
      double L = 0.0, o = 0.0;
#pragma unroll
      for (int ww = 0; ww < 4; ++ww) {
        const double wt = (double)__builtin_amdgcn_exp2f(m4[ww] - M);
        L = __builtin_fma(wt, l4[ww], L); o = __builtin_fma(wt, a4[ww], o);
      }
      v2u ov = __builtin_bit_cast(v2u, o); ov[0] |= 1u;
      __builtin_amdgcn_raw_buffer_store_b64(ov, wsr, WS_H1 + bid * H1_STRIDE + stid * 8, 0, AUX_SC1);
      if ((stid & 127) == 0) {
        const v2u lv = __builtin_bit_cast(v2u, L);
        v4u ml; ml[0] = 1u; ml[1] = __float_as_uint(M); ml[2] = lv[0] | 1u; ml[3] = lv[1];
        __builtin_amdgcn_raw_buffer_store_b128(ml, wsr, WS_H1 + bid * H1_STRIDE + 2048 + hh * 16, 0, AUX_SC1);
      }
    }
    STAMP(14);
    if (H1P) {
      for (int i = 0; i < sw; ++i) __builtin_amdgcn_s_sleep(STAG);
      gather_o(true);
    }
    if (is_final) {
      const int src = WS_H2 + fid * 8192 + sw * 2048 + voff;
      lds_wait<8>(smem, 6, 1u);
      v4u p0, p1;
      for (unsigned spins = 0;; ++spins) {
        p0 = __builtin_amdgcn_raw_buffer_load_b128(wsr, src, 0, AUX_SC1);
        p1 = __builtin_amdgcn_raw_buffer_load_b128(wsr, src, 1024, AUX_SC1);
        const unsigned m = (p0[0] & p0[1] & p0[2] & p0[3]) & (p1[0] & p1[1] & p1[2] & p1[3]) & 1u;
        if (__all(m != 0u) || spins > SPIN_CAP) break;
        __builtin_amdgcn_s_sleep(PSLEEP);
      }
      STAMP(10);
      f32x4 t4;
#pragma unroll
      for (int j = 0; j < 4; ++j) {
        float t = val(p0[j]) + val(p1[j]);
        t += dpp<ROR4>(t); t += dpp<ROR8>(t);
        t4[j] = xrow_sum(t);
      }
      if (lane < 4) *(LAS f32x4*)(smem + L_FB + (sw * 4 + lane) * 16) = t4;
      LDSW();
      if (lds_ticket(taddr + 4, vone) == 3u && lane < 8) {
        const float gt = *(const LAS float*)(smem + L_FFN + 64), og = rbf(1.f - gt);
        const f32x2 fv = *(const LAS f32x2*)(smem + L_FFN + lane * 8);
        f32x2 t = *(const LAS f32x2*)(smem + L_FB + lane * 8);
#pragma unroll
        for (int ww = 1; ww < 4; ++ww) t += *(const LAS f32x2*)(smem + L_FB + ww * 64 + lane * 8);
        out[fid * 8 + lane] = pk_bf16(rbf(gt * rbf(t[0])) + rbf(og * rbf(fv[0])), rbf(gt * rbf(t[1])) + rbf(og * rbf(fv[1])));
      }
    }
  } else {
    const int hw = w;
    unsigned short xq[9];
    v4u wqv[9];
    {
      const int r = lane >> 3, row0 = 288 * (sp & 1) + 72 * hw;
      const rsrc_t qr = mk_rsrc(wq, D * 8192);
      const int base = (row0 + r) * 8192 + (kvh * 256 + (sp >> 1) * 64) * 2 + (lane & 7) * 16;
#pragma unroll
      for (int i = 0; i < 9; ++i) wqv[i] = __builtin_amdgcn_raw_buffer_load_b128(qr, base, i * 8 * 8192, AUX_NT);
#pragma unroll
      for (int i = 0; i < 9; ++i) xq[i] = x[b * D + row0 + 8 * i + r];
    }
    const int xi = tid < 144 ? tid : 143;
    const v2u xa = ((const v2u*)x)[xi], xb = ((const v2u*)x)[144 + xi];
    if (tid < 3) { const v4u z = {0u, 0u, 0u, tid == 1 ? 4u * (NE + 2) : 0u}; *(LAS v4u*)(smem + L_SYNC + tid * 16) = z; }
    __builtin_amdgcn_sched_barrier(0);
    BAR();
    STAMP(1);
    const rsrc_t wsr = mk_rsrc(ws, WS_END);
    unsigned long long ev;
    asm volatile("s_load_dwordx2 %0, %1, 0x0" : "=s"(ev) : "s"(ws) : "memory");
    if (hw == 3) {
      const v4u z = {0u, 0u, 0u, 0u};
      __builtin_amdgcn_raw_buffer_store_b128(z, wsr, WS_H1 + bid * H1_STRIDE + voff, 0, AUX_SC1);
      __builtin_amdgcn_raw_buffer_store_b128(z, wsr, WS_H1 + bid * H1_STRIDE + 1024 + voff, 0, AUX_SC1);
      const int h2o = WS_H2 + (b * 36 * 128 + (bid & 127)) * 64 + (lane >> 2) * 8192 + (lane & 3) * 16;
      __builtin_amdgcn_raw_buffer_store_b128(z, wsr, h2o, 0, AUX_SC1);
      __builtin_amdgcn_raw_buffer_store_b128(z, wsr, h2o, 16 * 8192, AUX_SC1);
      int off = h2o + 32 * 8192;
      if (lane >= 16) off = WS_H1 + bid * H1_STRIDE + 2048 + (lane & 1) * 16;
      if (lane >= 32) off = sp == 0 ? WS_CNT + grp * 256 : OOB;
      __builtin_amdgcn_raw_buffer_store_b128(z, wsr, off, 0, AUX_SC1);
    }
    __builtin_amdgcn_sched_barrier(0);
    {
      float acc[8];
#pragma unroll
      for (int j = 0; j < 8; ++j) acc[j] = 0.f;
#pragma unroll
      for (int i = 0; i < 9; ++i) fma8(acc, bflo(xq[i]), wqv[i]);
      const f32x2 r2 = red8<false>(acc);
      if ((lane & 8) == 0) *(LAS f32x2*)(smem + L_QP + (hw * 64 + (lane & 7) * 8 + 4 * (lane >> 5) + 2 * ((lane >> 4) & 1)) * 4) = r2;
      if (tid < 144) {
        v4u xs4;
        xs4[0] = (xa[0] & 0xffffu) | (xb[0] << 16); xs4[1] = (xa[0] >> 16) | (xb[0] & 0xffff0000u);
        xs4[2] = (xa[1] & 0xffffu) | (xb[1] << 16); xs4[3] = (xa[1] >> 16) | (xb[1] & 0xffff0000u);
        *(LAS v4u*)(smem + L_XS + tid * 16) = xs4;
      }
      lds_inc(smem, 5, lane);
      lds_wait<0>(smem, 5, 4u);
    }
    STAMP(12);
    MAKE_MSK();
    v4u WO[16];
    const rsrc_t wor = mk_rsrc(wo, 4096 * D * 2);
    const int wo_lo = (kvh * 256 + 32 * sp + g) * 1152 + hw * 256 + c * 16;
    const int wo_hi = (hw == 0 && c < 8) ? wo_lo + 1024 : OOB;
    if (hw == 3) {
      asm volatile("" : "+s"(ev));
      const unsigned long long tag64 = ev * 6364136223846793005ull + 1442695040888963407ull;
      const unsigned tag_hi = (unsigned)(tag64 >> 32);
      const unsigned tag_lo8 = ((unsigned)tag64 >> 24) & 0xffu;
      const int fb = fid >= 36 ? 1 : 0;
      {
        float s = 0.f;
#pragma unroll
        for (int ww = 0; ww < 4; ++ww) s += *(const LAS float*)(smem + L_QP + (ww * 64 + lane) * 4);
        DRAIN();
        v2u gr; gr[0] = ((__float_as_uint(s) + 0x80u) & 0xffffff00u) | tag_lo8; gr[1] = tag_hi;
        __builtin_amdgcn_raw_buffer_store_b64(gr, wsr, WS_QG + ((grp * 2 + (sp & 1)) * 256 + (sp >> 1) * 64 + lane) * 8, 0, AUX_SC1);
      }
      STAMP(13);
      for (unsigned spins = 0;; ++spins) {
        v4u p[4];
#pragma unroll
        for (int i = 0; i < 4; ++i) p[i] = __builtin_amdgcn_raw_buffer_load_b128(wsr, WS_QG + grp * 4096 + lane * 32 + (i & 1) * 16 + (i >> 1) * 2048, 0, AUX_SC1);
        unsigned bad = 0;
#pragma unroll
        for (int i = 0; i < 4; ++i) bad |= (p[i][1] ^ tag_hi) | (p[i][3] ^ tag_hi) | ((p[i][0] ^ tag_lo8) & 0xffu) | ((p[i][2] ^ tag_lo8) & 0xffu);
        if (__all(bad == 0u) || spins > SPIN_CAP) {
          float qv[4];
#pragma unroll
          for (int i = 0; i < 4; ++i)
            qv[i] = __uint_as_float(p[i >> 1][(i & 1) * 2] & 0xffffff00u) + __uint_as_float(p[2 + (i >> 1)][(i & 1) * 2] & 0xffffff00u);
          v2u qq; qq[0] = pk_bf16(qv[0], qv[1]); qq[1] = pk_bf16(qv[2], qv[3]);
          *(LAS v2u*)(smem + L_QS + lane * 8) = qq;
          break;
        }
        __builtin_amdgcn_s_sleep(2);
      }
      lds_inc(smem, 0, lane);
      STAMP(3);
      if (has_task) {
        const rsrc_t dr = mk_rsrc(wd, F * D * 2);
        const int dbase = (task * 32 + g) * 1152 + c * 16;
        f32x4 da[5];
#pragma unroll
        for (int cb = 0; cb < 5; ++cb) da[cb] = f32x4{0.f, 0.f, 0.f, 0.f};
#pragma unroll
        for (int hf = 0; hf < 2; ++hf) {
          v4u wv[20];
#pragma unroll
          for (int i = 0; i < 4; ++i)
#pragma unroll
            for (int cb = 0; cb < 5; ++cb)
              wv[i * 5 + cb] = __builtin_amdgcn_raw_buffer_load_b128(dr, (cb == 4 && c >= 8) ? OOB : dbase + cb * 256, (hf * 4 + i) * 4608, AUX_NT);
          __builtin_amdgcn_sched_barrier(0);
          if (hf == 0) {
            lds_wait<4>(smem, 1, 3u);
            const LAS float* gu = (const LAS float*)(smem + L_GU) + lane;
            const float gv = rbf(gu[0] + gu[128] + gu[256]), uv = rbf(gu[64] + gu[192] + gu[320]);
            const float inner = 0.7978845608028654f * (gv + 0.044715f * gv * gv * gv);
            const float ge = rbf(0.5f * gv * (1.f + tanhf(inner)));
            *(LAS float*)(smem + L_HS + lane * 4) = rbf(ge * uv);
            LDSW();
          }
#pragma unroll
          for (int i = 0; i < 4; ++i) {
            const v4u bb = diag(*(const LAS float*)(smem + L_HS + ((c >> 3) * 32 + (hf * 4 + i) * 4 + g) * 4), msk);
#pragma unroll
            for (int cb = 0; cb < 5; ++cb) da[cb] = mfma(wv[i * 5 + cb], bb, da[cb]);
          }
          __builtin_amdgcn_sched_barrier(0);
        }
#pragma unroll
        for (int cb = 0; cb < 5; ++cb)
#pragma unroll
          for (int rr = 0; rr < 4; ++rr) {
            const int col = cb * 128 + (4 * g + rr) * 8 + (c & 7);
            if (col < D) *(LAS float*)(smem + L_FP + ((c >> 3) * D + col) * 4) = da[cb][rr];
          }
        LDSW();
#pragma unroll
        for (int i = 0; i < 5; ++i) {
          const int o = (i * 64 + lane) * 16;
          if (o < 4608) {
            const v4u v = *(const LAS v4u*)(smem + L_FP + o);
            __builtin_amdgcn_raw_buffer_store_b128(v, wsr, WS_FP + task * 4608 + o, 0, AUX_SC1);
          }
        }
        DRAIN();
        if (lane == 0) {
          v2u fl; fl[0] = (unsigned)tag64; fl[1] = tag_hi;
          __builtin_amdgcn_raw_buffer_store_b64(fl, wsr, WS_FF + task * 64, 0, AUX_SC1);
        }
      }
      STAMP(4);
      if (is_final) {
        for (unsigned spins = 0;; ++spins) {
          unsigned bad = 0;
#pragma unroll
          for (int i = 0; i < 4; ++i) {
            const v2u p = __builtin_amdgcn_raw_buffer_load_b64(wsr, WS_QG + (i * 64 + lane) * 512, 0, AUX_SC1);
            bad |= (p[1] ^ tag_hi) | ((p[0] ^ tag_lo8) & 0xffu);
          }
          if (__all(bad == 0u) || spins > SPIN_CAP) break;
          __builtin_amdgcn_s_sleep(16);
        }
        if (fid == 0 && lane == 0) {
          v2u e; e[0] = (unsigned)tag64; e[1] = tag_hi;
          __builtin_amdgcn_raw_buffer_store_b64(e, wsr, WS_E, 0, AUX_SC1);
        }
        {
          float z = 0.f;
#pragma unroll
          for (int i = 0; i < 9; ++i) {
            const unsigned xv = *(const LAS unsigned*)(smem + L_XS + (i * 64 + lane) * 4);
            z += rbf((fb ? bfhi(xv) : bflo(xv)) * bflo(gw[i * 64 + lane]));
          }
          z += dpp<XOR1>(z); z += dpp<XOR2>(z); z += dpp<ROR4>(z); z += dpp<ROR8>(z);
          z = rbf(xrow_sum(z));
          if (lane == 0) *(LAS float*)(smem + L_FFN + 64) = rbf(1.f / (1.f + __expf(-z)));
        }
        for (unsigned spins = 0;; ++spins) {
          unsigned bad = 0;
#pragma unroll
          for (int i = 0; i < 3; ++i) {
            const int tk = i * 64 + lane;
            const v2u p = __builtin_amdgcn_raw_buffer_load_b64(wsr, WS_FF + (tk < 144 ? tk : 0) * 64, 0, AUX_SC1);
            bad |= (p[1] ^ tag_hi) | (p[0] ^ (unsigned)tag64);
          }
          if (__all(bad == 0u) || spins > SPIN_CAP) break;
          __builtin_amdgcn_s_sleep(64);
        }
        __builtin_amdgcn_fence(__ATOMIC_ACQUIRE, "wavefront");
        {
          const int fo = WS_FP + (lane >> 2) * 4608 + (fid * 16 + (lane & 3) * 4) * 4;
          v4u pv[9];
#pragma unroll
          for (int i = 0; i < 9; ++i) pv[i] = __builtin_amdgcn_raw_buffer_load_b128(wsr, fo, i * 16 * 4608, AUX_SC1);
          f32x4 fa = {0.f, 0.f, 0.f, 0.f};
#pragma unroll
          for (int i = 0; i < 9; ++i) fa += __builtin_bit_cast(f32x4, pv[i]);
#pragma unroll
          for (int j = 0; j < 4; ++j) { float t = fa[j]; t += dpp<ROR4>(t); t += dpp<ROR8>(t); fa[j] = xrow_sum(t); }
          if (lane < 4) *(LAS f32x4*)(smem + L_FFN + lane * 16) = fa;
        }
        lds_inc(smem, 6, lane);
      }
      STAMP(5);
    } else if (has_task) {
      const int r = lane >> 2;
      const int gbase = (hw * 192 + r) * (F * 2) + task * 64 + (lane & 3) * 16;
#pragma unroll 1
      for (int mat = 0; mat < 2; ++mat) {
        const rsrc_t gr = mk_rsrc(mat ? wu : wg, D * F * 2);
        float a0[8], a1[8];
#pragma unroll
        for (int j = 0; j < 8; ++j) { a0[j] = 0.f; a1[j] = 0.f; }
#pragma unroll 1
        for (int bt = 0; bt < 2; ++bt) {
          v4u wv[6];
#pragma unroll
          for (int i = 0; i < 6; ++i) wv[i] = __builtin_amdgcn_raw_buffer_load_b128(gr, gbase, (bt * 6 + i) * 16 * (F * 2), AUX_NT);
          __builtin_amdgcn_sched_barrier(0);
#pragma unroll
          for (int i = 0; i < 6; ++i) {
            const unsigned xv = *(const LAS unsigned*)(smem + L_XS + (hw * 192 + (bt * 6 + i) * 16 + r) * 4);
            fma8(a0, bflo(xv), wv[i]); fma8(a1, bfhi(xv), wv[i]);
          }
        }
        const f32x2 r0 = red8<true>(a0), r1 = red8<true>(a1);
        if ((lane & 12) == 0) {
          const int col = (lane & 3) * 8 + 4 * (lane >> 5) + 2 * ((lane >> 4) & 1);
          *(LAS f32x2*)(smem + L_GU + (((hw * 2 + mat) * 2 + 0) * 32 + col) * 4) = r0;
          *(LAS f32x2*)(smem + L_GU + (((hw * 2 + mat) * 2 + 1) * 32 + col) * 4) = r1;
        }
      }
      lds_inc(smem, 1, lane);
    }
#pragma unroll
    for (int i = 0; i < 8; ++i) WO[i] = __builtin_amdgcn_raw_buffer_load_b128(wor, wo_lo, i * 4608, AUX_NT);
#pragma unroll
    for (int i = 8; i < 16; ++i) WO[i] = __builtin_amdgcn_raw_buffer_load_b128(wor, wo_hi, (i - 8) * 4608, AUX_NT);
    __builtin_amdgcn_sched_barrier(0);
    DRAIN();
    lds_wait<32>(smem, 2, 1u);
    if (!H1P && hw == 3) gather_o(false);
    lds_wait<0>(smem, 4, 1u);
    const unsigned pp = *(const LAS unsigned*)(smem + L_OS + lane * 4);
    STAMP(9);
    {
      f32x4 y0 = {0.f, 0.f, 0.f, 0.f}, y1 = {0.f, 0.f, 0.f, 0.f}, z0 = {0.f, 0.f, 0.f, 0.f};
#define PRJ(i, y) { const unsigned t_ = dppu<0x150 + (i)>(pp); v4u bb; bb[0] = t_ & msk[0]; bb[1] = t_ & msk[1]; bb[2] = t_ & msk[2]; bb[3] = t_ & msk[3]; \
                    y = mfma(WO[i], bb, y); z0 = mfma(WO[8 + (i)], bb, z0); }
      PRJ(0, y0) PRJ(1, y1) PRJ(2, y0) PRJ(3, y1) PRJ(4, y0) PRJ(5, y1) PRJ(6, y0) PRJ(7, y1)
#undef PRJ
      y0 += y1;
      const int dst = WS_H2 + ((b * 36 + hw * 8 + 2 * g) * 128 + (bid & 127)) * 64 + c * 4;
      __builtin_amdgcn_raw_buffer_store_b32(__float_as_uint(hi8 ? y0[1] : y0[0]) | 1u, wsr, dst, 0, AUX_SC1);
      __builtin_amdgcn_raw_buffer_store_b32(__float_as_uint(hi8 ? y0[3] : y0[2]) | 1u, wsr, dst, 8192, AUX_SC1);
      if (hw == 0) {
        const int dz = g < 2 ? dst + 32 * 8192 : OOB;
        __builtin_amdgcn_raw_buffer_store_b32(__float_as_uint(hi8 ? z0[1] : z0[0]) | 1u, wsr, dz, 0, AUX_SC1);
        __builtin_amdgcn_raw_buffer_store_b32(__float_as_uint(hi8 ? z0[3] : z0[2]) | 1u, wsr, dz, 8192, AUX_SC1);
      }
    }
    STAMP(15);
  }
  STAMP(11);
  STAMP_FLUSH();
}

extern "C" void kernel_launch(void* const* d_in, const int* in_sizes, int n_in, void* d_out, int out_size, void* d_ws, size_t ws_size,
                              hipStream_t stream) {
  k_fused<<<256, NTHR, 0, stream>>>((const unsigned short*)d_in[0], (const unsigned char*)d_in[1], (const unsigned char*)d_in[2],
                                    (const unsigned char*)d_in[3], (const unsigned char*)d_in[4], (const unsigned char*)d_in[6],
                                    (const unsigned char*)d_in[7], (const unsigned char*)d_in[8], (const unsigned short*)d_in[9],
                                    (unsigned char*)d_ws, (unsigned*)d_out);
}
```

```cpp
#define TOUCH 0
#include <hip/hip_runtime.h>
#include <hip/hip_bf16.h>
#include <stdint.h>

typedef unsigned v4u __attribute__((ext_vector_type(4)));
typedef unsigned v2u __attribute__((ext_vector_type(2)));
typedef float f32x4 __attribute__((ext_vector_type(4)));
typedef float f32x2 __attribute__((ext_vector_type(2)));
typedef short bf16x8 __attribute__((ext_vector_type(8)));
typedef __bf16 bf16x2 __attribute__((ext_vector_type(2)));
typedef __amdgpu_buffer_rsrc_t rsrc_t;
#define LAS __attribute__((address_space(3)))

constexpr int D = 576, SK = 32768, F = 4608;
constexpr int NW = 8, NTHR = NW * 64;
#define NSW_V 224
constexpr unsigned NSW = NSW_V;
constexpr unsigned ND = 8 * (256 - NSW);
constexpr unsigned NONE = 2048;
constexpr int AUX_NT = 2, AUX_SC1 = 16;
constexpr int OOB = 0x40000000;
constexpr unsigned SPIN_CAP = 200000u;
#define H1P 1
#define NE 2
#define STAG 4
#define H1MASK 1
#define NEARD 64
#define PSLEEP 2

constexpr int WS_E = 0;
constexpr int WS_CNT = 4096;
constexpr int WS_QG = 16384;
constexpr int WS_H1 = WS_QG + 131072;
constexpr int H1_STRIDE = 2176;
constexpr int WS_H2 = WS_H1 + 256 * H1_STRIDE;
constexpr int WS_FF = WS_H2 + 144 * 4096;
constexpr int WS_FP = WS_FF + 144 * 64;
constexpr int WS_END = WS_FP + 144 * 4608;

constexpr int L_XS = 0;
constexpr int L_QP = 2304;
constexpr int L_QS = L_QP + 2048;
constexpr int L_MRG = L_QS + 512;
constexpr int L_ML = L_MRG + 8192;
constexpr int L_OS = L_ML + 96;
constexpr int L_GU = L_OS + 256;
constexpr int L_HS = L_GU + 1536;
constexpr int L_FP = L_HS + 256;
constexpr int L_FFN = L_FP + 4608;
constexpr int L_FB = L_FFN + 80;
constexpr int L_SYNC = L_FB + 256;
constexpr int L_END = L_SYNC + 48;
constexpr int LDS_BYTES = 96 * 1024;
static_assert(L_END <= LDS_BYTES, "lds");

__device__ __forceinline__ float bflo(unsigned u) { return __builtin_bit_cast(float, u << 16); }
__device__ __forceinline__ float bfhi(unsigned u) { return __builtin_bit_cast(float, u & 0xffff0000u); }
__device__ __forceinline__ unsigned pk_bf16(float lo, float hi) {
  bf16x2 v; v[0] = (__bf16)lo; v[1] = (__bf16)hi; return __builtin_bit_cast(unsigned, v);
}
__device__ __forceinline__ float rbf(float f) { return bflo(pk_bf16(f, f)); }
__device__ __forceinline__ float dot2(unsigned a, unsigned b, float c) {
  return __builtin_amdgcn_fdot2_f32_bf16(__builtin_bit_cast(bf16x2, a), __builtin_bit_cast(bf16x2, b), c, false);
}
template <int CTRL> __device__ __forceinline__ float dpp(float x) {
  return __builtin_bit_cast(float, __builtin_amdgcn_mov_dpp(__builtin_bit_cast(int, x), CTRL, 0xf, 0xf, true));
}
constexpr int XOR1 = 0xB1, XOR2 = 0x4E, HMIR = 0x141, ROR4 = 0x124, ROR8 = 0x128;
__device__ __forceinline__ f32x4 mfma(v4u a, v4u b, f32x4 c) {
  return __builtin_amdgcn_mfma_f32_16x16x32_bf16(__builtin_bit_cast(bf16x8, a), __builtin_bit_cast(bf16x8, b), c, 0, 0, 0);
}
__device__ __forceinline__ float swap16_sum(float a, float b) {
  auto s = __builtin_amdgcn_permlane16_swap(__float_as_uint(a), __float_as_uint(b), false, false);
  return __uint_as_float(s[0]) + __uint_as_float(s[1]);
}
__device__ __forceinline__ float swap32_sum(float a, float b) {
  auto s = __builtin_amdgcn_permlane32_swap(__float_as_uint(a), __float_as_uint(b), false, false);
  return __uint_as_float(s[0]) + __uint_as_float(s[1]);
}
__device__ __forceinline__ float xrow_max(float x) {
  auto s = __builtin_amdgcn_permlane16_swap(__float_as_uint(x), __float_as_uint(x), false, false);
  x = fmaxf(__uint_as_float(s[0]), __uint_as_float(s[1]));
  auto t = __builtin_amdgcn_permlane32_swap(__float_as_uint(x), __float_as_uint(x), false, false);
  return fmaxf(__uint_as_float(t[0]), __uint_as_float(t[1]));
}
__device__ __forceinline__ float xrow_sum(float x) { x = swap16_sum(x, x); return swap32_sum(x, x); }
__device__ __forceinline__ rsrc_t mk_rsrc(const void* p, int bytes) { return __builtin_amdgcn_make_buffer_rsrc((void*)p, 0, bytes, 0x00020000); }
#define BAR() do { asm volatile("s_waitcnt lgkmcnt(0)" ::: "memory"); __builtin_amdgcn_s_barrier(); asm volatile("" ::: "memory"); } while (0)
#define DRAIN() do { __builtin_amdgcn_s_waitcnt(0x0F70); asm volatile("s_waitcnt vmcnt(0)" ::: "memory"); } while (0)
#define LDSW() asm volatile("s_waitcnt lgkmcnt(0)" ::: "memory")

#define STAMP(i) do {} while (0)
#define STAMP_FLUSH() do {} while (0)
constexpr float SCALE = 0.08838834764831845f, L2E = 1.4426950408889634f;

typedef double f64x4 __attribute__((ext_vector_type(4)));
struct St { float m; double l; f64x4 acc; };
__device__ __forceinline__ void pin(St& st) { asm volatile("" : "+v"(st.m), "+v"(st.l), "+v"(st.acc) :: "memory"); }
__device__ __forceinline__ v4u diag(float v, const v4u& msk) {
  const unsigned pp = pk_bf16(v, v);
  v4u bb; bb[0] = pp & msk[0]; bb[1] = pp & msk[1]; bb[2] = pp & msk[2]; bb[3] = pp & msk[3];
  return bb;
}

__device__ __forceinline__ void process(const v4u (&KB)[4], const v4u (&VB)[4], const v4u& q0, const v4u& q1, const v4u& msk, bool hi8, St& st) {
  float s[4];
#pragma unroll
  for (int t = 0; t < 4; ++t) {
    float a0 = dot2(KB[t][0], q0[0], 0.f), a1 = dot2(KB[t][0], q1[0], 0.f);
#pragma unroll
    for (int k = 1; k < 4; ++k) { a0 = dot2(KB[t][k], q0[k], a0); a1 = dot2(KB[t][k], q1[k], a1); }
    const float keep = hi8 ? a1 : a0, send = hi8 ? a0 : a1;
    float v = keep + dpp<ROR8>(send);
    v += dpp<HMIR>(v); v += dpp<XOR2>(v); v += dpp<XOR1>(v);
    s[t] = v;
  }
  {
    const unsigned u0 = pk_bf16(s[0], s[1]), u1 = pk_bf16(s[2], s[3]);
    const unsigned w0 = pk_bf16(bflo(u0) * SCALE, bfhi(u0) * SCALE), w1 = pk_bf16(bflo(u1) * SCALE, bfhi(u1) * SCALE);
    s[0] = bflo(w0); s[1] = bfhi(w0); s[2] = bflo(w1); s[3] = bfhi(w1);
  }
  float tm = fmaxf(fmaxf(s[0], s[1]), fmaxf(s[2], s[3]));
  tm = xrow_max(tm);
  const float tl = tm * L2E, mt = __builtin_ceilf(tl), fr = tl - mt;
  const float mn = fmaxf(st.m, mt);
  const double alpha = (double)__builtin_amdgcn_exp2f(st.m - mn);
  const float beta = __builtin_amdgcn_exp2f(mt - mn);
  st.m = mn;
  float p[4];
#pragma unroll
  for (int t = 0; t < 4; ++t) p[t] = __builtin_amdgcn_exp2f(fmaf(s[t] - tm, L2E, fr));
  f32x4 ta = {0.f, 0.f, 0.f, 0.f};
#pragma unroll
  for (int t = 0; t < 4; ++t) ta = mfma(VB[t], diag(p[t], msk), ta);
  st.l = __builtin_fma(st.l, alpha, (double)(((p[0] + p[1]) + (p[2] + p[3])) * beta));
#pragma unroll
  for (int r = 0; r < 4; ++r) st.acc[r] = __builtin_fma(st.acc[r], alpha, (double)(ta[r] * beta));
}
__device__ __forceinline__ void issue(v4u (&KB)[4], v4u (&VB)[4], rsrc_t kr, rsrc_t vr, int voff, int so) {
#pragma unroll
  for (int t = 0; t < 4; ++t) KB[t] = __builtin_amdgcn_raw_buffer_load_b128(kr, voff + t * 1024, so, AUX_NT);
#pragma unroll
  for (int t = 0; t < 4; ++t) VB[t] = __builtin_amdgcn_raw_buffer_load_b128(vr, voff + t * 1024, so, AUX_NT);
}
__device__ __forceinline__ void atomic_inc_cond(unsigned& a, const void* head, unsigned x, unsigned lim, unsigned vzero, unsigned vone) {
  unsigned long long keep;
  asm volatile("s_cmp_lt_u32 %5, %6\n\ts_cbranch_scc0 1f\n\ts_mov_b64 %1, exec\n\ts_mov_b64 exec, 1\n\tglobal_atomic_add %0, %2, %3, %4 sc0\n\ts_mov_b64 exec, %1\n1:"
               : "+v"(a), "=&s"(keep) : "v"(vzero), "v"(vone), "s"(head), "s"(x), "s"(lim) : "memory", "scc");
}
__device__ __forceinline__ unsigned lds_ticket(unsigned lds_addr_v, unsigned vone) {
  unsigned long long keep; unsigned r, s;
  asm volatile("s_mov_b64 %1, exec\n\ts_mov_b64 exec, 1\n\tds_add_rtn_u32 %0, %3, %4\n\ts_mov_b64 exec, %1\n\ts_waitcnt lgkmcnt(0)\n\tv_readfirstlane_b32 %2, %0\n\ts_nop 1"
               : "=&v"(r), "=&s"(keep), "=s"(s) : "v"(lds_addr_v), "v"(vone) : "memory");
  return s;
}
template <int CTRL> __device__ __forceinline__ unsigned dppu(unsigned x) { return (unsigned)__builtin_amdgcn_mov_dpp((int)x, CTRL, 0xf, 0xf, true); }
template <int CTRL> __device__ __forceinline__ double dppd(double x) {
  const v2u u = __builtin_bit_cast(v2u, x);
  v2u r; r[0] = dppu<CTRL>(u[0]); r[1] = dppu<CTRL>(u[1]);
  return __builtin_bit_cast(double, r);
}
__device__ __forceinline__ double xrow_sumd(double x) {
  v2u u = __builtin_bit_cast(v2u, x), a, b2;
  { auto s0 = __builtin_amdgcn_permlane16_swap(u[0], u[0], false, false); auto s1 = __builtin_amdgcn_permlane16_swap(u[1], u[1], false, false);
    a[0] = s0[0]; a[1] = s1[0]; b2[0] = s0[1]; b2[1] = s1[1]; }
  x = __builtin_bit_cast(double, a) + __builtin_bit_cast(double, b2);
  u = __builtin_bit_cast(v2u, x);
  { auto s0 = __builtin_amdgcn_permlane32_swap(u[0], u[0], false, false); auto s1 = __builtin_amdgcn_permlane32_swap(u[1], u[1], false, false);
    a[0] = s0[0]; a[1] = s1[0]; b2[0] = s0[1]; b2[1] = s1[1]; }
  return __builtin_bit_cast(double, a) + __builtin_bit_cast(double, b2);
}
__device__ __forceinline__ void lds_inc(unsigned char* smem, int idx, int lane) {
  LDSW();
  if (lane == 0) __hip_atomic_fetch_add((LAS unsigned*)(LAS unsigned char*)smem + (L_SYNC / 4 + idx), 1u, __ATOMIC_RELAXED, __HIP_MEMORY_SCOPE_WORKGROUP);
}
template <int SLEEP> __device__ __forceinline__ void lds_wait(unsigned char* smem, int idx, unsigned want) {
#pragma unroll 1
  for (unsigned spins = 0; spins < 8000000u / (1 + SLEEP); ++spins) {
    const unsigned v = __builtin_amdgcn_readfirstlane(*((volatile LAS unsigned*)(LAS unsigned char*)smem + (L_SYNC / 4 + idx)));
    if (v >= want) break;
    if (SLEEP) __builtin_amdgcn_s_sleep(SLEEP);
  }
  asm volatile("" ::: "memory");
}

__device__ __forceinline__ void fma8(float (&acc)[8], float xv, const v4u& w) {
#pragma unroll
  for (int k = 0; k < 4; ++k) { acc[2 * k] = fmaf(xv, bflo(w[k]), acc[2 * k]); acc[2 * k + 1] = fmaf(xv, bfhi(w[k]), acc[2 * k + 1]); }
}
template <bool BIT2> __device__ __forceinline__ f32x2 red8(const float (&acc)[8]) {
  float a[4];
#pragma unroll
  for (int j = 0; j < 4; ++j) a[j] = swap32_sum(acc[j], acc[j + 4]);
  float b0 = swap16_sum(a[0], a[2]), b1 = swap16_sum(a[1], a[3]);
  if (BIT2) { b0 += dpp<ROR4>(b0); b1 += dpp<ROR4>(b1); }
  b0 += dpp<ROR8>(b0); b1 += dpp<ROR8>(b1);
  f32x2 r; r[0] = b0; r[1] = b1; return r;
}
__device__ __forceinline__ float val(unsigned u) { return __uint_as_float(u & ~1u); }
__device__ __forceinline__ bool all4(const v4u& p) { return (p[0] & p[1] & p[2] & p[3] & 1u) != 0u; }

__global__ void __launch_bounds__(NTHR, 2)
k_fused(const unsigned short* __restrict__ x, const unsigned char* Kc, const unsigned char* Vc,
        const unsigned char* __restrict__ wq, const unsigned char* wo, const unsigned char* wg,
        const unsigned char* wu, const unsigned char* wd, const unsigned short* __restrict__ gw,
        unsigned char* ws, unsigned* out) {
  __shared__ __attribute__((aligned(16))) unsigned char smem[LDS_BYTES];
  const int tid = threadIdx.x, lane = tid & 63, w = __builtin_amdgcn_readfirstlane(tid >> 6);
  const int bid = blockIdx.x, grp = bid >> 3, sp = bid & 7, b = grp >> 4, kvh = grp & 15;
  const int g = lane >> 4, c = lane & 15;
  unsigned tsv = 0xffffffffu; (void)tsv;
  STAMP(0);
  const int voff = lane * 16;
  const int ridx = (grp + 4 * sp) & 31;
  const bool has_task = ridx < 18;
  const int task = sp * 18 + ridx;
  const bool is_final = ridx >= 18 && ridx < 27;
  const int fid = sp * 9 + ridx - 18;
  const bool hi8 = (c & 8) != 0;
#define MAKE_MSK() v4u msk; _Pragma("unroll") for (int d = 0; d < 4; ++d) msk[d] = (((c & 7) >> 1) == d) ? ((c & 1) ? 0xffff0000u : 0x0000ffffu) : 0u;

  auto gather_o = [&](bool watch_flag) {
    const rsrc_t wsr = mk_rsrc(ws, WS_END);
    const int o_acc = WS_H1 + (grp * 8 + 4 * (c >> 3)) * H1_STRIDE + (32 * sp + 8 * g + (c & 7)) * 8;
    const int o_ml = WS_H1 + (grp * 8 + (c & 7)) * H1_STRIDE + 2048 + (sp >> 2) * 16;
    v2u pv[4];
    v4u pm = {0u, 0u, 0u, 0u};
#pragma unroll
    for (int i = 0; i < 4; ++i) pv[i] = v2u{0u, 0u};
    for (unsigned spins = 0;; ++spins) {
      if (H1MASK) {
        if (!(pv[0][0] & pv[1][0] & pv[2][0] & pv[3][0] & 1u)) {
#pragma unroll
          for (int i = 0; i < 4; ++i) pv[i] = __builtin_amdgcn_raw_buffer_load_b64(wsr, o_acc, i * H1_STRIDE, AUX_SC1);
        }
        if (!(pm[0] & pm[2] & 1u)) pm = __builtin_amdgcn_raw_buffer_load_b128(wsr, o_ml, 0, AUX_SC1);
      } else {
#pragma unroll
        for (int i = 0; i < 4; ++i) pv[i] = __builtin_amdgcn_raw_buffer_load_b64(wsr, o_acc, i * H1_STRIDE, AUX_SC1);
        pm = __builtin_amdgcn_raw_buffer_load_b128(wsr, o_ml, 0, AUX_SC1);
      }
      if (__all((pv[0][0] & pv[1][0] & pv[2][0] & pv[3][0] & pm[0] & pm[2] & 1u) != 0u) || spins > SPIN_CAP) break;
      if (watch_flag && __builtin_amdgcn_readfirstlane(*((volatile LAS unsigned*)(smem + L_SYNC) + 4)) != 0u) return;
      __builtin_amdgcn_s_sleep(PSLEEP);
    }
    STAMP(8);
    const float Ms = __uint_as_float(pm[1]);
    float Mx = fmaxf(Ms, dpp<XOR1>(Ms)); Mx = fmaxf(Mx, dpp<XOR2>(Mx)); Mx = fmaxf(Mx, dpp<HMIR>(Mx));
    const float wt = __builtin_amdgcn_exp2f(Ms - Mx);
    v2u lv; lv[0] = pm[2]; lv[1] = pm[3];
    double den = (double)wt * __builtin_bit_cast(double, lv);
    den += dppd<XOR1>(den); den += dppd<XOR2>(den); den += dppd<HMIR>(den);
    double num = 0.0;
#define ACC1(i) { const float wl_ = dpp<0x150 + (i)>(wt), wh_ = dpp<0x154 + (i)>(wt); num = __builtin_fma((double)(hi8 ? wh_ : wl_), __builtin_bit_cast(double, pv[i]), num); }
    ACC1(0) ACC1(1) ACC1(2) ACC1(3)
#undef ACC1
    num += dppd<ROR8>(num);
    const float ov = (float)num / (float)den;
    *(LAS unsigned*)(smem + L_OS + lane * 4) = pk_bf16(ov, ov);
    lds_inc(smem, 4, lane);
  };

  if (w >= 4) {
    const int sw = w - 4, stid = tid - 256;
    const rsrc_t kr = mk_rsrc(Kc + (size_t)grp * (SK * 256), SK * 256);
    const rsrc_t vr = mk_rsrc(Vc + (size_t)grp * (SK * 256), SK * 256);
    const unsigned sbase = (unsigned)sp * 256u;
    v4u Kb[2][4], Vb[2][4], Ke[NE][4], Ve[NE][4];
    issue(Ke[0], Ve[0], kr, vr, voff, (int)((sbase + sw) * 4096u));
    __builtin_amdgcn_sched_barrier(0);
    asm volatile("" ::: "memory");
    __builtin_amdgcn_s_barrier();
    asm volatile("" ::: "memory");
    STAMP(1);
    MAKE_MSK();
#pragma unroll
    for (int e = 1; e < NE; ++e) {
      DRAIN();
      issue(Ke[e], Ve[e], kr, vr, voff, (int)((sbase + 4 * e + sw) * 4096u));
      __builtin_amdgcn_sched_barrier(0);
    }
    DRAIN();
    issue(Kb[0], Vb[0], kr, vr, voff, (int)((sbase + 4 * NE + sw) * 4096u));
    __builtin_amdgcn_sched_barrier(0);
    DRAIN();
    issue(Kb[1], Vb[1], kr, vr, voff, (int)((sbase + 4 * NE + 4 + sw) * 4096u));
    __builtin_amdgcn_sched_barrier(0);
    STAMP(2);
    const unsigned char* head = ws + WS_CNT + grp * 256;
    unsigned vzero = 0u, vone = 1u, a = 0u;
    unsigned taddr = (unsigned)(size_t)(LAS unsigned char*)(smem + L_SYNC + 28);
    asm volatile("" : "+v"(vzero), "+v"(vone), "+v"(taddr), "+v"(a));
    unsigned lastt = 0;
#define NEXT(nx)                                                                                                   \
    {                                                                                                              \
      unsigned cmpv;                                                                                               \
      if (lastt + 4 < NSW) {                                                                                       \
        lastt = lds_ticket(taddr, vone);                                                                           \
        nx = sbase + lastt;                                                                                        \
        cmpv = (lastt + 4 >= NSW) ? 0u : ND;                                                                       \
      } else {                                                                                                     \
        cmpv = __builtin_amdgcn_readfirstlane(a);                                                                  \
        nx = cmpv < ND ? (cmpv & 7u) * 256u + NSW + (cmpv >> 3) : NONE;                                            \
        if (cmpv + NEARD >= ND) *((volatile LAS unsigned*)(smem + L_SYNC) + 2) = 1u;                               \
      }                                                                                                            \
      atomic_inc_cond(a, head, cmpv, ND, vzero, vone);                                                             \
    }
    lds_wait<1>(smem, 0, 1u);
    STAMP(3);
    const v4u q0 = *(const LAS v4u*)(smem + L_QS + c * 16), q1 = *(const LAS v4u*)(smem + L_QS + 256 + c * 16);
    St st; st.m = -INFINITY; st.l = 0.0; st.acc = f64x4{0.0, 0.0, 0.0, 0.0};
    process(Kb[0], Vb[0], q0, q1, msk, hi8, st); pin(st);
    __builtin_amdgcn_sched_barrier(0);
    {
      unsigned nx;
      NEXT(nx);
      issue(Kb[0], Vb[0], kr, vr, voff, (int)(nx * 4096u));
    }
    __builtin_amdgcn_sched_barrier(0);
#pragma unroll
    for (int e = 0; e < NE; ++e) {
      process(Ke[e], Ve[e], q0, q1, msk, hi8, st); pin(st);
      __builtin_amdgcn_sched_barrier(0);
    }
    process(Kb[1], Vb[1], q0, q1, msk, hi8, st); pin(st);
    __builtin_amdgcn_sched_barrier(0);
    while (true) {
      unsigned nx;
      __builtin_amdgcn_s_waitcnt(0x0F70);
      asm volatile("s_waitcnt vmcnt(0)" : "+v"(a) :: "memory");
      NEXT(nx);
      issue(Kb[1], Vb[1], kr, vr, voff, (int)(nx * 4096u));
      __builtin_amdgcn_sched_barrier(0);
      process(Kb[0], Vb[0], q0, q1, msk, hi8, st); pin(st);
      __builtin_amdgcn_sched_barrier(0);
      if (nx >= NONE) break;
      __builtin_amdgcn_s_waitcnt(0x0F70);
      asm volatile("s_waitcnt vmcnt(0)" : "+v"(a) :: "memory");
      NEXT(nx);
      issue(Kb[0], Vb[0], kr, vr, voff, (int)(nx * 4096u));
      __builtin_amdgcn_sched_barrier(0);
      process(Kb[1], Vb[1], q0, q1, msk, hi8, st); pin(st);
      __builtin_amdgcn_sched_barrier(0);
      if (nx >= NONE) break;
    }
#undef NEXT
    STAMP(6);
    const rsrc_t wsr = mk_rsrc(ws, WS_END);
    {
      const double l = xrow_sumd(st.l);
      const int h = c >> 3;
#pragma unroll
      for (int r = 0; r < 4; ++r) {
        const int dim = 8 * (4 * g + r) + (c & 7);
        *(LAS double*)(smem + L_MRG + (sw * 256 + h * 128 + (dim & 96) + (dim & 3) * 8 + ((dim >> 2) & 7)) * 8) = st.acc[r];
      }
      if (g == 0 && (c & 7) == 0) { *(LAS float*)(smem + L_ML + (h * 4 + sw) * 4) = st.m; *(LAS double*)(smem + L_ML + 32 + (h * 4 + sw) * 8) = l; }
    }
    lds_inc(smem, 3, lane);
    lds_wait<0>(smem, 3, 4u);
    STAMP(7);
    {
      const int hh = sw >> 1;
      const f32x4 m4 = *(const LAS f32x4*)(smem + L_ML + hh * 16);
      const f64x4 l4 = *(const LAS f64x4*)(smem + L_ML + 32 + hh * 32);
      double a4[4];
#pragma unroll
      for (int ww = 0; ww < 4; ++ww) a4[ww] = *(const LAS double*)(smem + L_MRG + (ww * 256 + stid) * 8);
      const float M = fmaxf(fmaxf(m4[0], m4[1]), fmaxf(m4[2], m4[3]));
      double L = 0.0, o = 0.0;
#pragma unroll
      for (int ww = 0; ww < 4; ++ww) {
        const double wt = (double)__builtin_amdgcn_exp2f(m4[ww] - M);
        L = __builtin_fma(wt, l4[ww], L); o = __builtin_fma(wt, a4[ww], o);
      }
      v2u ov = __builtin_bit_cast(v2u, o); ov[0] |= 1u;
      __builtin_amdgcn_raw_buffer_store_b64(ov, wsr, WS_H1 + bid * H1_STRIDE + stid * 8, 0, AUX_SC1);
      if ((stid & 127) == 0) {
        const v2u lv = __builtin_bit_cast(v2u, L);
        v4u ml; ml[0] = 1u; ml[1] = __float_as_uint(M); ml[2] = lv[0] | 1u; ml[3] = lv[1];
        __builtin_amdgcn_raw_buffer_store_b128(ml, wsr, WS_H1 + bid * H1_STRIDE + 2048 + hh * 16, 0, AUX_SC1);
      }
    }
    STAMP(14);
    if (H1P) {
      for (int i = 0; i < sw; ++i) __builtin_amdgcn_s_sleep(STAG);
      gather_o(true);
    }
    if (is_final) {
      const int src = WS_H2 + fid * 8192 + sw * 2048 + voff;
      lds_wait<8>(smem, 6, 1u);
      v4u p0, p1;
      for (unsigned spins = 0;; ++spins) {
        p0 = __builtin_amdgcn_raw_buffer_load_b128(wsr, src, 0, AUX_SC1);
        p1 = __builtin_amdgcn_raw_buffer_load_b128(wsr, src, 1024, AUX_SC1);
        const unsigned m = (p0[0] & p0[1] & p0[2] & p0[3]) & (p1[0] & p1[1] & p1[2] & p1[3]) & 1u;
        if (__all(m != 0u) || spins > SPIN_CAP) break;
        __builtin_amdgcn_s_sleep(PSLEEP);
      }
      STAMP(10);
      f32x4 t4;
#pragma unroll
      for (int j = 0; j < 4; ++j) {
        float t = val(p0[j]) + val(p1[j]);
        t += dpp<ROR4>(t); t += dpp<ROR8>(t);
        t4[j] = xrow_sum(t);
      }
      if (lane < 4) *(LAS f32x4*)(smem + L_FB + (sw * 4 + lane) * 16) = t4;
      LDSW();
      if (lds_ticket(taddr + 4, vone) == 3u && lane < 8) {
        const float gt = *(const LAS float*)(smem + L_FFN + 64), og = rbf(1.f - gt);
        const f32x2 fv = *(const LAS f32x2*)(smem + L_FFN + lane * 8);
        f32x2 t = *(const LAS f32x2*)(smem + L_FB + lane * 8);
#pragma unroll
        for (int ww = 1; ww < 4; ++ww) t += *(const LAS f32x2*)(smem + L_FB + ww * 64 + lane * 8);
        out[fid * 8 + lane] = pk_bf16(rbf(gt * rbf(t[0])) + rbf(og * rbf(fv[0])), rbf(gt * rbf(t[1])) + rbf(og * rbf(fv[1])));
      }
    }
  } else {
    const int hw = w;
    unsigned short xq[9];
    v4u wqv[9];
    {
      const int r = lane >> 3, row0 = 288 * (sp & 1) + 72 * hw;
      const rsrc_t qr = mk_rsrc(wq, D * 8192);
      const int base = (row0 + r) * 8192 + (kvh * 256 + (sp >> 1) * 64) * 2 + (lane & 7) * 16;
#pragma unroll
      for (int i = 0; i < 9; ++i) wqv[i] = __builtin_amdgcn_raw_buffer_load_b128(qr, base, i * 8 * 8192, AUX_NT);
#pragma unroll
      for (int i = 0; i < 9; ++i) xq[i] = x[b * D + row0 + 8 * i + r];
    }
    const int xi = tid < 144 ? tid : 143;
    const v2u xa = ((const v2u*)x)[xi], xb = ((const v2u*)x)[144 + xi];
    if (tid < 3) { const v4u z = {0u, 0u, 0u, tid == 1 ? 4u * (NE + 2) : 0u}; *(LAS v4u*)(smem + L_SYNC + tid * 16) = z; }
    __builtin_amdgcn_sched_barrier(0);
    BAR();
    STAMP(1);
    const rsrc_t wsr = mk_rsrc(ws, WS_END);
    v2u evv = {0u, 0u};
    if (hw == 3) {
      evv = __builtin_amdgcn_raw_buffer_load_b64(wsr, WS_E, 0, AUX_SC1);
      const v4u z = {0u, 0u, 0u, 0u};
      __builtin_amdgcn_raw_buffer_store_b128(z, wsr, WS_H1 + bid * H1_STRIDE + voff, 0, AUX_SC1);
      __builtin_amdgcn_raw_buffer_store_b128(z, wsr, WS_H1 + bid * H1_STRIDE + 1024 + voff, 0, AUX_SC1);
      const int h2o = WS_H2 + (b * 36 * 128 + (bid & 127)) * 64 + (lane >> 2) * 8192 + (lane & 3) * 16;
      __builtin_amdgcn_raw_buffer_store_b128(z, wsr, h2o, 0, AUX_SC1);
      __builtin_amdgcn_raw_buffer_store_b128(z, wsr, h2o, 16 * 8192, AUX_SC1);
      int off = h2o + 32 * 8192;
      if (lane >= 16) off = WS_H1 + bid * H1_STRIDE + 2048 + (lane & 1) * 16;
      if (lane >= 32) off = sp == 0 ? WS_CNT + grp * 256 : OOB;
      __builtin_amdgcn_raw_buffer_store_b128(z, wsr, off, 0, AUX_SC1);
    }
    __builtin_amdgcn_sched_barrier(0);
    {
      float acc[8];
#pragma unroll
      for (int j = 0; j < 8; ++j) acc[j] = 0.f;
#pragma unroll
      for (int i = 0; i < 9; ++i) fma8(acc, bflo(xq[i]), wqv[i]);
      const f32x2 r2 = red8<false>(acc);
      if ((lane & 8) == 0) *(LAS f32x2*)(smem + L_QP + (hw * 64 + (lane & 7) * 8 + 4 * (lane >> 5) + 2 * ((lane >> 4) & 1)) * 4) = r2;
      if (tid < 144) {
        v4u xs4;
        xs4[0] = (xa[0] & 0xffffu) | (xb[0] << 16); xs4[1] = (xa[0] >> 16) | (xb[0] & 0xffff0000u);
        xs4[2] = (xa[1] & 0xffffu) | (xb[1] << 16); xs4[3] = (xa[1] >> 16) | (xb[1] & 0xffff0000u);
        *(LAS v4u*)(smem + L_XS + tid * 16) = xs4;
      }
      lds_inc(smem, 5, lane);
      lds_wait<0>(smem, 5, 4u);
    }
    STAMP(12);
    MAKE_MSK();
    v4u WO[16];
    const rsrc_t wor = mk_rsrc(wo, 4096 * D * 2);
    const int wo_lo = (kvh * 256 + 32 * sp + g) * 1152 + hw * 256 + c * 16;
    const int wo_hi = (hw == 0 && c < 8) ? wo_lo + 1024 : OOB;
    if (hw == 3) {
      const unsigned long long ev = ((unsigned long long)(unsigned)__builtin_amdgcn_readfirstlane(evv[1]) << 32) | (unsigned)__builtin_amdgcn_readfirstlane(evv[0]);
      const unsigned long long tag64 = ev * 6364136223846793005ull + 1442695040888963407ull;
      const unsigned tag_hi = (unsigned)(tag64 >> 32);
      const unsigned tag_lo8 = ((unsigned)tag64 >> 24) & 0xffu;
      const int fb = fid >= 36 ? 1 : 0;
      {
        float s = 0.f;
#pragma unroll
        for (int ww = 0; ww < 4; ++ww) s += *(const LAS float*)(smem + L_QP + (ww * 64 + lane) * 4);
        DRAIN();
        v2u gr; gr[0] = ((__float_as_uint(s) + 0x80u) & 0xffffff00u) | tag_lo8; gr[1] = tag_hi;
        __builtin_amdgcn_raw_buffer_store_b64(gr, wsr, WS_QG + ((grp * 2 + (sp & 1)) * 256 + (sp >> 1) * 64 + lane) * 8, 0, AUX_SC1);
      }
      STAMP(13);
      for (unsigned spins = 0;; ++spins) {
        v4u p[4];
#pragma unroll
        for (int i = 0; i < 4; ++i) p[i] = __builtin_amdgcn_raw_buffer_load_b128(wsr, WS_QG + grp * 4096 + lane * 32 + (i & 1) * 16 + (i >> 1) * 2048, 0, AUX_SC1);
        unsigned bad = 0;
#pragma unroll
        for (int i = 0; i < 4; ++i) bad |= (p[i][1] ^ tag_hi) | (p[i][3] ^ tag_hi) | ((p[i][0] ^ tag_lo8) & 0xffu) | ((p[i][2] ^ tag_lo8) & 0xffu);
        if (__all(bad == 0u) || spins > SPIN_CAP) {
          float qv[4];
#pragma unroll
          for (int i = 0; i < 4; ++i)
            qv[i] = __uint_as_float(p[i >> 1][(i & 1) * 2] & 0xffffff00u) + __uint_as_float(p[2 + (i >> 1)][(i & 1) * 2] & 0xffffff00u);
          v2u qq; qq[0] = pk_bf16(qv[0], qv[1]); qq[1] = pk_bf16(qv[2], qv[3]);
          *(LAS v2u*)(smem + L_QS + lane * 8) = qq;
          break;
        }
        __builtin_amdgcn_s_sleep(2);
      }
      lds_inc(smem, 0, lane);
      STAMP(3);
      if (has_task) {
        const rsrc_t dr = mk_rsrc(wd, F * D * 2);
        const int dbase = (task * 32 + g) * 1152 + c * 16;
        f32x4 da[5];
#pragma unroll
        for (int cb = 0; cb < 5; ++cb) da[cb] = f32x4{0.f, 0.f, 0.f, 0.f};
#pragma unroll
        for (int hf = 0; hf < 2; ++hf) {
          v4u wv[20];
#pragma unroll
          for (int i = 0; i < 4; ++i)
#pragma unroll
            for (int cb = 0; cb < 5; ++cb)
              wv[i * 5 + cb] = __builtin_amdgcn_raw_buffer_load_b128(dr, (cb == 4 && c >= 8) ? OOB : dbase + cb * 256, (hf * 4 + i) * 4608, AUX_NT);
          __builtin_amdgcn_sched_barrier(0);
          if (hf == 0) {
            lds_wait<4>(smem, 1, 3u);
            const LAS float* gu = (const LAS float*)(smem + L_GU) + lane;
            const float gv = rbf(gu[0] + gu[128] + gu[256]), uv = rbf(gu[64] + gu[192] + gu[320]);
            const float inner = 0.7978845608028654f * (gv + 0.044715f * gv * gv * gv);
            const float ge = rbf(0.5f * gv * (1.f + tanhf(inner)));
            *(LAS float*)(smem + L_HS + lane * 4) = rbf(ge * uv);
            LDSW();
          }
#pragma unroll
          for (int i = 0; i < 4; ++i) {
            const v4u bb = diag(*(const LAS float*)(smem + L_HS + ((c >> 3) * 32 + (hf * 4 + i) * 4 + g) * 4), msk);
#pragma unroll
            for (int cb = 0; cb < 5; ++cb) da[cb] = mfma(wv[i * 5 + cb], bb, da[cb]);
          }
          __builtin_amdgcn_sched_barrier(0);
        }
#pragma unroll
        for (int cb = 0; cb < 5; ++cb)
#pragma unroll
          for (int rr = 0; rr < 4; ++rr) {
            const int col = cb * 128 + (4 * g + rr) * 8 + (c & 7);
            if (col < D) *(LAS float*)(smem + L_FP + ((c >> 3) * D + col) * 4) = da[cb][rr];
          }
        LDSW();
#pragma unroll
        for (int i = 0; i < 5; ++i) {
          const int o = (i * 64 + lane) * 16;
          if (o < 4608) {
            const v4u v = *(const LAS v4u*)(smem + L_FP + o);
            __builtin_amdgcn_raw_buffer_store_b128(v, wsr, WS_FP + task * 4608 + o, 0, AUX_SC1);
          }
        }
        DRAIN();
        if (lane == 0) {
          v2u fl; fl[0] = (unsigned)tag64; fl[1] = tag_hi;
          __builtin_amdgcn_raw_buffer_store_b64(fl, wsr, WS_FF + task * 64, 0, AUX_SC1);
        }
      }
      STAMP(4);
      if (is_final) {
        for (unsigned spins = 0;; ++spins) {
          unsigned bad = 0;
#pragma unroll
          for (int i = 0; i < 4; ++i) {
            const v2u p = __builtin_amdgcn_raw_buffer_load_b64(wsr, WS_QG + (i * 64 + lane) * 512, 0, AUX_SC1);
            bad |= (p[1] ^ tag_hi) | ((p[0] ^ tag_lo8) & 0xffu);
          }
          if (__all(bad == 0u) || spins > SPIN_CAP) break;
          __builtin_amdgcn_s_sleep(16);
        }
        if (fid == 0 && lane == 0) {
          v2u e; e[0] = (unsigned)tag64; e[1] = tag_hi;
          __builtin_amdgcn_raw_buffer_store_b64(e, wsr, WS_E, 0, AUX_SC1);
        }
        {
          float z = 0.f;
#pragma unroll
          for (int i = 0; i < 9; ++i) {
            const unsigned xv = *(const LAS unsigned*)(smem + L_XS + (i * 64 + lane) * 4);
            z += rbf((fb ? bfhi(xv) : bflo(xv)) * bflo(gw[i * 64 + lane]));
          }
          z += dpp<XOR1>(z); z += dpp<XOR2>(z); z += dpp<ROR4>(z); z += dpp<ROR8>(z);
          z = rbf(xrow_sum(z));
          if (lane == 0) *(LAS float*)(smem + L_FFN + 64) = rbf(1.f / (1.f + __expf(-z)));
        }
        for (unsigned spins = 0;; ++spins) {
          unsigned bad = 0;
#pragma unroll
          for (int i = 0; i < 3; ++i) {
            const int tk = i * 64 + lane;
            const v2u p = __builtin_amdgcn_raw_buffer_load_b64(wsr, WS_FF + (tk < 144 ? tk : 0) * 64, 0, AUX_SC1);
            bad |= (p[1] ^ tag_hi) | (p[0] ^ (unsigned)tag64);
          }
          if (__all(bad == 0u) || spins > SPIN_CAP) break;
          __builtin_amdgcn_s_sleep(64);
        }
        __builtin_amdgcn_fence(__ATOMIC_ACQUIRE, "wavefront");
        {
          const int fo = WS_FP + (lane >> 2) * 4608 + (fid * 16 + (lane & 3) * 4) * 4;
          v4u pv[9];
#pragma unroll
          for (int i = 0; i < 9; ++i) pv[i] = __builtin_amdgcn_raw_buffer_load_b128(wsr, fo, i * 16 * 4608, AUX_SC1);
          f32x4 fa = {0.f, 0.f, 0.f, 0.f};
#pragma unroll
          for (int i = 0; i < 9; ++i) fa += __builtin_bit_cast(f32x4, pv[i]);
#pragma unroll
          for (int j = 0; j < 4; ++j) { float t = fa[j]; t += dpp<ROR4>(t); t += dpp<ROR8>(t); fa[j] = xrow_sum(t); }
          if (lane < 4) *(LAS f32x4*)(smem + L_FFN + lane * 16) = fa;
        }
        lds_inc(smem, 6, lane);
      }
      STAMP(5);
    } else if (has_task) {
      const int r = lane >> 2;
      const int gbase = (hw * 192 + r) * (F * 2) + task * 64 + (lane & 3) * 16;
#pragma unroll 1
      for (int mat = 0; mat < 2; ++mat) {
        const rsrc_t gr = mk_rsrc(mat ? wu : wg, D * F * 2);
        float a0[8], a1[8];
#pragma unroll
        for (int j = 0; j < 8; ++j) { a0[j] = 0.f; a1[j] = 0.f; }
#pragma unroll 1
        for (int bt = 0; bt < 2; ++bt) {
          v4u wv[6];
#pragma unroll
          for (int i = 0; i < 6; ++i) wv[i] = __builtin_amdgcn_raw_buffer_load_b128(gr, gbase, (bt * 6 + i) * 16 * (F * 2), AUX_NT);
          __builtin_amdgcn_sched_barrier(0);
#pragma unroll
          for (int i = 0; i < 6; ++i) {
            const unsigned xv = *(const LAS unsigned*)(smem + L_XS + (hw * 192 + (bt * 6 + i) * 16 + r) * 4);
            fma8(a0, bflo(xv), wv[i]); fma8(a1, bfhi(xv), wv[i]);
          }
        }
        const f32x2 r0 = red8<true>(a0), r1 = red8<true>(a1);
        if ((lane & 12) == 0) {
          const int col = (lane & 3) * 8 + 4 * (lane >> 5) + 2 * ((lane >> 4) & 1);
          *(LAS f32x2*)(smem + L_GU + (((hw * 2 + mat) * 2 + 0) * 32 + col) * 4) = r0;
          *(LAS f32x2*)(smem + L_GU + (((hw * 2 + mat) * 2 + 1) * 32 + col) * 4) = r1;
        }
      }
      lds_inc(smem, 1, lane);
    }
#pragma unroll
    for (int i = 0; i < 8; ++i) WO[i] = __builtin_amdgcn_raw_buffer_load_b128(wor, wo_lo, i * 4608, AUX_NT);
#pragma unroll
    for (int i = 8; i < 16; ++i) WO[i] = __builtin_amdgcn_raw_buffer_load_b128(wor, wo_hi, (i - 8) * 4608, AUX_NT);
    if (TOUCH && hw == 1) {
      const unsigned char* tp = (lane & 8 ? Vc : Kc) + (size_t)grp * (SK * 256) + ((lane & 7) * 256 + 255) * 4096 + 4032;
      if (lane >= 16) tp = (const unsigned char*)out;
      const unsigned tv = __builtin_nontemporal_load((const unsigned*)tp);
      asm volatile("" :: "v"(tv));
    }
    __builtin_amdgcn_sched_barrier(0);
    DRAIN();
    lds_wait<32>(smem, 2, 1u);
    if (!H1P && hw == 3) gather_o(false);
    lds_wait<0>(smem, 4, 1u);
    const unsigned pp = *(const LAS unsigned*)(smem + L_OS + lane * 4);
    STAMP(9);
    {
      f32x4 y0 = {0.f, 0.f, 0.f, 0.f}, y1 = {0.f, 0.f, 0.f, 0.f}, z0 = {0.f, 0.f, 0.f, 0.f};
#define PRJ(i, y) { const unsigned t_ = dppu<0x150 + (i)>(pp); v4u bb; bb[0] = t_ & msk[0]; bb[1] = t_ & msk[1]; bb[2] = t_ & msk[2]; bb[3] = t_ & msk[3]; \
                    y = mfma(WO[i], bb, y); z0 = mfma(WO[8 + (i)], bb, z0); }
      PRJ(0, y0) PRJ(1, y1) PRJ(2, y0) PRJ(3, y1) PRJ(4, y0) PRJ(5, y1) PRJ(6, y0) PRJ(7, y1)
#undef PRJ
      y0 += y1;
      const int dst = WS_H2 + ((b * 36 + hw * 8 + 2 * g) * 128 + (bid & 127)) * 64 + c * 4;
      __builtin_amdgcn_raw_buffer_store_b32(__float_as_uint(hi8 ? y0[1] : y0[0]) | 1u, wsr, dst, 0, AUX_SC1);
      __builtin_amdgcn_raw_buffer_store_b32(__float_as_uint(hi8 ? y0[3] : y0[2]) | 1u, wsr, dst, 8192, AUX_SC1);
      if (hw == 0) {
        const int dz = g < 2 ? dst + 32 * 8192 : OOB;
        __builtin_amdgcn_raw_buffer_store_b32(__float_as_uint(hi8 ? z0[1] : z0[0]) | 1u, wsr, dz, 0, AUX_SC1);
        __builtin_amdgcn_raw_buffer_store_b32(__float_as_uint(hi8 ? z0[3] : z0[2]) | 1u, wsr, dz, 8192, AUX_SC1);
      }
    }
    STAMP(15);
  }
  STAMP(11);
  STAMP_FLUSH();
}

extern "C" void kernel_launch(void* const* d_in, const int* in_sizes, int n_in, void* d_out, int out_size, void* d_ws, size_t ws_size,
                              hipStream_t stream) {
  k_fused<<<256, NTHR, 0, stream>>>((const unsigned short*)d_in[0], (const unsigned char*)d_in[1], (const unsigned char*)d_in[2],
                                    (const unsigned char*)d_in[3], (const unsigned char*)d_in[4], (const unsigned char*)d_in[6],
                                    (const unsigned char*)d_in[7], (const unsigned char*)d_in[8], (const unsigned short*)d_in[9],
                                    (unsigned char*)d_ws, (unsigned*)d_out);
}
```

```cpp
#include <hip/hip_runtime.h>
#include <hip/hip_bf16.h>
#include <stdint.h>

typedef unsigned v4u __attribute__((ext_vector_type(4)));
typedef unsigned v2u __attribute__((ext_vector_type(2)));
typedef float f32x4 __attribute__((ext_vector_type(4)));
typedef float f32x2 __attribute__((ext_vector_type(2)));
typedef short bf16x8 __attribute__((ext_vector_type(8)));
typedef __bf16 bf16x2 __attribute__((ext_vector_type(2)));
typedef __amdgpu_buffer_rsrc_t rsrc_t;
#define LAS __attribute__((address_space(3)))

constexpr int D = 576, SK = 32768, F = 4608;
constexpr int NW = 8, NTHR = NW * 64;
#define NSU_V 108
constexpr unsigned NSU = NSU_V;
constexpr unsigned ND = 8 * (128 - NSU);
constexpr unsigned NONE = 1024;
static_assert(NSU % 4 == 0 && (NSU / 4) % 2 == 1, "static V steps per wave: odd");
constexpr int AUX_NT = 2, AUX_SC1 = 16;
constexpr int OOB = 0x40000000;
constexpr unsigned SPIN_CAP = 200000u;
#define H1P 1
#define NE 2
#define STAG 4
#define H1MASK 1
#define TOUCH 0
#define LATE0 0
#define NEARD 64
#define PSLEEP 2

constexpr int WS_E = 0;
constexpr int WS_CNT = 4096;
constexpr int WS_QG = 16384;
constexpr int WS_H1 = WS_QG + 131072;
constexpr int H1_STRIDE = 2176;
constexpr int WS_H2 = WS_H1 + 256 * H1_STRIDE;
constexpr int WS_FF = WS_H2 + 144 * 4096;
constexpr int WS_FP = WS_FF + 144 * 64;
constexpr int WS_SF = WS_FP + 144 * 4608;
constexpr int WS_SG = WS_SF + 256 * 64;
constexpr int WS_END = WS_SG + 32 * 1024 * 128;

constexpr int L_XS = 0;
constexpr int L_QP = 2304;
constexpr int L_QS = L_QP + 2048;
constexpr int L_MRG = L_QS + 512;
constexpr int L_ML = L_MRG + 8192;
constexpr int L_OS = L_ML + 96;
constexpr int L_GU = L_OS + 256;
constexpr int L_HS = L_GU + 1536;
constexpr int L_FP = L_HS + 256;
constexpr int L_FFN = L_FP + 4608;
constexpr int L_FB = L_FFN + 80;
constexpr int L_SYNC = L_FB + 256;
constexpr int L_SC = L_SYNC + 48;
constexpr int L_END = L_SC + 16384;
constexpr int LDS_BYTES = 96 * 1024;
static_assert(L_END <= LDS_BYTES, "lds");

__device__ __forceinline__ float bflo(unsigned u) { return __builtin_bit_cast(float, u << 16); }
__device__ __forceinline__ float bfhi(unsigned u) { return __builtin_bit_cast(float, u & 0xffff0000u); }
__device__ __forceinline__ unsigned pk_bf16(float lo, float hi) {
  bf16x2 v; v[0] = (__bf16)lo; v[1] = (__bf16)hi; return __builtin_bit_cast(unsigned, v);
}
__device__ __forceinline__ float rbf(float f) { return bflo(pk_bf16(f, f)); }
__device__ __forceinline__ float dot2(unsigned a, unsigned b, float c) {
  return __builtin_amdgcn_fdot2_f32_bf16(__builtin_bit_cast(bf16x2, a), __builtin_bit_cast(bf16x2, b), c, false);
}
template <int CTRL> __device__ __forceinline__ float dpp(float x) {
  return __builtin_bit_cast(float, __builtin_amdgcn_mov_dpp(__builtin_bit_cast(int, x), CTRL, 0xf, 0xf, true));
}
constexpr int XOR1 = 0xB1, XOR2 = 0x4E, HMIR = 0x141, ROR4 = 0x124, ROR8 = 0x128;
__device__ __forceinline__ f32x4 mfma(v4u a, v4u b, f32x4 c) {
  return __builtin_amdgcn_mfma_f32_16x16x32_bf16(__builtin_bit_cast(bf16x8, a), __builtin_bit_cast(bf16x8, b), c, 0, 0, 0);
}
__device__ __forceinline__ float swap16_sum(float a, float b) {
  auto s = __builtin_amdgcn_permlane16_swap(__float_as_uint(a), __float_as_uint(b), false, false);
  return __uint_as_float(s[0]) + __uint_as_float(s[1]);
}
__device__ __forceinline__ float swap32_sum(float a, float b) {
  auto s = __builtin_amdgcn_permlane32_swap(__float_as_uint(a), __float_as_uint(b), false, false);
  return __uint_as_float(s[0]) + __uint_as_float(s[1]);
}
__device__ __forceinline__ float xrow_max(float x) {
  auto s = __builtin_amdgcn_permlane16_swap(__float_as_uint(x), __float_as_uint(x), false, false);
  x = fmaxf(__uint_as_float(s[0]), __uint_as_float(s[1]));
  auto t = __builtin_amdgcn_permlane32_swap(__float_as_uint(x), __float_as_uint(x), false, false);
  return fmaxf(__uint_as_float(t[0]), __uint_as_float(t[1]));
}
__device__ __forceinline__ float xrow_sum(float x) { x = swap16_sum(x, x); return swap32_sum(x, x); }
__device__ __forceinline__ rsrc_t mk_rsrc(const void* p, int bytes) { return __builtin_amdgcn_make_buffer_rsrc((void*)p, 0, bytes, 0x00020000); }
#define BAR() do { asm volatile("s_waitcnt lgkmcnt(0)" ::: "memory"); __builtin_amdgcn_s_barrier(); asm volatile("" ::: "memory"); } while (0)
#define DRAIN() do { __builtin_amdgcn_s_waitcnt(0x0F70); asm volatile("s_waitcnt vmcnt(0)" ::: "memory"); } while (0)
#define LDSW() asm volatile("s_waitcnt lgkmcnt(0)" ::: "memory")

#define STAMP(i) do {} while (0)
#define STAMP_FLUSH() do {} while (0)
constexpr float SCALE = 0.08838834764831845f, L2E = 1.4426950408889634f;

typedef double f64x4 __attribute__((ext_vector_type(4)));
struct St { float m; double l; f64x4 acc; };
__device__ __forceinline__ void pin(St& st) { asm volatile("" : "+v"(st.m), "+v"(st.l), "+v"(st.acc) :: "memory"); }
__device__ __forceinline__ v4u diag(float v, const v4u& msk) {
  const unsigned pp = pk_bf16(v, v);
  v4u bb; bb[0] = pp & msk[0]; bb[1] = pp & msk[1]; bb[2] = pp & msk[2]; bb[3] = pp & msk[3];
  return bb;
}

__device__ __forceinline__ v2u scores(const v4u* KB, const v4u& q0, const v4u& q1, bool hi8) {
  float s[4];
#pragma unroll
  for (int t = 0; t < 4; ++t) {
    float a0 = dot2(KB[t][0], q0[0], 0.f), a1 = dot2(KB[t][0], q1[0], 0.f);
#pragma unroll
    for (int k = 1; k < 4; ++k) { a0 = dot2(KB[t][k], q0[k], a0); a1 = dot2(KB[t][k], q1[k], a1); }
    const float keep = hi8 ? a1 : a0, send = hi8 ? a0 : a1;
    float v = keep + dpp<ROR8>(send);
    v += dpp<HMIR>(v); v += dpp<XOR2>(v); v += dpp<XOR1>(v);
    s[t] = v;
  }
  const unsigned u0 = pk_bf16(s[0], s[1]), u1 = pk_bf16(s[2], s[3]);
  v2u r; r[0] = pk_bf16(bflo(u0) * SCALE, bfhi(u0) * SCALE); r[1] = pk_bf16(bflo(u1) * SCALE, bfhi(u1) * SCALE);
  return r;
}
__device__ __forceinline__ void pv_unit(const v4u (&VB)[8], const v4u sc, const v4u& msk, St& st) {
  float s[8];
#pragma unroll
  for (int t = 0; t < 4; ++t) { s[2 * t] = bflo(sc[t]); s[2 * t + 1] = bfhi(sc[t]); }
  float tm = fmaxf(fmaxf(fmaxf(s[0], s[1]), fmaxf(s[2], s[3])), fmaxf(fmaxf(s[4], s[5]), fmaxf(s[6], s[7])));
  tm = xrow_max(tm);
  const float tl = tm * L2E, mt = __builtin_ceilf(tl), fr = tl - mt;
  const float mn = fmaxf(st.m, mt);
  const double alpha = (double)__builtin_amdgcn_exp2f(st.m - mn);
  const float beta = __builtin_amdgcn_exp2f(mt - mn);
  st.m = mn;
  float p[8];
#pragma unroll
  for (int t = 0; t < 8; ++t) p[t] = __builtin_amdgcn_exp2f(fmaf(s[t] - tm, L2E, fr));
  f32x4 ta = {0.f, 0.f, 0.f, 0.f};
#pragma unroll
  for (int t = 0; t < 8; ++t) ta = mfma(VB[t], diag(p[t], msk), ta);
  st.l = __builtin_fma(st.l, alpha, (double)((((p[0] + p[1]) + (p[2] + p[3])) + ((p[4] + p[5]) + (p[6] + p[7]))) * beta));
#pragma unroll
  for (int r = 0; r < 4; ++r) st.acc[r] = __builtin_fma(st.acc[r], alpha, (double)(ta[r] * beta));
}
__device__ __forceinline__ void issue(v4u (&B)[8], rsrc_t r, int voff, unsigned unit) {
  const int so = (int)(unit * 8192u);
#pragma unroll
  for (int t = 0; t < 4; ++t) B[t] = __builtin_amdgcn_raw_buffer_load_b128(r, voff + t * 1024, so, AUX_NT);
#pragma unroll
  for (int t = 0; t < 4; ++t) B[4 + t] = __builtin_amdgcn_raw_buffer_load_b128(r, voff + t * 1024, so + 4096, AUX_NT);
}
__device__ __forceinline__ unsigned atomic_inc(rsrc_t r, int voff0, bool on, int so) {
  return (unsigned)__builtin_amdgcn_raw_ptr_buffer_atomic_add_i32(1, r, voff0, on ? so : OOB, 0);
}
__device__ __forceinline__ unsigned lds_ticket(unsigned lds_addr_v, unsigned vone) {
  unsigned long long keep; unsigned r, s;
  asm volatile("s_mov_b64 %1, exec\n\ts_mov_b64 exec, 1\n\tds_add_rtn_u32 %0, %3, %4\n\ts_mov_b64 exec, %1\n\ts_waitcnt lgkmcnt(0)\n\tv_readfirstlane_b32 %2, %0\n\ts_nop 1"
               : "=&v"(r), "=&s"(keep), "=s"(s) : "v"(lds_addr_v), "v"(vone) : "memory");
  return s;
}
template <int CTRL> __device__ __forceinline__ unsigned dppu(unsigned x) { return (unsigned)__builtin_amdgcn_mov_dpp((int)x, CTRL, 0xf, 0xf, true); }
template <int CTRL> __device__ __forceinline__ double dppd(double x) {
  const v2u u = __builtin_bit_cast(v2u, x);
  v2u r; r[0] = dppu<CTRL>(u[0]); r[1] = dppu<CTRL>(u[1]);
  return __builtin_bit_cast(double, r);
}
__device__ __forceinline__ double xrow_sumd(double x) {
  v2u u = __builtin_bit_cast(v2u, x), a, b2;
  { auto s0 = __builtin_amdgcn_permlane16_swap(u[0], u[0], false, false); auto s1 = __builtin_amdgcn_permlane16_swap(u[1], u[1], false, false);
    a[0] = s0[0]; a[1] = s1[0]; b2[0] = s0[1]; b2[1] = s1[1]; }
  x = __builtin_bit_cast(double, a) + __builtin_bit_cast(double, b2);
  u = __builtin_bit_cast(v2u, x);
  { auto s0 = __builtin_amdgcn_permlane32_swap(u[0], u[0], false, false); auto s1 = __builtin_amdgcn_permlane32_swap(u[1], u[1], false, false);
    a[0] = s0[0]; a[1] = s1[0]; b2[0] = s0[1]; b2[1] = s1[1]; }
  return __builtin_bit_cast(double, a) + __builtin_bit_cast(double, b2);
}
__device__ __forceinline__ void lds_inc(unsigned char* smem, int idx, int lane) {
  LDSW();
  if (lane == 0) __hip_atomic_fetch_add((LAS unsigned*)(LAS unsigned char*)smem + (L_SYNC / 4 + idx), 1u, __ATOMIC_RELAXED, __HIP_MEMORY_SCOPE_WORKGROUP);
}
template <int SLEEP> __device__ __forceinline__ void lds_wait(unsigned char* smem, int idx, unsigned want) {
#pragma unroll 1
  for (unsigned spins = 0; spins < 8000000u / (1 + SLEEP); ++spins) {
    const unsigned v = __builtin_amdgcn_readfirstlane(*((volatile LAS unsigned*)(LAS unsigned char*)smem + (L_SYNC / 4 + idx)));
    if (v >= want) break;
    if (SLEEP) __builtin_amdgcn_s_sleep(SLEEP);
  }
  asm volatile("" ::: "memory");
}

__device__ __forceinline__ void fma8(float (&acc)[8], float xv, const v4u& w) {
#pragma unroll
  for (int k = 0; k < 4; ++k) { acc[2 * k] = fmaf(xv, bflo(w[k]), acc[2 * k]); acc[2 * k + 1] = fmaf(xv, bfhi(w[k]), acc[2 * k + 1]); }
}
template <bool BIT2> __device__ __forceinline__ f32x2 red8(const float (&acc)[8]) {
  float a[4];
#pragma unroll
  for (int j = 0; j < 4; ++j) a[j] = swap32_sum(acc[j], acc[j + 4]);
  float b0 = swap16_sum(a[0], a[2]), b1 = swap16_sum(a[1], a[3]);
  if (BIT2) { b0 += dpp<ROR4>(b0); b1 += dpp<ROR4>(b1); }
  b0 += dpp<ROR8>(b0); b1 += dpp<ROR8>(b1);
  f32x2 r; r[0] = b0; r[1] = b1; return r;
}
__device__ __forceinline__ float val(unsigned u) { return __uint_as_float(u & ~1u); }
__device__ __forceinline__ bool all4(const v4u& p) { return (p[0] & p[1] & p[2] & p[3] & 1u) != 0u; }

__global__ void __launch_bounds__(NTHR, 2)
k_fused(const unsigned short* __restrict__ x, const unsigned char* Kc, const unsigned char* Vc,
        const unsigned char* __restrict__ wq, const unsigned char* wo, const unsigned char* wg,
        const unsigned char* wu, const unsigned char* wd, const unsigned short* __restrict__ gw,
        unsigned char* ws, unsigned* out) {
  __shared__ __attribute__((aligned(16))) unsigned char smem[LDS_BYTES];
  const int tid = threadIdx.x, lane = tid & 63, w = __builtin_amdgcn_readfirstlane(tid >> 6);
  const int bid = blockIdx.x, grp = bid >> 3, sp = bid & 7, b = grp >> 4, kvh = grp & 15;
  const int g = lane >> 4, c = lane & 15;
  unsigned tsv = 0xffffffffu; (void)tsv;
  STAMP(0);
  const int voff = lane * 16;
  const int ridx = (grp + 4 * sp) & 31;
  const bool has_task = ridx < 18;
  const int task = sp * 18 + ridx;
  const bool is_final = ridx >= 18 && ridx < 27;
  const int fid = sp * 9 + ridx - 18;
  const bool hi8 = (c & 8) != 0;
#define MAKE_MSK() v4u msk; _Pragma("unroll") for (int d = 0; d < 4; ++d) msk[d] = (((c & 7) >> 1) == d) ? ((c & 1) ? 0xffff0000u : 0x0000ffffu) : 0u;

  auto gather_o = [&](bool watch_flag) {
    const rsrc_t wsr = mk_rsrc(ws, WS_END);
    const int o_acc = WS_H1 + (grp * 8 + 4 * (c >> 3)) * H1_STRIDE + (32 * sp + 8 * g + (c & 7)) * 8;
    const int o_ml = WS_H1 + (grp * 8 + (c & 7)) * H1_STRIDE + 2048 + (sp >> 2) * 16;
    v2u pv[4];
    v4u pm = {0u, 0u, 0u, 0u};
#pragma unroll
    for (int i = 0; i < 4; ++i) pv[i] = v2u{0u, 0u};
    for (unsigned spins = 0;; ++spins) {
      if (H1MASK) {
        if (!(pv[0][0] & pv[1][0] & pv[2][0] & pv[3][0] & 1u)) {
#pragma unroll
          for (int i = 0; i < 4; ++i) pv[i] = __builtin_amdgcn_raw_buffer_load_b64(wsr, o_acc, i * H1_STRIDE, AUX_SC1);
        }
        if (!(pm[0] & pm[2] & 1u)) pm = __builtin_amdgcn_raw_buffer_load_b128(wsr, o_ml, 0, AUX_SC1);
      } else {
#pragma unroll
        for (int i = 0; i < 4; ++i) pv[i] = __builtin_amdgcn_raw_buffer_load_b64(wsr, o_acc, i * H1_STRIDE, AUX_SC1);
        pm = __builtin_amdgcn_raw_buffer_load_b128(wsr, o_ml, 0, AUX_SC1);
      }
      if (__all((pv[0][0] & pv[1][0] & pv[2][0] & pv[3][0] & pm[0] & pm[2] & 1u) != 0u) || spins > SPIN_CAP) break;
      if (watch_flag && __builtin_amdgcn_readfirstlane(*((volatile LAS unsigned*)(smem + L_SYNC) + 4)) != 0u) return;
      __builtin_amdgcn_s_sleep(PSLEEP);
    }
    STAMP(8);
    const float Ms = __uint_as_float(pm[1]);
    float Mx = fmaxf(Ms, dpp<XOR1>(Ms)); Mx = fmaxf(Mx, dpp<XOR2>(Mx)); Mx = fmaxf(Mx, dpp<HMIR>(Mx));
    const float wt = __builtin_amdgcn_exp2f(Ms - Mx);
    v2u lv; lv[0] = pm[2]; lv[1] = pm[3];
    double den = (double)wt * __builtin_bit_cast(double, lv);
    den += dppd<XOR1>(den); den += dppd<XOR2>(den); den += dppd<HMIR>(den);
    double num = 0.0;
#define ACC1(i) { const float wl_ = dpp<0x150 + (i)>(wt), wh_ = dpp<0x154 + (i)>(wt); num = __builtin_fma((double)(hi8 ? wh_ : wl_), __builtin_bit_cast(double, pv[i]), num); }
    ACC1(0) ACC1(1) ACC1(2) ACC1(3)
#undef ACC1
    num += dppd<ROR8>(num);
    const float ov = (float)num / (float)den;
    *(LAS unsigned*)(smem + L_OS + lane * 4) = pk_bf16(ov, ov);
    lds_inc(smem, 4, lane);
  };

  if (w >= 4) {
    const int sw = w - 4, stid = tid - 256;
    const rsrc_t kr = mk_rsrc(Kc + (size_t)grp * (SK * 256), SK * 256);
    const rsrc_t vr = mk_rsrc(Vc + (size_t)grp * (SK * 256), SK * 256);
    const rsrc_t wsr = mk_rsrc(ws, WS_END);
    const unsigned ubase = (unsigned)(sp * 128 + sw);
    v4u Ba[8], Bb[8], Be[NE][8];
    if (!LATE0) issue(Be[0], kr, voff, ubase);
    __builtin_amdgcn_sched_barrier(0);
    asm volatile("" ::: "memory");
    __builtin_amdgcn_s_barrier();
    asm volatile("" ::: "memory");
    if (LATE0) issue(Be[0], kr, voff, ubase);
    __builtin_amdgcn_sched_barrier(0);
    STAMP(1);
    MAKE_MSK();
#pragma unroll
    for (int e = 1; e < NE; ++e) {
      DRAIN();
      issue(Be[e], kr, voff, ubase + 4 * e);
      __builtin_amdgcn_sched_barrier(0);
    }
    DRAIN();
    issue(Ba, kr, voff, ubase + 4 * NE);
    __builtin_amdgcn_sched_barrier(0);
    DRAIN();
    issue(Bb, kr, voff, ubase + 4 * NE + 4);
    __builtin_amdgcn_sched_barrier(0);
    STAMP(2);
    lds_wait<1>(smem, 0, 1u);
    STAMP(3);
    const v4u q0 = *(const LAS v4u*)(smem + L_QS + c * 16), q1 = *(const LAS v4u*)(smem + L_QS + 256 + c * 16);
    const int sc_off = ((c >> 3) * 4 + g) * 16;
    const int sg_st = (c & 7) == 0 ? WS_SG + grp * 131072 + sc_off : OOB;
#define KSC(B) const v2u s0_ = scores(&B[0], q0, q1, hi8), s1_ = scores(&B[4], q0, q1, hi8);                        \
      v4u sc_; sc_[0] = s0_[0]; sc_[1] = s0_[1]; sc_[2] = s1_[0]; sc_[3] = s1_[1];
#define KSTEP(B, i)                                                                                                \
    {                                                                                                              \
      KSC(B)                                                                                                       \
      if ((c & 7) == 0) *(LAS v4u*)(smem + L_SC + (4 * (i) + sw) * 128 + sc_off) = sc_;                            \
      asm volatile("" ::: "memory");                                                                               \
      __builtin_amdgcn_sched_barrier(0);                                                                           \
    }
#define KSTEPG(B, u)                                                                                               \
    {                                                                                                              \
      KSC(B)                                                                                                       \
      __builtin_amdgcn_raw_buffer_store_b128(sc_, wsr, sg_st, (int)((u) * 128u), AUX_SC1);                         \
      asm volatile("" ::: "memory");                                                                               \
      __builtin_amdgcn_sched_barrier(0);                                                                           \
    }
    const int cnt0 = lane == 0 ? WS_CNT + grp * 256 : OOB;
    unsigned vone = 1u, a = 0u;
    unsigned taddr = (unsigned)(size_t)(LAS unsigned char*)(smem + L_SYNC + 28);
    asm volatile("" : "+v"(vone), "+v"(taddr));
    KSTEP(Ba, NE);
    issue(Ba, kr, voff, ubase + 4 * (NE + 2));
    __builtin_amdgcn_sched_barrier(0);
#pragma unroll
    for (int e = 0; e < NE; ++e) KSTEP(Be[e], e);
    KSTEP(Bb, NE + 1);
    static_assert(NE % 2 == 0, "K steps left: odd");
#pragma unroll 1
    for (int i = NE + 2; i < (int)(NSU / 4) - 1; i += 2) {
      DRAIN();
      issue(Bb, kr, voff, ubase + 4 * (i + 1));
      __builtin_amdgcn_sched_barrier(0);
      KSTEP(Ba, i);
      DRAIN();
      a = atomic_inc(wsr, cnt0, i + 3 >= (int)(NSU / 4), 0);
      issue(Ba, kr, voff, ubase + 4 * (i + 2));
      __builtin_amdgcn_sched_barrier(0);
      KSTEP(Bb, i + 1);
    }
#define MAPD(d) (((d) & 7u) * 128u + NSU + ((d) >> 3))
#define WAITA() DRAIN()
    {
      unsigned ua, ub;
      WAITA();
      unsigned d = __builtin_amdgcn_readfirstlane(a);
      a = atomic_inc(wsr, cnt0, d < ND, 0);
      ub = MAPD(d);
      issue(Bb, d < ND ? kr : vr, voff, d < ND ? ub : ubase);
      __builtin_amdgcn_sched_barrier(0);
      KSTEP(Ba, NSU / 4 - 1);
      bool in_b = true;
      while (d < ND) {
        WAITA();
        d = __builtin_amdgcn_readfirstlane(a);
        a = atomic_inc(wsr, cnt0, d < ND, 0);
        ua = MAPD(d);
        issue(Ba, d < ND ? kr : vr, voff, d < ND ? ua : ubase);
        __builtin_amdgcn_sched_barrier(0);
        KSTEPG(Bb, ub);
        if (d >= ND) { in_b = false; break; }
        WAITA();
        d = __builtin_amdgcn_readfirstlane(a);
        a = atomic_inc(wsr, cnt0, d < ND, 0);
        ub = MAPD(d);
        issue(Bb, d < ND ? kr : vr, voff, d < ND ? ub : ubase);
        __builtin_amdgcn_sched_barrier(0);
        KSTEPG(Ba, ua);
      }
      STAMP(4);
      DRAIN();
      if (in_b) {
#pragma unroll
        for (int t = 0; t < 8; ++t) Ba[t] = Bb[t];
      }
    }
#undef KSTEP
#undef KSTEPG
#undef KSC
    St st; st.m = -INFINITY; st.l = 0.0; st.acc = f64x4{0.0, 0.0, 0.0, 0.0};
#define LSC(i) (*(const LAS v4u*)(smem + L_SC + (4 * (i) + sw) * 128 + sc_off))
    lds_inc(smem, 7, lane);
#pragma unroll 1
    for (int i = 0; i < (int)(NSU / 4) - 1; i += 2) {
      DRAIN();
      issue(Bb, vr, voff, ubase + 4 * (i + 1));
      __builtin_amdgcn_sched_barrier(0);
      pv_unit(Ba, LSC(i), msk, st); pin(st);
      __builtin_amdgcn_sched_barrier(0);
      DRAIN();
      a = atomic_inc(wsr, cnt0, i + 3 >= (int)(NSU / 4), 4);
      issue(Ba, vr, voff, ubase + 4 * (i + 2));
      __builtin_amdgcn_sched_barrier(0);
      pv_unit(Bb, LSC(i + 1), msk, st); pin(st);
      __builtin_amdgcn_sched_barrier(0);
    }
    lds_wait<2>(smem, 9, 1u);
    v4u sa = LSC(NSU / 4 - 1), sb;
#undef LSC
    const int sg_ld = WS_SG + grp * 131072 + sc_off;
#define NEXT(nx)                                                                                                   \
    {                                                                                                              \
      const unsigned d_ = __builtin_amdgcn_readfirstlane(a);                                                       \
      nx = d_ < ND ? (d_ & 7u) * 128u + NSU + (d_ >> 3) : NONE;                                                    \
      if (d_ + NEARD >= ND) *((volatile LAS unsigned*)(smem + L_SYNC) + 2) = 1u;                                   \
      a = atomic_inc(wsr, cnt0, d_ < ND, 4);                                                               \
    }
    while (true) {
      unsigned nx;
      DRAIN();
      NEXT(nx);
      sb = __builtin_amdgcn_raw_buffer_load_b128(wsr, sg_ld, nx < NONE ? (int)(nx * 128u) : OOB, AUX_SC1);
      issue(Bb, vr, voff, nx);
      __builtin_amdgcn_sched_barrier(0);
      pv_unit(Ba, sa, msk, st); pin(st);
      __builtin_amdgcn_sched_barrier(0);
      if (nx >= NONE) break;
      DRAIN();
      NEXT(nx);
      sa = __builtin_amdgcn_raw_buffer_load_b128(wsr, sg_ld, nx < NONE ? (int)(nx * 128u) : OOB, AUX_SC1);
      issue(Ba, vr, voff, nx);
      __builtin_amdgcn_sched_barrier(0);
      pv_unit(Bb, sb, msk, st); pin(st);
      __builtin_amdgcn_sched_barrier(0);
      if (nx >= NONE) break;
    }
#undef NEXT
    STAMP(6);
    {
      const double l = xrow_sumd(st.l);
      const int h = c >> 3;
#pragma unroll
      for (int r = 0; r < 4; ++r) {
        const int dim = 8 * (4 * g + r) + (c & 7);
        *(LAS double*)(smem + L_MRG + (sw * 256 + h * 128 + (dim & 96) + (dim & 3) * 8 + ((dim >> 2) & 7)) * 8) = st.acc[r];
      }
      if (g == 0 && (c & 7) == 0) { *(LAS float*)(smem + L_ML + (h * 4 + sw) * 4) = st.m; *(LAS double*)(smem + L_ML + 32 + (h * 4 + sw) * 8) = l; }
    }
    lds_inc(smem, 3, lane);
    lds_wait<0>(smem, 3, 4u);
    STAMP(7);
    {
      const int hh = sw >> 1;
      const f32x4 m4 = *(const LAS f32x4*)(smem + L_ML + hh * 16);
      const f64x4 l4 = *(const LAS f64x4*)(smem + L_ML + 32 + hh * 32);
      double a4[4];
#pragma unroll
      for (int ww = 0; ww < 4; ++ww) a4[ww] = *(const LAS double*)(smem + L_MRG + (ww * 256 + stid) * 8);
      const float M = fmaxf(fmaxf(m4[0], m4[1]), fmaxf(m4[2], m4[3]));
      double L = 0.0, o = 0.0;
#pragma unroll
      for (int ww = 0; ww < 4; ++ww) {
        const double wt = (double)__builtin_amdgcn_exp2f(m4[ww] - M);
        L = __builtin_fma(wt, l4[ww], L); o = __builtin_fma(wt, a4[ww], o);
      }
      v2u ov = __builtin_bit_cast(v2u, o); ov[0] |= 1u;
      __builtin_amdgcn_raw_buffer_store_b64(ov, wsr, WS_H1 + bid * H1_STRIDE + stid * 8, 0, AUX_SC1);
      if ((stid & 127) == 0) {
        const v2u lv = __builtin_bit_cast(v2u, L);
        v4u ml; ml[0] = 1u; ml[1] = __float_as_uint(M); ml[2] = lv[0] | 1u; ml[3] = lv[1];
        __builtin_amdgcn_raw_buffer_store_b128(ml, wsr, WS_H1 + bid * H1_STRIDE + 2048 + hh * 16, 0, AUX_SC1);
      }
    }
    STAMP(14);
    if (H1P) {
      for (int i = 0; i < sw; ++i) __builtin_amdgcn_s_sleep(STAG);
      gather_o(true);
    }
    if (is_final) {
      const int src = WS_H2 + fid * 8192 + sw * 2048 + voff;
      lds_wait<8>(smem, 6, 1u);
      v4u p0, p1;
      for (unsigned spins = 0;; ++spins) {
        p0 = __builtin_amdgcn_raw_buffer_load_b128(wsr, src, 0, AUX_SC1);
        p1 = __builtin_amdgcn_raw_buffer_load_b128(wsr, src, 1024, AUX_SC1);
        const unsigned m = (p0[0] & p0[1] & p0[2] & p0[3]) & (p1[0] & p1[1] & p1[2] & p1[3]) & 1u;
        if (__all(m != 0u) || spins > SPIN_CAP) break;
        __builtin_amdgcn_s_sleep(PSLEEP);
      }
      STAMP(10);
      f32x4 t4;
#pragma unroll
      for (int j = 0; j < 4; ++j) {
        float t = val(p0[j]) + val(p1[j]);
        t += dpp<ROR4>(t); t += dpp<ROR8>(t);
        t4[j] = xrow_sum(t);
      }
      if (lane < 4) *(LAS f32x4*)(smem + L_FB + (sw * 4 + lane) * 16) = t4;
      LDSW();
      if (lds_ticket(taddr + 4, vone) == 3u && lane < 8) {
        const float gt = *(const LAS float*)(smem + L_FFN + 64), og = rbf(1.f - gt);
        const f32x2 fv = *(const LAS f32x2*)(smem + L_FFN + lane * 8);
        f32x2 t = *(const LAS f32x2*)(smem + L_FB + lane * 8);
#pragma unroll
        for (int ww = 1; ww < 4; ++ww) t += *(const LAS f32x2*)(smem + L_FB + ww * 64 + lane * 8);
        out[fid * 8 + lane] = pk_bf16(rbf(gt * rbf(t[0])) + rbf(og * rbf(fv[0])), rbf(gt * rbf(t[1])) + rbf(og * rbf(fv[1])));
      }
    }
  } else {
    const int hw = w;
    unsigned short xq[9];
    v4u wqv[9];
    {
      const int r = lane >> 3, row0 = 288 * (sp & 1) + 72 * hw;
      const rsrc_t qr = mk_rsrc(wq, D * 8192);
      const int base = (row0 + r) * 8192 + (kvh * 256 + (sp >> 1) * 64) * 2 + (lane & 7) * 16;
#pragma unroll
      for (int i = 0; i < 9; ++i) wqv[i] = __builtin_amdgcn_raw_buffer_load_b128(qr, base, i * 8 * 8192, AUX_NT);
#pragma unroll
      for (int i = 0; i < 9; ++i) xq[i] = x[b * D + row0 + 8 * i + r];
    }
    const int xi = tid < 144 ? tid : 143;
    const v2u xa = ((const v2u*)x)[xi], xb = ((const v2u*)x)[144 + xi];
    if (tid < 3) { const v4u z = {0u, 0u, 0u, 0u}; *(LAS v4u*)(smem + L_SYNC + tid * 16) = z; }
    __builtin_amdgcn_sched_barrier(0);
    BAR();
    STAMP(1);
    const rsrc_t wsr = mk_rsrc(ws, WS_END);
    v2u evv = {0u, 0u};
    if (hw == 3) {
      evv = __builtin_amdgcn_raw_buffer_load_b64(wsr, WS_E, 0, AUX_SC1);
      const v4u z = {0u, 0u, 0u, 0u};
      __builtin_amdgcn_raw_buffer_store_b128(z, wsr, WS_H1 + bid * H1_STRIDE + voff, 0, AUX_SC1);
      __builtin_amdgcn_raw_buffer_store_b128(z, wsr, WS_H1 + bid * H1_STRIDE + 1024 + voff, 0, AUX_SC1);
      const int h2o = WS_H2 + (b * 36 * 128 + (bid & 127)) * 64 + (lane >> 2) * 8192 + (lane & 3) * 16;
      __builtin_amdgcn_raw_buffer_store_b128(z, wsr, h2o, 0, AUX_SC1);
      __builtin_amdgcn_raw_buffer_store_b128(z, wsr, h2o, 16 * 8192, AUX_SC1);
      int off = h2o + 32 * 8192;
      if (lane >= 16) off = WS_H1 + bid * H1_STRIDE + 2048 + (lane & 1) * 16;
      if (lane >= 32) off = sp == 0 ? WS_CNT + grp * 256 : OOB;
      __builtin_amdgcn_raw_buffer_store_b128(z, wsr, off, 0, AUX_SC1);
    }
    __builtin_amdgcn_sched_barrier(0);
    {
      float acc[8];
#pragma unroll
      for (int j = 0; j < 8; ++j) acc[j] = 0.f;
#pragma unroll
      for (int i = 0; i < 9; ++i) fma8(acc, bflo(xq[i]), wqv[i]);
      const f32x2 r2 = red8<false>(acc);
      if ((lane & 8) == 0) *(LAS f32x2*)(smem + L_QP + (hw * 64 + (lane & 7) * 8 + 4 * (lane >> 5) + 2 * ((lane >> 4) & 1)) * 4) = r2;
      if (tid < 144) {
        v4u xs4;
        xs4[0] = (xa[0] & 0xffffu) | (xb[0] << 16); xs4[1] = (xa[0] >> 16) | (xb[0] & 0xffff0000u);
        xs4[2] = (xa[1] & 0xffffu) | (xb[1] << 16); xs4[3] = (xa[1] >> 16) | (xb[1] & 0xffff0000u);
        *(LAS v4u*)(smem + L_XS + tid * 16) = xs4;
      }
      lds_inc(smem, 5, lane);
      lds_wait<0>(smem, 5, 4u);
    }
    STAMP(12);
    MAKE_MSK();
    v4u WO[16];
    const rsrc_t wor = mk_rsrc(wo, 4096 * D * 2);
    const int wo_lo = (kvh * 256 + 32 * sp + g) * 1152 + hw * 256 + c * 16;
    const int wo_hi = (hw == 0 && c < 8) ? wo_lo + 1024 : OOB;
    if (hw == 3) {
      const unsigned long long ev = ((unsigned long long)(unsigned)__builtin_amdgcn_readfirstlane(evv[1]) << 32) | (unsigned)__builtin_amdgcn_readfirstlane(evv[0]);
      const unsigned long long tag64 = ev * 6364136223846793005ull + 1442695040888963407ull;
      const unsigned tag_hi = (unsigned)(tag64 >> 32);
      const unsigned tag_lo8 = ((unsigned)tag64 >> 24) & 0xffu;
      const int fb = fid >= 36 ? 1 : 0;
      {
        float s = 0.f;
#pragma unroll
        for (int ww = 0; ww < 4; ++ww) s += *(const LAS float*)(smem + L_QP + (ww * 64 + lane) * 4);
        DRAIN();
        v2u gr; gr[0] = ((__float_as_uint(s) + 0x80u) & 0xffffff00u) | tag_lo8; gr[1] = tag_hi;
        __builtin_amdgcn_raw_buffer_store_b64(gr, wsr, WS_QG + ((grp * 2 + (sp & 1)) * 256 + (sp >> 1) * 64 + lane) * 8, 0, AUX_SC1);
      }
      STAMP(13);
      for (unsigned spins = 0;; ++spins) {
        v4u p[4];
#pragma unroll
        for (int i = 0; i < 4; ++i) p[i] = __builtin_amdgcn_raw_buffer_load_b128(wsr, WS_QG + grp * 4096 + lane * 32 + (i & 1) * 16 + (i >> 1) * 2048, 0, AUX_SC1);
        unsigned bad = 0;
#pragma unroll
        for (int i = 0; i < 4; ++i) bad |= (p[i][1] ^ tag_hi) | (p[i][3] ^ tag_hi) | ((p[i][0] ^ tag_lo8) & 0xffu) | ((p[i][2] ^ tag_lo8) & 0xffu);
        if (__all(bad == 0u) || spins > SPIN_CAP) {
          float qv[4];
#pragma unroll
          for (int i = 0; i < 4; ++i)
            qv[i] = __uint_as_float(p[i >> 1][(i & 1) * 2] & 0xffffff00u) + __uint_as_float(p[2 + (i >> 1)][(i & 1) * 2] & 0xffffff00u);
          v2u qq; qq[0] = pk_bf16(qv[0], qv[1]); qq[1] = pk_bf16(qv[2], qv[3]);
          *(LAS v2u*)(smem + L_QS + lane * 8) = qq;
          break;
        }
        __builtin_amdgcn_s_sleep(2);
      }
      lds_inc(smem, 0, lane);
      STAMP(3);
      if (has_task) {
        const rsrc_t dr = mk_rsrc(wd, F * D * 2);
        const int dbase = (task * 32 + g) * 1152 + c * 16;
        f32x4 da[5];
#pragma unroll
        for (int cb = 0; cb < 5; ++cb) da[cb] = f32x4{0.f, 0.f, 0.f, 0.f};
#pragma unroll
        for (int hf = 0; hf < 2; ++hf) {
          v4u wv[20];
#pragma unroll
          for (int i = 0; i < 4; ++i)
#pragma unroll
            for (int cb = 0; cb < 5; ++cb)
              wv[i * 5 + cb] = __builtin_amdgcn_raw_buffer_load_b128(dr, (cb == 4 && c >= 8) ? OOB : dbase + cb * 256, (hf * 4 + i) * 4608, AUX_NT);
          __builtin_amdgcn_sched_barrier(0);
          if (hf == 0) {
            lds_wait<4>(smem, 1, 3u);
            const LAS float* gu = (const LAS float*)(smem + L_GU) + lane;
            const float gv = rbf(gu[0] + gu[128] + gu[256]), uv = rbf(gu[64] + gu[192] + gu[320]);
            const float inner = 0.7978845608028654f * (gv + 0.044715f * gv * gv * gv);
            const float ge = rbf(0.5f * gv * (1.f + tanhf(inner)));
            *(LAS float*)(smem + L_HS + lane * 4) = rbf(ge * uv);
            LDSW();
          }
#pragma unroll
          for (int i = 0; i < 4; ++i) {
            const v4u bb = diag(*(const LAS float*)(smem + L_HS + ((c >> 3) * 32 + (hf * 4 + i) * 4 + g) * 4), msk);
#pragma unroll
            for (int cb = 0; cb < 5; ++cb) da[cb] = mfma(wv[i * 5 + cb], bb, da[cb]);
          }
          __builtin_amdgcn_sched_barrier(0);
        }
#pragma unroll
        for (int cb = 0; cb < 5; ++cb)
#pragma unroll
          for (int rr = 0; rr < 4; ++rr) {
            const int col = cb * 128 + (4 * g + rr) * 8 + (c & 7);
            if (col < D) *(LAS float*)(smem + L_FP + ((c >> 3) * D + col) * 4) = da[cb][rr];
          }
        LDSW();
#pragma unroll
        for (int i = 0; i < 5; ++i) {
          const int o = (i * 64 + lane) * 16;
          if (o < 4608) {
            const v4u v = *(const LAS v4u*)(smem + L_FP + o);
            __builtin_amdgcn_raw_buffer_store_b128(v, wsr, WS_FP + task * 4608 + o, 0, AUX_SC1);
          }
        }
        DRAIN();
        if (lane == 0) {
          v2u fl; fl[0] = (unsigned)tag64; fl[1] = tag_hi;
          __builtin_amdgcn_raw_buffer_store_b64(fl, wsr, WS_FF + task * 64, 0, AUX_SC1);
        }
      }
      if (is_final) {
        for (unsigned spins = 0;; ++spins) {
          unsigned bad = 0;
#pragma unroll
          for (int i = 0; i < 4; ++i) {
            const v2u p = __builtin_amdgcn_raw_buffer_load_b64(wsr, WS_QG + (i * 64 + lane) * 512, 0, AUX_SC1);
            bad |= (p[1] ^ tag_hi) | ((p[0] ^ tag_lo8) & 0xffu);
          }
          if (__all(bad == 0u) || spins > SPIN_CAP) break;
          __builtin_amdgcn_s_sleep(16);
        }
        if (fid == 0 && lane == 0) {
          v2u e; e[0] = (unsigned)tag64; e[1] = tag_hi;
          __builtin_amdgcn_raw_buffer_store_b64(e, wsr, WS_E, 0, AUX_SC1);
        }
        {
          float z = 0.f;
#pragma unroll
          for (int i = 0; i < 9; ++i) {
            const unsigned xv = *(const LAS unsigned*)(smem + L_XS + (i * 64 + lane) * 4);
            z += rbf((fb ? bfhi(xv) : bflo(xv)) * bflo(gw[i * 64 + lane]));
          }
          z += dpp<XOR1>(z); z += dpp<XOR2>(z); z += dpp<ROR4>(z); z += dpp<ROR8>(z);
          z = rbf(xrow_sum(z));
          if (lane == 0) *(LAS float*)(smem + L_FFN + 64) = rbf(1.f / (1.f + __expf(-z)));
        }
        for (unsigned spins = 0;; ++spins) {
          unsigned bad = 0;
#pragma unroll
          for (int i = 0; i < 3; ++i) {
            const int tk = i * 64 + lane;
            const v2u p = __builtin_amdgcn_raw_buffer_load_b64(wsr, WS_FF + (tk < 144 ? tk : 0) * 64, 0, AUX_SC1);
            bad |= (p[1] ^ tag_hi) | (p[0] ^ (unsigned)tag64);
          }
          if (__all(bad == 0u) || spins > SPIN_CAP) break;
          __builtin_amdgcn_s_sleep(64);
        }
        __builtin_amdgcn_fence(__ATOMIC_ACQUIRE, "wavefront");
        {
          const int fo = WS_FP + (lane >> 2) * 4608 + (fid * 16 + (lane & 3) * 4) * 4;
          v4u pv[9];
#pragma unroll
          for (int i = 0; i < 9; ++i) pv[i] = __builtin_amdgcn_raw_buffer_load_b128(wsr, fo, i * 16 * 4608, AUX_SC1);
          f32x4 fa = {0.f, 0.f, 0.f, 0.f};
#pragma unroll
          for (int i = 0; i < 9; ++i) fa += __builtin_bit_cast(f32x4, pv[i]);
#pragma unroll
          for (int j = 0; j < 4; ++j) { float t = fa[j]; t += dpp<ROR4>(t); t += dpp<ROR8>(t); fa[j] = xrow_sum(t); }
          if (lane < 4) *(LAS f32x4*)(smem + L_FFN + lane * 16) = fa;
        }
        lds_inc(smem, 6, lane);
      }
      STAMP(5);
      lds_wait<32>(smem, 7, 4u);
      if (lane == 0) {
        v2u fl; fl[0] = (unsigned)tag64; fl[1] = tag_hi;
        __builtin_amdgcn_raw_buffer_store_b64(fl, wsr, WS_SF + bid * 64, 0, AUX_SC1);
      }
      for (unsigned spins = 0;; ++spins) {
        const v2u p = __builtin_amdgcn_raw_buffer_load_b64(wsr, WS_SF + (grp * 8 + (lane & 7)) * 64, 0, AUX_SC1);
        if (__all(((p[1] ^ tag_hi) | (p[0] ^ (unsigned)tag64)) == 0u) || spins > SPIN_CAP) break;
        __builtin_amdgcn_s_sleep(32);
      }
      lds_inc(smem, 9, lane);
    } else if (has_task) {
      const int r = lane >> 2;
      const int gbase = (hw * 192 + r) * (F * 2) + task * 64 + (lane & 3) * 16;
#pragma unroll 1
      for (int mat = 0; mat < 2; ++mat) {
        const rsrc_t gr = mk_rsrc(mat ? wu : wg, D * F * 2);
        float a0[8], a1[8];
#pragma unroll
        for (int j = 0; j < 8; ++j) { a0[j] = 0.f; a1[j] = 0.f; }
#pragma unroll 1
        for (int bt = 0; bt < 2; ++bt) {
          v4u wv[6];
#pragma unroll
          for (int i = 0; i < 6; ++i) wv[i] = __builtin_amdgcn_raw_buffer_load_b128(gr, gbase, (bt * 6 + i) * 16 * (F * 2), AUX_NT);
          __builtin_amdgcn_sched_barrier(0);
#pragma unroll
          for (int i = 0; i < 6; ++i) {
            const unsigned xv = *(const LAS unsigned*)(smem + L_XS + (hw * 192 + (bt * 6 + i) * 16 + r) * 4);
            fma8(a0, bflo(xv), wv[i]); fma8(a1, bfhi(xv), wv[i]);
          }
        }
        const f32x2 r0 = red8<true>(a0), r1 = red8<true>(a1);
        if ((lane & 12) == 0) {
          const int col = (lane & 3) * 8 + 4 * (lane >> 5) + 2 * ((lane >> 4) & 1);
          *(LAS f32x2*)(smem + L_GU + (((hw * 2 + mat) * 2 + 0) * 32 + col) * 4) = r0;
          *(LAS f32x2*)(smem + L_GU + (((hw * 2 + mat) * 2 + 1) * 32 + col) * 4) = r1;
        }
      }
      lds_inc(smem, 1, lane);
    }
#pragma unroll
    for (int i = 0; i < 8; ++i) WO[i] = __builtin_amdgcn_raw_buffer_load_b128(wor, wo_lo, i * 4608, AUX_NT);
#pragma unroll
    for (int i = 8; i < 16; ++i) WO[i] = __builtin_amdgcn_raw_buffer_load_b128(wor, wo_hi, (i - 8) * 4608, AUX_NT);
    if (TOUCH && hw == 1) {
      const unsigned char* tp = (lane & 8 ? Vc : Kc) + (size_t)grp * (SK * 256) + ((lane & 7) * 256 + 255) * 4096 + 4032;
      if (lane >= 16) tp = (const unsigned char*)out;
      const unsigned tv = __builtin_nontemporal_load((const unsigned*)tp);
      asm volatile("" :: "v"(tv));
    }
    __builtin_amdgcn_sched_barrier(0);
    DRAIN();
    lds_wait<32>(smem, 2, 1u);
    if (!H1P && hw == 3) gather_o(false);
    lds_wait<0>(smem, 4, 1u);
    const unsigned pp = *(const LAS unsigned*)(smem + L_OS + lane * 4);
    STAMP(9);
    {
      f32x4 y0 = {0.f, 0.f, 0.f, 0.f}, y1 = {0.f, 0.f, 0.f, 0.f}, z0 = {0.f, 0.f, 0.f, 0.f};
#define PRJ(i, y) { const unsigned t_ = dppu<0x150 + (i)>(pp); v4u bb; bb[0] = t_ & msk[0]; bb[1] = t_ & msk[1]; bb[2] = t_ & msk[2]; bb[3] = t_ & msk[3]; \
                    y = mfma(WO[i], bb, y); z0 = mfma(WO[8 + (i)], bb, z0); }
      PRJ(0, y0) PRJ(1, y1) PRJ(2, y0) PRJ(3, y1) PRJ(4, y0) PRJ(5, y1) PRJ(6, y0) PRJ(7, y1)
#undef PRJ
      y0 += y1;
      const int dst = WS_H2 + ((b * 36 + hw * 8 + 2 * g) * 128 + (bid & 127)) * 64 + c * 4;
      __builtin_amdgcn_raw_buffer_store_b32(__float_as_uint(hi8 ? y0[1] : y0[0]) | 1u, wsr, dst, 0, AUX_SC1);
      __builtin_amdgcn_raw_buffer_store_b32(__float_as_uint(hi8 ? y0[3] : y0[2]) | 1u, wsr, dst, 8192, AUX_SC1);
      if (hw == 0) {
        const int dz = g < 2 ? dst + 32 * 8192 : OOB;
        __builtin_amdgcn_raw_buffer_store_b32(__float_as_uint(hi8 ? z0[1] : z0[0]) | 1u, wsr, dz, 0, AUX_SC1);
        __builtin_amdgcn_raw_buffer_store_b32(__float_as_uint(hi8 ? z0[3] : z0[2]) | 1u, wsr, dz, 8192, AUX_SC1);
      }
    }
    STAMP(15);
  }
  STAMP(11);
  STAMP_FLUSH();
}

extern "C" void kernel_launch(void* const* d_in, const int* in_sizes, int n_in, void* d_out, int out_size, void* d_ws, size_t ws_size,
                              hipStream_t stream) {
  k_fused<<<256, NTHR, 0, stream>>>((const unsigned short*)d_in[0], (const unsigned char*)d_in[1], (const unsigned char*)d_in[2],
                                    (const unsigned char*)d_in[3], (const unsigned char*)d_in[4], (const unsigned char*)d_in[6],
                                    (const unsigned char*)d_in[7], (const unsigned char*)d_in[8], (const unsigned short*)d_in[9],
                                    (unsigned char*)d_ws, (unsigned*)d_out);
}
```

```cpp
#include <hip/hip_runtime.h>
#include <hip/hip_bf16.h>
#include <stdint.h>

typedef unsigned v4u __attribute__((ext_vector_type(4)));
typedef unsigned v2u __attribute__((ext_vector_type(2)));
typedef float f32x4 __attribute__((ext_vector_type(4)));
typedef float f32x2 __attribute__((ext_vector_type(2)));
typedef short bf16x8 __attribute__((ext_vector_type(8)));
typedef __bf16 bf16x2 __attribute__((ext_vector_type(2)));
typedef __amdgpu_buffer_rsrc_t rsrc_t;
#define LAS __attribute__((address_space(3)))

constexpr int D = 576, SK = 32768, F = 4608;
constexpr int NW = 8, NTHR = NW * 64;
#define NSU_V 116
constexpr unsigned NSU = NSU_V;
constexpr unsigned ND = 8 * (128 - NSU);
constexpr unsigned NONE = 1024;
static_assert(NSU % 4 == 0 && (NSU / 4) % 2 == 1, "static V steps per wave: odd");
constexpr int AUX_NT = 2, AUX_SC1 = 16;
constexpr int OOB = 0x40000000;
constexpr unsigned SPIN_CAP = 200000u;
#define H1P 1
#define NE 2
#define STAG 4
#define H1MASK 1
#define TOUCH 0
#define LATE0 0
#define WTS 0
#define WTD 4
#define HDELAY 100
#define NEARD 64
#define PSLEEP 2

constexpr int WS_E = 0;
constexpr int WS_CNT = 4096;
constexpr int WS_QG = 16384;
constexpr int WS_H1 = WS_QG + 131072;
constexpr int H1_STRIDE = 2176;
constexpr int WS_H2 = WS_H1 + 256 * H1_STRIDE;
constexpr int WS_ST = WS_H2 + 144 * 4096;
constexpr int WS_SF = WS_ST + 4096;
constexpr int WS_SG = WS_SF + 256 * 64;
constexpr int WS_F1 = WS_SG + 32 * 1024 * 128;
constexpr int WS_G1 = WS_F1 + 4096;
constexpr int WS_END = WS_G1 + 36 * 14 * 1024;

constexpr int L_XS = 0;
constexpr int L_QP = 2304;
constexpr int L_QS = L_QP + 2048;
constexpr int L_MRG = L_QS + 512;
constexpr int L_ML = L_MRG + 8192;
constexpr int L_OS = L_ML + 96;
constexpr int L_GU = L_OS + 256;
constexpr int L_TAG = L_GU + 2048;
constexpr int L_FFN = L_TAG + 16;
constexpr int L_FB = L_FFN + 256;
constexpr int L_SYNC = L_FB + 256;
constexpr int L_SC = L_SYNC + 48;
constexpr int L_END = L_SC + 16384;
constexpr int LDS_BYTES = 96 * 1024;
static_assert(L_END <= LDS_BYTES, "lds");

__device__ __forceinline__ float bflo(unsigned u) { return __builtin_bit_cast(float, u << 16); }
__device__ __forceinline__ float bfhi(unsigned u) { return __builtin_bit_cast(float, u & 0xffff0000u); }
__device__ __forceinline__ unsigned pk_bf16(float lo, float hi) {
  bf16x2 v; v[0] = (__bf16)lo; v[1] = (__bf16)hi; return __builtin_bit_cast(unsigned, v);
}
__device__ __forceinline__ float rbf(float f) { return bflo(pk_bf16(f, f)); }
__device__ __forceinline__ float dot2(unsigned a, unsigned b, float c) {
  return __builtin_amdgcn_fdot2_f32_bf16(__builtin_bit_cast(bf16x2, a), __builtin_bit_cast(bf16x2, b), c, false);
}
template <int CTRL> __device__ __forceinline__ float dpp(float x) {
  return __builtin_bit_cast(float, __builtin_amdgcn_mov_dpp(__builtin_bit_cast(int, x), CTRL, 0xf, 0xf, true));
}
constexpr int XOR1 = 0xB1, XOR2 = 0x4E, HMIR = 0x141, ROR4 = 0x124, ROR8 = 0x128;
__device__ __forceinline__ f32x4 mfma(v4u a, v4u b, f32x4 c) {
  return __builtin_amdgcn_mfma_f32_16x16x32_bf16(__builtin_bit_cast(bf16x8, a), __builtin_bit_cast(bf16x8, b), c, 0, 0, 0);
}
__device__ __forceinline__ float swap16_sum(float a, float b) {
  auto s = __builtin_amdgcn_permlane16_swap(__float_as_uint(a), __float_as_uint(b), false, false);
  return __uint_as_float(s[0]) + __uint_as_float(s[1]);
}
__device__ __forceinline__ float swap32_sum(float a, float b) {
  auto s = __builtin_amdgcn_permlane32_swap(__float_as_uint(a), __float_as_uint(b), false, false);
  return __uint_as_float(s[0]) + __uint_as_float(s[1]);
}
__device__ __forceinline__ float xrow_max(float x) {
  auto s = __builtin_amdgcn_permlane16_swap(__float_as_uint(x), __float_as_uint(x), false, false);
  x = fmaxf(__uint_as_float(s[0]), __uint_as_float(s[1]));
  auto t = __builtin_amdgcn_permlane32_swap(__float_as_uint(x), __float_as_uint(x), false, false);
  return fmaxf(__uint_as_float(t[0]), __uint_as_float(t[1]));
}
__device__ __forceinline__ float xrow_sum(float x) { x = swap16_sum(x, x); return swap32_sum(x, x); }
__device__ __forceinline__ rsrc_t mk_rsrc(const void* p, int bytes) { return __builtin_amdgcn_make_buffer_rsrc((void*)p, 0, bytes, 0x00020000); }
#define BAR() do { asm volatile("s_waitcnt lgkmcnt(0)" ::: "memory"); __builtin_amdgcn_s_barrier(); asm volatile("" ::: "memory"); } while (0)
#define DRAIN() do { __builtin_amdgcn_s_waitcnt(0x0F70); asm volatile("s_waitcnt vmcnt(0)" ::: "memory"); } while (0)
#define WAITV(n) do { __builtin_amdgcn_s_waitcnt(0x0F70 | ((n) & 15) | (((n) >> 4) << 14)); asm volatile("" ::: "memory"); } while (0)
#define LDSW() asm volatile("s_waitcnt lgkmcnt(0)" ::: "memory")

#define STAMP(i) do {} while (0)
#define STAMP_FLUSH() do {} while (0)
constexpr float SCALE = 0.08838834764831845f, L2E = 1.4426950408889634f;

typedef double f64x4 __attribute__((ext_vector_type(4)));
struct St { float m; double l; f64x4 acc; };
__device__ __forceinline__ void pin(St& st) { asm volatile("" : "+v"(st.m), "+v"(st.l), "+v"(st.acc) :: "memory"); }
__device__ __forceinline__ v4u diag(float v, const v4u& msk) {
  const unsigned pp = pk_bf16(v, v);
  v4u bb; bb[0] = pp & msk[0]; bb[1] = pp & msk[1]; bb[2] = pp & msk[2]; bb[3] = pp & msk[3];
  return bb;
}

__device__ __forceinline__ v2u scores(const v4u* KB, const v4u& q0, const v4u& q1, bool hi8) {
  float s[4];
#pragma unroll
  for (int t = 0; t < 4; ++t) {
    float a0 = dot2(KB[t][0], q0[0], 0.f), a1 = dot2(KB[t][0], q1[0], 0.f);
#pragma unroll
    for (int k = 1; k < 4; ++k) { a0 = dot2(KB[t][k], q0[k], a0); a1 = dot2(KB[t][k], q1[k], a1); }
    const float keep = hi8 ? a1 : a0, send = hi8 ? a0 : a1;
    float v = keep + dpp<ROR8>(send);
    v += dpp<HMIR>(v); v += dpp<XOR2>(v); v += dpp<XOR1>(v);
    s[t] = v;
  }
  const unsigned u0 = pk_bf16(s[0], s[1]), u1 = pk_bf16(s[2], s[3]);
  v2u r; r[0] = pk_bf16(bflo(u0) * SCALE, bfhi(u0) * SCALE); r[1] = pk_bf16(bflo(u1) * SCALE, bfhi(u1) * SCALE);
  return r;
}
__device__ __forceinline__ void pv_unit(const v4u (&VB)[8], const v4u sc, const v4u& msk, St& st) {
  float s[8];
#pragma unroll
  for (int t = 0; t < 4; ++t) { s[2 * t] = bflo(sc[t]); s[2 * t + 1] = bfhi(sc[t]); }
  float tm = fmaxf(fmaxf(fmaxf(s[0], s[1]), fmaxf(s[2], s[3])), fmaxf(fmaxf(s[4], s[5]), fmaxf(s[6], s[7])));
  tm = xrow_max(tm);
  const float tl = tm * L2E, mt = __builtin_ceilf(tl), fr = tl - mt;
  const float mn = fmaxf(st.m, mt);
  const double alpha = (double)__builtin_amdgcn_exp2f(st.m - mn);
  const float beta = __builtin_amdgcn_exp2f(mt - mn);
  st.m = mn;
  float p[8];
#pragma unroll
  for (int t = 0; t < 8; ++t) p[t] = __builtin_amdgcn_exp2f(fmaf(s[t] - tm, L2E, fr));
  f32x4 ta = {0.f, 0.f, 0.f, 0.f};
#pragma unroll
  for (int t = 0; t < 8; ++t) ta = mfma(VB[t], diag(p[t], msk), ta);
  st.l = __builtin_fma(st.l, alpha, (double)((((p[0] + p[1]) + (p[2] + p[3])) + ((p[4] + p[5]) + (p[6] + p[7]))) * beta));
#pragma unroll
  for (int r = 0; r < 4; ++r) st.acc[r] = __builtin_fma(st.acc[r], alpha, (double)(ta[r] * beta));
}
__device__ __forceinline__ void issue(v4u (&B)[8], rsrc_t r, int voff, unsigned unit) {
  const int so = (int)(unit * 8192u);
#pragma unroll
  for (int t = 0; t < 4; ++t) B[t] = __builtin_amdgcn_raw_buffer_load_b128(r, voff + t * 1024, so, AUX_NT);
#pragma unroll
  for (int t = 0; t < 4; ++t) B[4 + t] = __builtin_amdgcn_raw_buffer_load_b128(r, voff + t * 1024, so + 4096, AUX_NT);
}
__device__ __forceinline__ unsigned atomic_inc(rsrc_t r, int voff0, bool on, int so) {
  return (unsigned)__builtin_amdgcn_raw_ptr_buffer_atomic_add_i32(1, r, voff0, on ? so : OOB, 0);
}
__device__ __forceinline__ unsigned lds_ticket(unsigned lds_addr_v, unsigned vone) {
  unsigned long long keep; unsigned r, s;
  asm volatile("s_mov_b64 %1, exec\n\ts_mov_b64 exec, 1\n\tds_add_rtn_u32 %0, %3, %4\n\ts_mov_b64 exec, %1\n\ts_waitcnt lgkmcnt(0)\n\tv_readfirstlane_b32 %2, %0\n\ts_nop 1"
               : "=&v"(r), "=&s"(keep), "=s"(s) : "v"(lds_addr_v), "v"(vone) : "memory");
  return s;
}
template <int CTRL> __device__ __forceinline__ unsigned dppu(unsigned x) { return (unsigned)__builtin_amdgcn_mov_dpp((int)x, CTRL, 0xf, 0xf, true); }
template <int CTRL> __device__ __forceinline__ double dppd(double x) {
  const v2u u = __builtin_bit_cast(v2u, x);
  v2u r; r[0] = dppu<CTRL>(u[0]); r[1] = dppu<CTRL>(u[1]);
  return __builtin_bit_cast(double, r);
}
__device__ __forceinline__ double xrow_sumd(double x) {
  v2u u = __builtin_bit_cast(v2u, x), a, b2;
  { auto s0 = __builtin_amdgcn_permlane16_swap(u[0], u[0], false, false); auto s1 = __builtin_amdgcn_permlane16_swap(u[1], u[1], false, false);
    a[0] = s0[0]; a[1] = s1[0]; b2[0] = s0[1]; b2[1] = s1[1]; }
  x = __builtin_bit_cast(double, a) + __builtin_bit_cast(double, b2);
  u = __builtin_bit_cast(v2u, x);
  { auto s0 = __builtin_amdgcn_permlane32_swap(u[0], u[0], false, false); auto s1 = __builtin_amdgcn_permlane32_swap(u[1], u[1], false, false);
    a[0] = s0[0]; a[1] = s1[0]; b2[0] = s0[1]; b2[1] = s1[1]; }
  return __builtin_bit_cast(double, a) + __builtin_bit_cast(double, b2);
}
__device__ __forceinline__ void lds_inc(unsigned char* smem, int idx, int lane) {
  LDSW();
  if (lane == 0) __hip_atomic_fetch_add((LAS unsigned*)(LAS unsigned char*)smem + (L_SYNC / 4 + idx), 1u, __ATOMIC_RELAXED, __HIP_MEMORY_SCOPE_WORKGROUP);
}
template <int SLEEP> __device__ __forceinline__ void lds_wait(unsigned char* smem, int idx, unsigned want) {
#pragma unroll 1
  for (unsigned spins = 0; spins < 8000000u / (1 + SLEEP); ++spins) {
    const unsigned v = __builtin_amdgcn_readfirstlane(*((volatile LAS unsigned*)(LAS unsigned char*)smem + (L_SYNC / 4 + idx)));
    if (v >= want) break;
    if (SLEEP) __builtin_amdgcn_s_sleep(SLEEP);
  }
  asm volatile("" ::: "memory");
}

__device__ __forceinline__ void fma8(float (&acc)[8], float xv, const v4u& w) {
#pragma unroll
  for (int k = 0; k < 4; ++k) { acc[2 * k] = fmaf(xv, bflo(w[k]), acc[2 * k]); acc[2 * k + 1] = fmaf(xv, bfhi(w[k]), acc[2 * k + 1]); }
}
template <bool BIT2> __device__ __forceinline__ f32x2 red8(const float (&acc)[8]) {
  float a[4];
#pragma unroll
  for (int j = 0; j < 4; ++j) a[j] = swap32_sum(acc[j], acc[j + 4]);
  float b0 = swap16_sum(a[0], a[2]), b1 = swap16_sum(a[1], a[3]);
  if (BIT2) { b0 += dpp<ROR4>(b0); b1 += dpp<ROR4>(b1); }
  b0 += dpp<ROR8>(b0); b1 += dpp<ROR8>(b1);
  f32x2 r; r[0] = b0; r[1] = b1; return r;
}
__device__ __forceinline__ float val(unsigned u) { return __uint_as_float(u & ~1u); }
__device__ __forceinline__ bool all4(const v4u& p) { return (p[0] & p[1] & p[2] & p[3] & 1u) != 0u; }

__global__ void __launch_bounds__(NTHR, 2)
k_fused(const unsigned short* __restrict__ x, const unsigned char* Kc, const unsigned char* Vc,
        const unsigned char* __restrict__ wq, const unsigned char* wo, const unsigned char* wg,
        const unsigned char* wu, const unsigned char* wd, const unsigned short* __restrict__ gw,
        unsigned char* ws, unsigned* out) {
  __shared__ __attribute__((aligned(16))) unsigned char smem[LDS_BYTES];
  const int tid = threadIdx.x, lane = tid & 63, w = __builtin_amdgcn_readfirstlane(tid >> 6);
  const int bid = blockIdx.x, grp = bid >> 3, sp = bid & 7, b = grp >> 4, kvh = grp & 15;
  const int g = lane >> 4, c = lane & 15;
  unsigned tsv = 0xffffffffu; (void)tsv;
  STAMP(0);
  const int voff = lane * 16;
  const int ridx = (grp + 4 * sp) & 31;
  const bool is_final = ridx < 9;
  const int fid = sp * 9 + ridx;
  const bool hi8 = (c & 8) != 0;
#define MAKE_MSK() v4u msk; _Pragma("unroll") for (int d = 0; d < 4; ++d) msk[d] = (((c & 7) >> 1) == d) ? ((c & 1) ? 0xffff0000u : 0x0000ffffu) : 0u;

  auto gather_o = [&](bool watch_flag) {
    const rsrc_t wsr = mk_rsrc(ws, WS_END);
    const int o_acc = WS_H1 + (grp * 8 + 4 * (c >> 3)) * H1_STRIDE + (32 * sp + 8 * g + (c & 7)) * 8;
    const int o_ml = WS_H1 + (grp * 8 + (c & 7)) * H1_STRIDE + 2048 + (sp >> 2) * 16;
    v2u pv[4];
    v4u pm = {0u, 0u, 0u, 0u};
#pragma unroll
    for (int i = 0; i < 4; ++i) pv[i] = v2u{0u, 0u};
    for (unsigned spins = 0;; ++spins) {
      if (H1MASK) {
        if (!(pv[0][0] & pv[1][0] & pv[2][0] & pv[3][0] & 1u)) {
#pragma unroll
          for (int i = 0; i < 4; ++i) pv[i] = __builtin_amdgcn_raw_buffer_load_b64(wsr, o_acc, i * H1_STRIDE, AUX_SC1);
        }
        if (!(pm[0] & pm[2] & 1u)) pm = __builtin_amdgcn_raw_buffer_load_b128(wsr, o_ml, 0, AUX_SC1);
      } else {
#pragma unroll
        for (int i = 0; i < 4; ++i) pv[i] = __builtin_amdgcn_raw_buffer_load_b64(wsr, o_acc, i * H1_STRIDE, AUX_SC1);
        pm = __builtin_amdgcn_raw_buffer_load_b128(wsr, o_ml, 0, AUX_SC1);
      }
      if (__all((pv[0][0] & pv[1][0] & pv[2][0] & pv[3][0] & pm[0] & pm[2] & 1u) != 0u) || spins > SPIN_CAP) break;
      if (watch_flag && __builtin_amdgcn_readfirstlane(*((volatile LAS unsigned*)(smem + L_SYNC) + 4)) != 0u) return;
      __builtin_amdgcn_s_sleep(PSLEEP);
    }
    STAMP(8);
    const float Ms = __uint_as_float(pm[1]);
    float Mx = fmaxf(Ms, dpp<XOR1>(Ms)); Mx = fmaxf(Mx, dpp<XOR2>(Mx)); Mx = fmaxf(Mx, dpp<HMIR>(Mx));
    const float wt = __builtin_amdgcn_exp2f(Ms - Mx);
    v2u lv; lv[0] = pm[2]; lv[1] = pm[3];
    double den = (double)wt * __builtin_bit_cast(double, lv);
    den += dppd<XOR1>(den); den += dppd<XOR2>(den); den += dppd<HMIR>(den);
    double num = 0.0;
#define ACC1(i) { const float wl_ = dpp<0x150 + (i)>(wt), wh_ = dpp<0x154 + (i)>(wt); num = __builtin_fma((double)(hi8 ? wh_ : wl_), __builtin_bit_cast(double, pv[i]), num); }
    ACC1(0) ACC1(1) ACC1(2) ACC1(3)
#undef ACC1
    num += dppd<ROR8>(num);
    const float ov = (float)num / (float)den;
    *(LAS unsigned*)(smem + L_OS + lane * 4) = pk_bf16(ov, ov);
    lds_inc(smem, 4, lane);
  };

  if (w >= 4) {
    const int sw = w - 4, stid = tid - 256;
    const rsrc_t kr = mk_rsrc(Kc + (size_t)grp * (SK * 256), SK * 256);
    const rsrc_t vr = mk_rsrc(Vc + (size_t)grp * (SK * 256), SK * 256);
    const rsrc_t wsr = mk_rsrc(ws, WS_END);
    const unsigned ubase = (unsigned)(sp * 128 + sw);
    v4u Ba[8], Bb[8], Be[NE][8];
    if (!LATE0) issue(Be[0], kr, voff, ubase);
    __builtin_amdgcn_sched_barrier(0);
    asm volatile("" ::: "memory");
    __builtin_amdgcn_s_barrier();
    asm volatile("" ::: "memory");
    if (LATE0) issue(Be[0], kr, voff, ubase);
    __builtin_amdgcn_sched_barrier(0);
    STAMP(1);
    MAKE_MSK();
#pragma unroll
    for (int e = 1; e < NE; ++e) {
      DRAIN();
      issue(Be[e], kr, voff, ubase + 4 * e);
      __builtin_amdgcn_sched_barrier(0);
    }
    DRAIN();
    issue(Ba, kr, voff, ubase + 4 * NE);
    __builtin_amdgcn_sched_barrier(0);
    DRAIN();
    issue(Bb, kr, voff, ubase + 4 * NE + 4);
    __builtin_amdgcn_sched_barrier(0);
    STAMP(2);
    lds_wait<1>(smem, 0, 1u);
    STAMP(3);
    const v4u q0 = *(const LAS v4u*)(smem + L_QS + c * 16), q1 = *(const LAS v4u*)(smem + L_QS + 256 + c * 16);
    const int sc_off = ((c >> 3) * 4 + g) * 16;
    const int sg_st = (c & 7) == 0 ? WS_SG + grp * 131072 + sc_off : OOB;
#define KSC(B) const v2u s0_ = scores(&B[0], q0, q1, hi8), s1_ = scores(&B[4], q0, q1, hi8);                        \
      v4u sc_; sc_[0] = s0_[0]; sc_[1] = s0_[1]; sc_[2] = s1_[0]; sc_[3] = s1_[1];
#define KSTEP(B, i)                                                                                                \
    {                                                                                                              \
      KSC(B)                                                                                                       \
      if ((c & 7) == 0) *(LAS v4u*)(smem + L_SC + (4 * (i) + sw) * 128 + sc_off) = sc_;                            \
      asm volatile("" ::: "memory");                                                                               \
      __builtin_amdgcn_sched_barrier(0);                                                                           \
    }
#define KSTEPG(B, u)                                                                                               \
    {                                                                                                              \
      KSC(B)                                                                                                       \
      __builtin_amdgcn_raw_buffer_store_b128(sc_, wsr, sg_st, (int)((u) * 128u), AUX_SC1);                         \
      asm volatile("" ::: "memory");                                                                               \
      __builtin_amdgcn_sched_barrier(0);                                                                           \
    }
    const int cnt0 = lane == 0 ? WS_CNT + grp * 256 : OOB;
    unsigned vone = 1u, a = 0u;
    unsigned taddr = (unsigned)(size_t)(LAS unsigned char*)(smem + L_SYNC + 28);
    asm volatile("" : "+v"(vone), "+v"(taddr));
    KSTEP(Ba, NE);
    issue(Ba, kr, voff, ubase + 4 * (NE + 2));
    __builtin_amdgcn_sched_barrier(0);
#pragma unroll
    for (int e = 0; e < NE; ++e) KSTEP(Be[e], e);
    KSTEP(Bb, NE + 1);
    static_assert(NE % 2 == 0, "K steps left: odd");
#pragma unroll 1
    for (int i = NE + 2; i < (int)(NSU / 4) - 1; i += 2) {
      DRAIN();
      issue(Bb, kr, voff, ubase + 4 * (i + 1));
      __builtin_amdgcn_sched_barrier(0);
      KSTEP(Ba, i);
      DRAIN();
      a = atomic_inc(wsr, cnt0, i + 3 >= (int)(NSU / 4), 0);
      issue(Ba, kr, voff, ubase + 4 * (i + 2));
      __builtin_amdgcn_sched_barrier(0);
      KSTEP(Bb, i + 1);
    }
#define MAPD(d) (((d) & 7u) * 128u + NSU + ((d) >> 3))
#define WAITA() DRAIN()
    {
      unsigned ua, ub;
      WAITA();
      unsigned d = __builtin_amdgcn_readfirstlane(a);
      a = atomic_inc(wsr, cnt0, d < ND, 0);
      ub = MAPD(d);
      issue(Bb, d < ND ? kr : vr, voff, d < ND ? ub : ubase);
      __builtin_amdgcn_sched_barrier(0);
      KSTEP(Ba, NSU / 4 - 1);
      bool in_b = true;
      while (d < ND) {
        WAITA();
        d = __builtin_amdgcn_readfirstlane(a);
        a = atomic_inc(wsr, cnt0, d < ND, 0);
        ua = MAPD(d);
        issue(Ba, d < ND ? kr : vr, voff, d < ND ? ua : ubase);
        __builtin_amdgcn_sched_barrier(0);
        KSTEPG(Bb, ub);
        if (d >= ND) { in_b = false; break; }
        WAITA();
        d = __builtin_amdgcn_readfirstlane(a);
        a = atomic_inc(wsr, cnt0, d < ND, 0);
        ub = MAPD(d);
        issue(Bb, d < ND ? kr : vr, voff, d < ND ? ub : ubase);
        __builtin_amdgcn_sched_barrier(0);
        KSTEPG(Ba, ua);
      }
      STAMP(4);
      DRAIN();
      if (in_b) {
#pragma unroll
        for (int t = 0; t < 8; ++t) Ba[t] = Bb[t];
      }
    }
#undef KSTEP
#undef KSTEPG
#undef KSC
    St st; st.m = -INFINITY; st.l = 0.0; st.acc = f64x4{0.0, 0.0, 0.0, 0.0};
#define LSC(i) (*(const LAS v4u*)(smem + L_SC + (4 * (i) + sw) * 128 + sc_off))
    lds_inc(smem, 7, lane);
#pragma unroll 1
    for (int i = 0; i < (int)(NSU / 4) - 1; i += 2) {
      DRAIN();
      issue(Bb, vr, voff, ubase + 4 * (i + 1));
      __builtin_amdgcn_sched_barrier(0);
      pv_unit(Ba, LSC(i), msk, st); pin(st);
      __builtin_amdgcn_sched_barrier(0);
      DRAIN();
      a = atomic_inc(wsr, cnt0, i + 3 >= (int)(NSU / 4), 4);
      issue(Ba, vr, voff, ubase + 4 * (i + 2));
      __builtin_amdgcn_sched_barrier(0);
      pv_unit(Bb, LSC(i + 1), msk, st); pin(st);
      __builtin_amdgcn_sched_barrier(0);
    }
    lds_wait<2>(smem, 9, 1u);
    v4u sa = LSC(NSU / 4 - 1), sb;
#undef LSC
    const int sg_ld = WS_SG + grp * 131072 + sc_off;
#define NEXT(nx)                                                                                                   \
    {                                                                                                              \
      const unsigned d_ = __builtin_amdgcn_readfirstlane(a);                                                       \
      nx = d_ < ND ? (d_ & 7u) * 128u + NSU + (d_ >> 3) : NONE;                                                    \
      if (d_ + NEARD >= ND) *((volatile LAS unsigned*)(smem + L_SYNC) + 2) = 1u;                                   \
      a = atomic_inc(wsr, cnt0, d_ < ND, 4);                                                               \
    }
    while (true) {
      unsigned nx;
      DRAIN();
      NEXT(nx);
      sb = __builtin_amdgcn_raw_buffer_load_b128(wsr, sg_ld, nx < NONE ? (int)(nx * 128u) : OOB, AUX_SC1);
      issue(Bb, vr, voff, nx);
      __builtin_amdgcn_sched_barrier(0);
      pv_unit(Ba, sa, msk, st); pin(st);
      __builtin_amdgcn_sched_barrier(0);
      if (nx >= NONE) break;
      DRAIN();
      NEXT(nx);
      sa = __builtin_amdgcn_raw_buffer_load_b128(wsr, sg_ld, nx < NONE ? (int)(nx * 128u) : OOB, AUX_SC1);
      issue(Ba, vr, voff, nx);
      __builtin_amdgcn_sched_barrier(0);
      pv_unit(Bb, sb, msk, st); pin(st);
      __builtin_amdgcn_sched_barrier(0);
      if (nx >= NONE) break;
    }
#undef NEXT
    STAMP(6);
    {
      const double l = xrow_sumd(st.l);
      const int h = c >> 3;
#pragma unroll
      for (int r = 0; r < 4; ++r) {
        const int dim = 8 * (4 * g + r) + (c & 7);
        *(LAS double*)(smem + L_MRG + (sw * 256 + h * 128 + (dim & 96) + (dim & 3) * 8 + ((dim >> 2) & 7)) * 8) = st.acc[r];
      }
      if (g == 0 && (c & 7) == 0) { *(LAS float*)(smem + L_ML + (h * 4 + sw) * 4) = st.m; *(LAS double*)(smem + L_ML + 32 + (h * 4 + sw) * 8) = l; }
    }
    lds_inc(smem, 3, lane);
    lds_wait<0>(smem, 3, 4u);
    STAMP(7);
    {
      const int hh = sw >> 1;
      const f32x4 m4 = *(const LAS f32x4*)(smem + L_ML + hh * 16);
      const f64x4 l4 = *(const LAS f64x4*)(smem + L_ML + 32 + hh * 32);
      double a4[4];
#pragma unroll
      for (int ww = 0; ww < 4; ++ww) a4[ww] = *(const LAS double*)(smem + L_MRG + (ww * 256 + stid) * 8);
      const float M = fmaxf(fmaxf(m4[0], m4[1]), fmaxf(m4[2], m4[3]));
      double L = 0.0, o = 0.0;
#pragma unroll
      for (int ww = 0; ww < 4; ++ww) {
        const double wt = (double)__builtin_amdgcn_exp2f(m4[ww] - M);
        L = __builtin_fma(wt, l4[ww], L); o = __builtin_fma(wt, a4[ww], o);
      }
      v2u ov = __builtin_bit_cast(v2u, o); ov[0] |= 1u;
      __builtin_amdgcn_raw_buffer_store_b64(ov, wsr, WS_H1 + bid * H1_STRIDE + stid * 8, 0, AUX_SC1);
      if ((stid & 127) == 0) {
        const v2u lv = __builtin_bit_cast(v2u, L);
        v4u ml; ml[0] = 1u; ml[1] = __float_as_uint(M); ml[2] = lv[0] | 1u; ml[3] = lv[1];
        __builtin_amdgcn_raw_buffer_store_b128(ml, wsr, WS_H1 + bid * H1_STRIDE + 2048 + hh * 16, 0, AUX_SC1);
      }
    }
    STAMP(14);
    if (H1P) {
      for (int i = 0; i < sw; ++i) __builtin_amdgcn_s_sleep(STAG);
      gather_o(true);
    }
    if (is_final) {
      const int src = WS_H2 + fid * 8192 + sw * 2048 + voff;
      lds_wait<8>(smem, 6, 1u);
      v4u p0, p1;
      for (unsigned spins = 0;; ++spins) {
        p0 = __builtin_amdgcn_raw_buffer_load_b128(wsr, src, 0, AUX_SC1);
        p1 = __builtin_amdgcn_raw_buffer_load_b128(wsr, src, 1024, AUX_SC1);
        const unsigned m = (p0[0] & p0[1] & p0[2] & p0[3]) & (p1[0] & p1[1] & p1[2] & p1[3]) & 1u;
        if (__all(m != 0u) || spins > SPIN_CAP) break;
        __builtin_amdgcn_s_sleep(PSLEEP);
      }
      STAMP(10);
      f32x4 t4;
#pragma unroll
      for (int j = 0; j < 4; ++j) {
        float t = val(p0[j]) + val(p1[j]);
        t += dpp<ROR4>(t); t += dpp<ROR8>(t);
        t4[j] = xrow_sum(t);
      }
      if (lane < 4) *(LAS f32x4*)(smem + L_FB + (sw * 4 + lane) * 16) = t4;
      LDSW();
      if (lds_ticket(taddr + 4, vone) == 3u && lane < 8) {
        f32x2 t = *(const LAS f32x2*)(smem + L_FB + lane * 8);
#pragma unroll
        for (int ww = 1; ww < 4; ++ww) t += *(const LAS f32x2*)(smem + L_FB + ww * 64 + lane * 8);
        out[fid * 8 + lane] = pk_bf16(t[0], t[1]);
      }
    }
  } else {
    const int hw = w;
    unsigned short xq[9];
    v4u wqv[9];
    {
      const int r = lane >> 3, row0 = 288 * (sp & 1) + 72 * hw;
      const rsrc_t qr = mk_rsrc(wq, D * 8192);
      const int base = (row0 + r) * 8192 + (kvh * 256 + (sp >> 1) * 64) * 2 + (lane & 7) * 16;
#pragma unroll
      for (int i = 0; i < 9; ++i) wqv[i] = __builtin_amdgcn_raw_buffer_load_b128(qr, base, i * 8 * 8192, AUX_NT);
#pragma unroll
      for (int i = 0; i < 9; ++i) xq[i] = x[b * D + row0 + 8 * i + r];
    }
    const int xi = tid < 144 ? tid : 143;
    const v2u xa = ((const v2u*)x)[xi], xb = ((const v2u*)x)[144 + xi];
    if (tid < 3) { const v4u z = {0u, 0u, 0u, 0u}; *(LAS v4u*)(smem + L_SYNC + tid * 16) = z; }
    __builtin_amdgcn_sched_barrier(0);
    BAR();
    STAMP(1);
    const rsrc_t wsr = mk_rsrc(ws, WS_END);
    v2u evv = {0u, 0u};
    if (hw == 3) {
      evv = __builtin_amdgcn_raw_buffer_load_b64(wsr, WS_E, 0, AUX_SC1);
      const v4u z = {0u, 0u, 0u, 0u};
      __builtin_amdgcn_raw_buffer_store_b128(z, wsr, WS_H1 + bid * H1_STRIDE + voff, 0, AUX_SC1);
      __builtin_amdgcn_raw_buffer_store_b128(z, wsr, WS_H1 + bid * H1_STRIDE + 1024 + voff, 0, AUX_SC1);
      const int h2o = WS_H2 + (b * 36 * 128 + (bid & 127)) * 64 + (lane >> 2) * 8192 + (lane & 3) * 16;
      __builtin_amdgcn_raw_buffer_store_b128(z, wsr, h2o, 0, AUX_SC1);
      __builtin_amdgcn_raw_buffer_store_b128(z, wsr, h2o, 16 * 8192, AUX_SC1);
      int off = h2o + 32 * 8192;
      if (lane >= 16) off = WS_H1 + bid * H1_STRIDE + 2048 + (lane & 1) * 16;
      if (lane >= 32) off = sp == 0 ? WS_CNT + grp * 256 : OOB;
      __builtin_amdgcn_raw_buffer_store_b128(z, wsr, off, 0, AUX_SC1);
    }
    __builtin_amdgcn_sched_barrier(0);
    {
      float acc[8];
#pragma unroll
      for (int j = 0; j < 8; ++j) acc[j] = 0.f;
#pragma unroll
      for (int i = 0; i < 9; ++i) fma8(acc, bflo(xq[i]), wqv[i]);
      const f32x2 r2 = red8<false>(acc);
      if ((lane & 8) == 0) *(LAS f32x2*)(smem + L_QP + (hw * 64 + (lane & 7) * 8 + 4 * (lane >> 5) + 2 * ((lane >> 4) & 1)) * 4) = r2;
      if (tid < 144) {
        v4u xs4;
        xs4[0] = (xa[0] & 0xffffu) | (xb[0] << 16); xs4[1] = (xa[0] >> 16) | (xb[0] & 0xffff0000u);
        xs4[2] = (xa[1] & 0xffffu) | (xb[1] << 16); xs4[3] = (xa[1] >> 16) | (xb[1] & 0xffff0000u);
        *(LAS v4u*)(smem + L_XS + tid * 16) = xs4;
      }
      lds_inc(smem, 5, lane);
      lds_wait<0>(smem, 5, 4u);
    }
    STAMP(12);
    MAKE_MSK();
    v4u WO[16];
    const rsrc_t wor = mk_rsrc(wo, 4096 * D * 2);
    const int wo_lo = (kvh * 256 + 32 * sp + g) * 1152 + hw * 256 + c * 16;
    const int wo_hi = (hw == 0 && c < 8) ? wo_lo + 1024 : OOB;
    if (hw == 3) {
      const unsigned long long ev = ((unsigned long long)(unsigned)__builtin_amdgcn_readfirstlane(evv[1]) << 32) | (unsigned)__builtin_amdgcn_readfirstlane(evv[0]);
      const unsigned long long tag64 = ev * 6364136223846793005ull + 1442695040888963407ull;
      const unsigned tag_hi = (unsigned)(tag64 >> 32);
      const unsigned tag_lo8 = ((unsigned)tag64 >> 24) & 0xffu;
      {
        float s = 0.f;
#pragma unroll
        for (int ww = 0; ww < 4; ++ww) s += *(const LAS float*)(smem + L_QP + (ww * 64 + lane) * 4);
        DRAIN();
        v2u gr; gr[0] = ((__float_as_uint(s) + 0x80u) & 0xffffff00u) | tag_lo8; gr[1] = tag_hi;
        __builtin_amdgcn_raw_buffer_store_b64(gr, wsr, WS_QG + ((grp * 2 + (sp & 1)) * 256 + (sp >> 1) * 64 + lane) * 8, 0, AUX_SC1);
        v2u fl; fl[0] = (unsigned)tag64; fl[1] = tag_hi;
        __builtin_amdgcn_raw_buffer_store_b64(fl, wsr, lane == 0 ? WS_ST + bid * 8 : OOB, 0, AUX_SC1);
      }
      STAMP(13);
      for (unsigned spins = 0;; ++spins) {
        v4u p[4];
#pragma unroll
        for (int i = 0; i < 4; ++i) p[i] = __builtin_amdgcn_raw_buffer_load_b128(wsr, WS_QG + grp * 4096 + lane * 32 + (i & 1) * 16 + (i >> 1) * 2048, 0, AUX_SC1);
        unsigned bad = 0;
#pragma unroll
        for (int i = 0; i < 4; ++i) bad |= (p[i][1] ^ tag_hi) | (p[i][3] ^ tag_hi) | ((p[i][0] ^ tag_lo8) & 0xffu) | ((p[i][2] ^ tag_lo8) & 0xffu);
        if (__all(bad == 0u) || spins > SPIN_CAP) {
          float qv[4];
#pragma unroll
          for (int i = 0; i < 4; ++i)
            qv[i] = __uint_as_float(p[i >> 1][(i & 1) * 2] & 0xffffff00u) + __uint_as_float(p[2 + (i >> 1)][(i & 1) * 2] & 0xffffff00u);
          v2u qq; qq[0] = pk_bf16(qv[0], qv[1]); qq[1] = pk_bf16(qv[2], qv[3]);
          *(LAS v2u*)(smem + L_QS + lane * 8) = qq;
          break;
        }
        __builtin_amdgcn_s_sleep(2);
      }
      lds_inc(smem, 0, lane);
      STAMP(3);
      if (lane == 0) { v2u t2; t2[0] = (unsigned)tag64; t2[1] = tag_hi; *(LAS v2u*)(smem + L_TAG) = t2; }
    }
    v4u G1[11], WD[18];
    const int cm = bid % 36, rb = bid / 36, mat1 = cm >= 18 ? 1 : 0, cblk = cm - 18 * mat1;
    const int hb = 2 * rb + (hw >> 1);
    const int rg0 = hb * 72 / 7, nrg = (hb + 1) * 72 / 7 - rg0;
    const bool has_s1 = bid < 252;
    const int u0 = 36 * (bid & 127);
    unsigned short gwv[9];
    {
      const rsrc_t gr = mk_rsrc(mat1 ? wu : wg, has_s1 ? D * F * 2 : 0);
      const int go = (4 * rg0 + g) * (F * 2) + cblk * 512 + (hw & 1) * 256 + c * 16;
#pragma unroll
      for (int i = 0; i < 11; ++i) G1[i] = __builtin_amdgcn_raw_buffer_load_b128(gr, go, (i < 10 || nrg == 11) ? i * 4 * (F * 2) : OOB, AUX_NT);
      const rsrc_t dr = mk_rsrc(wd, F * D * 2);
      const int wd_lo = (u0 + g) * 1152 + hw * 256 + c * 16;
      const int wd_hi = (hw == 0 && c < 8) ? wd_lo + 1024 : OOB;
#pragma unroll
      for (int i = 0; i < 9; ++i) WD[i] = __builtin_amdgcn_raw_buffer_load_b128(dr, wd_lo, i * 4608, AUX_NT);
#pragma unroll
      for (int i = 9; i < 18; ++i) WD[i] = __builtin_amdgcn_raw_buffer_load_b128(dr, wd_hi, (i - 9) * 4608, AUX_NT);
#pragma unroll
      for (int i = 0; i < 8; ++i) WO[i] = __builtin_amdgcn_raw_buffer_load_b128(wor, wo_lo, i * 4608, AUX_NT);
#pragma unroll
      for (int i = 8; i < 16; ++i) WO[i] = __builtin_amdgcn_raw_buffer_load_b128(wor, wo_hi, (i - 8) * 4608, AUX_NT);
#pragma unroll
      for (int i = 0; i < 9; ++i) gwv[i] = gw[i * 64 + lane];
    }
    __builtin_amdgcn_sched_barrier(0);
    {
      f32x4 ga = {0.f, 0.f, 0.f, 0.f};
#pragma unroll
      for (int i = 0; i < 11; ++i) {
        const int row = 4 * (rg0 + i) + g;
        const unsigned xv = *(const LAS unsigned*)(smem + L_XS + (row < D ? row : 0) * 4);
        const unsigned t_ = hi8 ? ((xv & 0xffff0000u) | (xv >> 16)) : ((xv << 16) | (xv & 0xffffu));
        v4u bb; bb[0] = t_ & msk[0]; bb[1] = t_ & msk[1]; bb[2] = t_ & msk[2]; bb[3] = t_ & msk[3];
        ga = mfma(G1[i], bb, ga);
      }
      if (hw < 2) {
        *(LAS f32x4*)(smem + L_GU + hw * 1024 + voff) = ga;
        lds_inc(smem, 10, lane);
      } else {
        lds_wait<2>(smem, 10, 2u);
        ga = *(const LAS f32x4*)(smem + L_GU + (hw & 1) * 1024 + voff) + ga;
        __builtin_amdgcn_raw_buffer_store_b128(__builtin_bit_cast(v4u, ga), wsr, has_s1 ? WS_G1 + (((cblk * 2 + (hw & 1)) * 2 + mat1) * 7 + rb) * 1024 + voff : OOB, 0, AUX_SC1);
        DRAIN();
        lds_inc(smem, 1, lane);
      }
    }
    float gt;
    {
      float z = 0.f;
#pragma unroll
      for (int i = 0; i < 9; ++i) {
        const unsigned xv = *(const LAS unsigned*)(smem + L_XS + (i * 64 + lane) * 4);
        z += rbf((b ? bfhi(xv) : bflo(xv)) * bflo(gwv[i]));
      }
      z += dpp<XOR1>(z); z += dpp<XOR2>(z); z += dpp<ROR4>(z); z += dpp<ROR8>(z);
      z = rbf(xrow_sum(z));
      gt = rbf(1.f / (1.f + __expf(-z)));
    }
    unsigned tag_lo = 0, tag_hi = 0;
    if (hw == 3) {
      const v2u tg = *(const LAS v2u*)(smem + L_TAG);
      tag_lo = __builtin_amdgcn_readfirstlane(tg[0]); tag_hi = __builtin_amdgcn_readfirstlane(tg[1]);
      lds_wait<4>(smem, 1, 2u);
      v2u fl; fl[0] = tag_lo; fl[1] = tag_hi;
      __builtin_amdgcn_raw_buffer_store_b64(fl, wsr, (lane == 0 && has_s1) ? WS_F1 + cblk * 128 + (mat1 * 7 + rb) * 8 : OOB, 0, AUX_SC1);
      __builtin_amdgcn_s_sleep(HDELAY);
      {
        const int fblk = lane < 32 ? (u0 >> 8) : ((u0 + 35) >> 8);
        const int fo = WS_F1 + fblk * 128 + ((lane & 31) < 14 ? (lane & 31) : 0) * 8;
        for (unsigned spins = 0;; ++spins) {
          const v2u p = __builtin_amdgcn_raw_buffer_load_b64(wsr, fo, 0, AUX_SC1);
          if (__all(((p[1] ^ tag_hi) | (p[0] ^ tag_lo)) == 0u) || spins > SPIN_CAP) break;
          __builtin_amdgcn_s_sleep(64);
        }
      }
      __builtin_amdgcn_fence(__ATOMIC_ACQUIRE, "wavefront");
      const int u = u0 + 4 * c + g;
      const int so = c < 9 ? WS_G1 + (u >> 7) * 14 * 1024 + (((u >> 5) & 3) * 16 + b * 8 + (u & 7)) * 16 + ((u >> 3) & 3) * 4 : OOB;
      float pv[14];
#pragma unroll
      for (int i = 0; i < 14; ++i) pv[i] = __uint_as_float(__builtin_amdgcn_raw_buffer_load_b32(wsr, so, i * 1024, AUX_SC1));
      const float gv = rbf(((pv[0] + pv[1]) + (pv[2] + pv[3])) + ((pv[4] + pv[5]) + pv[6]));
      const float uv = rbf(((pv[7] + pv[8]) + (pv[9] + pv[10])) + ((pv[11] + pv[12]) + pv[13]));
      const float inner = 0.7978845608028654f * (gv + 0.044715f * gv * gv * gv);
      const float hv = rbf(0.5f * gv * (1.f + tanhf(inner))) * uv;
      *(LAS unsigned*)(smem + L_FFN + lane * 4) = pk_bf16(hv, hv);
      lds_inc(smem, 11, lane);
    }
    f32x4 yf = {0.f, 0.f, 0.f, 0.f}, zf = {0.f, 0.f, 0.f, 0.f};
    {
      lds_wait<16>(smem, 11, 1u);
      const unsigned hp = *(const LAS unsigned*)(smem + L_FFN + lane * 4);
#define PRD(i) { const unsigned t_ = dppu<0x150 + (i)>(hp); \
                 v4u bb; bb[0] = t_ & msk[0]; bb[1] = t_ & msk[1]; bb[2] = t_ & msk[2]; bb[3] = t_ & msk[3]; \
                 yf = mfma(WD[i], bb, yf); zf = mfma(WD[9 + (i)], bb, zf); }
      PRD(0) PRD(1) PRD(2) PRD(3) PRD(4) PRD(5) PRD(6) PRD(7) PRD(8)
#undef PRD
      const float og = rbf(1.f - gt);
      yf *= og; zf *= og;
      asm volatile("" : "+v"(yf), "+v"(zf));
    }
    __builtin_amdgcn_sched_barrier(0);
    STAMP(5);
    if (hw == 3) {
      const unsigned long long tag64 = tag_lo;
      lds_wait<32>(smem, 7, 4u);
      if (lane == 0) {
        v2u fl; fl[0] = (unsigned)tag64; fl[1] = tag_hi;
        __builtin_amdgcn_raw_buffer_store_b64(fl, wsr, WS_SF + bid * 64, 0, AUX_SC1);
      }
      for (unsigned spins = 0;; ++spins) {
        const v2u p = __builtin_amdgcn_raw_buffer_load_b64(wsr, WS_SF + (grp * 8 + (lane & 7)) * 64, 0, AUX_SC1);
        if (__all(((p[1] ^ tag_hi) | (p[0] ^ (unsigned)tag64)) == 0u) || spins > SPIN_CAP) break;
        __builtin_amdgcn_s_sleep(32);
      }
      lds_inc(smem, 9, lane);
      if (is_final) {
        for (unsigned spins = 0;; ++spins) {
          unsigned bad = 0;
#pragma unroll
          for (int i = 0; i < 2; ++i) {
            const v4u p = __builtin_amdgcn_raw_buffer_load_b128(wsr, WS_ST + i * 1024 + voff, 0, AUX_SC1);
            bad |= (p[1] ^ tag_hi) | (p[0] ^ tag_lo) | (p[3] ^ tag_hi) | (p[2] ^ tag_lo);
          }
          if (__all(bad == 0u) || spins > SPIN_CAP) break;
          __builtin_amdgcn_s_sleep(64);
        }
        if (fid == 0 && lane == 0) {
          v2u e; e[0] = tag_lo; e[1] = tag_hi;
          __builtin_amdgcn_raw_buffer_store_b64(e, wsr, WS_E, 0, AUX_SC1);
        }
        lds_inc(smem, 6, lane);
      }
    }
    __builtin_amdgcn_sched_barrier(0);
    DRAIN();
    lds_wait<32>(smem, 2, 1u);
    if (!H1P && hw == 3) gather_o(false);
    lds_wait<0>(smem, 4, 1u);
    const unsigned pp = *(const LAS unsigned*)(smem + L_OS + lane * 4);
    STAMP(9);
    {
      f32x4 y0 = {0.f, 0.f, 0.f, 0.f}, y1 = {0.f, 0.f, 0.f, 0.f}, z0 = {0.f, 0.f, 0.f, 0.f};
#define PRJ(i, y) { const unsigned t_ = dppu<0x150 + (i)>(pp); v4u bb; bb[0] = t_ & msk[0]; bb[1] = t_ & msk[1]; bb[2] = t_ & msk[2]; bb[3] = t_ & msk[3]; \
                    y = mfma(WO[i], bb, y); z0 = mfma(WO[8 + (i)], bb, z0); }
      PRJ(0, y0) PRJ(1, y1) PRJ(2, y0) PRJ(3, y1) PRJ(4, y0) PRJ(5, y1) PRJ(6, y0) PRJ(7, y1)
#undef PRJ
      y0 = (y0 + y1) * gt + yf;
      z0 = z0 * gt + zf;
      const int dst = WS_H2 + ((b * 36 + hw * 8 + 2 * g) * 128 + (bid & 127)) * 64 + c * 4;
      __builtin_amdgcn_raw_buffer_store_b32(__float_as_uint(hi8 ? y0[1] : y0[0]) | 1u, wsr, dst, 0, AUX_SC1);
      __builtin_amdgcn_raw_buffer_store_b32(__float_as_uint(hi8 ? y0[3] : y0[2]) | 1u, wsr, dst, 8192, AUX_SC1);
      if (hw == 0) {
        const int dz = g < 2 ? dst + 32 * 8192 : OOB;
        __builtin_amdgcn_raw_buffer_store_b32(__float_as_uint(hi8 ? z0[1] : z0[0]) | 1u, wsr, dz, 0, AUX_SC1);
        __builtin_amdgcn_raw_buffer_store_b32(__float_as_uint(hi8 ? z0[3] : z0[2]) | 1u, wsr, dz, 8192, AUX_SC1);
      }
    }
    STAMP(15);
  }
  STAMP(11);
  STAMP_FLUSH();
}

extern "C" void kernel_launch(void* const* d_in, const int* in_sizes, int n_in, void* d_out, int out_size, void* d_ws, size_t ws_size,
                              hipStream_t stream) {
  k_fused<<<256, NTHR, 0, stream>>>((const unsigned short*)d_in[0], (const unsigned char*)d_in[1], (const unsigned char*)d_in[2],
                                    (const unsigned char*)d_in[3], (const unsigned char*)d_in[4], (const unsigned char*)d_in[6],
                                    (const unsigned char*)d_in[7], (const unsigned char*)d_in[8], (const unsigned short*)d_in[9],
                                    (unsigned char*)d_ws, (unsigned*)d_out);
}
```

```cpp
#include <hip/hip_runtime.h>
#include <hip/hip_bf16.h>
#include <stdint.h>

typedef unsigned v4u __attribute__((ext_vector_type(4)));
typedef unsigned v2u __attribute__((ext_vector_type(2)));
typedef float f32x4 __attribute__((ext_vector_type(4)));
typedef float f32x2 __attribute__((ext_vector_type(2)));
typedef short bf16x8 __attribute__((ext_vector_type(8)));
typedef __bf16 bf16x2 __attribute__((ext_vector_type(2)));
typedef __amdgpu_buffer_rsrc_t rsrc_t;
#define LAS __attribute__((address_space(3)))

constexpr int D = 576, SK = 32768, F = 4608;
constexpr int NW = 8, NTHR = NW * 64;
#define NSU_V 116
constexpr unsigned NSU = NSU_V;
constexpr unsigned ND = 8 * (128 - NSU);
constexpr unsigned NONE = 1024;
static_assert(NSU % 4 == 0 && (NSU / 4) % 2 == 1, "static V steps per wave: odd");
constexpr int AUX_NT = 2, AUX_SC1 = 16;
constexpr int OOB = 0x40000000;
constexpr unsigned SPIN_CAP = 200000u;
#define H1P 1
#define NE 2
#define STAG 4
#define H1MASK 1
#define TOUCH 0
#define LATE0 0
#define WTS 0
#define WTD 4
#define HDELAY 100
#define SWAPR 0
#define SBATCH 1
#define PACET 1750
#define NEARD 64
#define PSLEEP 2

constexpr int WS_E = 0;
constexpr int WS_CNT = 4096;
constexpr int WS_QG = 16384;
constexpr int WS_H1 = WS_QG + 131072;
constexpr int H1_STRIDE = 2176;
constexpr int WS_H2 = WS_H1 + 256 * H1_STRIDE;
constexpr int WS_ST = WS_H2 + 144 * 4096;
constexpr int WS_SF = WS_ST + 4096;
constexpr int WS_SG = WS_SF + 256 * 64;
constexpr int WS_F1 = WS_SG + 32 * 1024 * 128;
constexpr int WS_G1 = WS_F1 + 4096;
constexpr int WS_END = WS_G1 + 36 * 14 * 1024;

constexpr int L_XS = 0;
constexpr int L_QP = 2304;
constexpr int L_QS = L_QP + 2048;
constexpr int L_MRG = L_QS + 512;
constexpr int L_ML = L_MRG + 8192;
constexpr int L_OS = L_ML + 96;
constexpr int L_GU = L_OS + 256;
constexpr int L_TAG = L_GU + 2048;
constexpr int L_FFN = L_TAG + 16;
constexpr int L_FB = L_FFN + 256;
constexpr int L_SYNC = L_FB + 256;
constexpr int L_SC = L_SYNC + 48;
constexpr int L_SB = L_SC + 16384;
constexpr int L_END = L_SB + 4096;
constexpr int LDS_BYTES = 96 * 1024;
static_assert(L_END <= LDS_BYTES, "lds");

__device__ __forceinline__ float bflo(unsigned u) { return __builtin_bit_cast(float, u << 16); }
__device__ __forceinline__ float bfhi(unsigned u) { return __builtin_bit_cast(float, u & 0xffff0000u); }
__device__ __forceinline__ unsigned pk_bf16(float lo, float hi) {
  bf16x2 v; v[0] = (__bf16)lo; v[1] = (__bf16)hi; return __builtin_bit_cast(unsigned, v);
}
__device__ __forceinline__ float rbf(float f) { return bflo(pk_bf16(f, f)); }
__device__ __forceinline__ float dot2(unsigned a, unsigned b, float c) {
  return __builtin_amdgcn_fdot2_f32_bf16(__builtin_bit_cast(bf16x2, a), __builtin_bit_cast(bf16x2, b), c, false);
}
template <int CTRL> __device__ __forceinline__ float dpp(float x) {
  return __builtin_bit_cast(float, __builtin_amdgcn_mov_dpp(__builtin_bit_cast(int, x), CTRL, 0xf, 0xf, true));
}
constexpr int XOR1 = 0xB1, XOR2 = 0x4E, HMIR = 0x141, ROR4 = 0x124, ROR8 = 0x128;
__device__ __forceinline__ f32x4 mfma(v4u a, v4u b, f32x4 c) {
  return __builtin_amdgcn_mfma_f32_16x16x32_bf16(__builtin_bit_cast(bf16x8, a), __builtin_bit_cast(bf16x8, b), c, 0, 0, 0);
}
__device__ __forceinline__ float swap16_sum(float a, float b) {
  auto s = __builtin_amdgcn_permlane16_swap(__float_as_uint(a), __float_as_uint(b), false, false);
  return __uint_as_float(s[0]) + __uint_as_float(s[1]);
}
__device__ __forceinline__ float swap32_sum(float a, float b) {
  auto s = __builtin_amdgcn_permlane32_swap(__float_as_uint(a), __float_as_uint(b), false, false);
  return __uint_as_float(s[0]) + __uint_as_float(s[1]);
}
__device__ __forceinline__ float xrow_max(float x) {
  auto s = __builtin_amdgcn_permlane16_swap(__float_as_uint(x), __float_as_uint(x), false, false);
  x = fmaxf(__uint_as_float(s[0]), __uint_as_float(s[1]));
  auto t = __builtin_amdgcn_permlane32_swap(__float_as_uint(x), __float_as_uint(x), false, false);
  return fmaxf(__uint_as_float(t[0]), __uint_as_float(t[1]));
}
__device__ __forceinline__ float xrow_sum(float x) { x = swap16_sum(x, x); return swap32_sum(x, x); }
__device__ __forceinline__ rsrc_t mk_rsrc(const void* p, int bytes) { return __builtin_amdgcn_make_buffer_rsrc((void*)p, 0, bytes, 0x00020000); }
#define BAR() do { asm volatile("s_waitcnt lgkmcnt(0)" ::: "memory"); __builtin_amdgcn_s_barrier(); asm volatile("" ::: "memory"); } while (0)
#define DRAIN() do { __builtin_amdgcn_s_waitcnt(0x0F70); asm volatile("s_waitcnt vmcnt(0)" ::: "memory"); } while (0)
#define WAITV(n) do { __builtin_amdgcn_s_waitcnt(0x0F70 | ((n) & 15) | (((n) >> 4) << 14)); asm volatile("" ::: "memory"); } while (0)
#define LDSW() asm volatile("s_waitcnt lgkmcnt(0)" ::: "memory")

#define STAMP(i) do {} while (0)
#define STAMP_FLUSH() do {} while (0)
constexpr float SCALE = 0.08838834764831845f, L2E = 1.4426950408889634f;

typedef double f64x4 __attribute__((ext_vector_type(4)));
struct St { float m; double l; f64x4 acc; };
__device__ __forceinline__ void pin(St& st) { asm volatile("" : "+v"(st.m), "+v"(st.l), "+v"(st.acc) :: "memory"); }
__device__ __forceinline__ v4u diag(float v, const v4u& msk) {
  const unsigned pp = pk_bf16(v, v);
  v4u bb; bb[0] = pp & msk[0]; bb[1] = pp & msk[1]; bb[2] = pp & msk[2]; bb[3] = pp & msk[3];
  return bb;
}

__device__ __forceinline__ v2u scores(const v4u* KB, const v4u& q0, const v4u& q1, bool hi8) {
  float s[4];
#pragma unroll
  for (int t = 0; t < 4; ++t) {
    float a0 = dot2(KB[t][0], q0[0], 0.f), a1 = dot2(KB[t][0], q1[0], 0.f);
#pragma unroll
    for (int k = 1; k < 4; ++k) { a0 = dot2(KB[t][k], q0[k], a0); a1 = dot2(KB[t][k], q1[k], a1); }
    const float keep = hi8 ? a1 : a0, send = hi8 ? a0 : a1;
    float v = keep + dpp<ROR8>(send);
    v += dpp<HMIR>(v); v += dpp<XOR2>(v); v += dpp<XOR1>(v);
    s[t] = v;
  }
  const unsigned u0 = pk_bf16(s[0], s[1]), u1 = pk_bf16(s[2], s[3]);
  v2u r; r[0] = pk_bf16(bflo(u0) * SCALE, bfhi(u0) * SCALE); r[1] = pk_bf16(bflo(u1) * SCALE, bfhi(u1) * SCALE);
  return r;
}
__device__ __forceinline__ void pv_unit(const v4u (&VB)[8], const v4u sc, const v4u& msk, St& st) {
  float s[8];
#pragma unroll
  for (int t = 0; t < 4; ++t) { s[2 * t] = bflo(sc[t]); s[2 * t + 1] = bfhi(sc[t]); }
  float tm = fmaxf(fmaxf(fmaxf(s[0], s[1]), fmaxf(s[2], s[3])), fmaxf(fmaxf(s[4], s[5]), fmaxf(s[6], s[7])));
  tm = xrow_max(tm);
  const float tl = tm * L2E, mt = __builtin_ceilf(tl), fr = tl - mt;
  const float mn = fmaxf(st.m, mt);
  const double alpha = (double)__builtin_amdgcn_exp2f(st.m - mn);
  const float beta = __builtin_amdgcn_exp2f(mt - mn);
  st.m = mn;
  float p[8];
#pragma unroll
  for (int t = 0; t < 8; ++t) p[t] = __builtin_amdgcn_exp2f(fmaf(s[t] - tm, L2E, fr));
  f32x4 ta = {0.f, 0.f, 0.f, 0.f};
#pragma unroll
  for (int t = 0; t < 8; ++t) ta = mfma(VB[t], diag(p[t], msk), ta);
  st.l = __builtin_fma(st.l, alpha, (double)((((p[0] + p[1]) + (p[2] + p[3])) + ((p[4] + p[5]) + (p[6] + p[7]))) * beta));
#pragma unroll
  for (int r = 0; r < 4; ++r) st.acc[r] = __builtin_fma(st.acc[r], alpha, (double)(ta[r] * beta));
}
__device__ __forceinline__ void issue(v4u (&B)[8], rsrc_t r, int voff, unsigned unit) {
  const int so = (int)(unit * 8192u);
#pragma unroll
  for (int t = 0; t < 4; ++t) B[t] = __builtin_amdgcn_raw_buffer_load_b128(r, voff + t * 1024, so, AUX_NT);
#pragma unroll
  for (int t = 0; t < 4; ++t) B[4 + t] = __builtin_amdgcn_raw_buffer_load_b128(r, voff + t * 1024, so + 4096, AUX_NT);
}
__device__ __forceinline__ unsigned atomic_inc(rsrc_t r, int voff0, bool on, int so) {
  return (unsigned)__builtin_amdgcn_raw_ptr_buffer_atomic_add_i32(1, r, voff0, on ? so : OOB, 0);
}
__device__ __forceinline__ unsigned lds_ticket(unsigned lds_addr_v, unsigned vone) {
  unsigned long long keep; unsigned r, s;
  asm volatile("s_mov_b64 %1, exec\n\ts_mov_b64 exec, 1\n\tds_add_rtn_u32 %0, %3, %4\n\ts_mov_b64 exec, %1\n\ts_waitcnt lgkmcnt(0)\n\tv_readfirstlane_b32 %2, %0\n\ts_nop 1"
               : "=&v"(r), "=&s"(keep), "=s"(s) : "v"(lds_addr_v), "v"(vone) : "memory");
  return s;
}
template <int CTRL> __device__ __forceinline__ unsigned dppu(unsigned x) { return (unsigned)__builtin_amdgcn_mov_dpp((int)x, CTRL, 0xf, 0xf, true); }
template <int CTRL> __device__ __forceinline__ double dppd(double x) {
  const v2u u = __builtin_bit_cast(v2u, x);
  v2u r; r[0] = dppu<CTRL>(u[0]); r[1] = dppu<CTRL>(u[1]);
  return __builtin_bit_cast(double, r);
}
__device__ __forceinline__ double xrow_sumd(double x) {
  v2u u = __builtin_bit_cast(v2u, x), a, b2;
  { auto s0 = __builtin_amdgcn_permlane16_swap(u[0], u[0], false, false); auto s1 = __builtin_amdgcn_permlane16_swap(u[1], u[1], false, false);
    a[0] = s0[0]; a[1] = s1[0]; b2[0] = s0[1]; b2[1] = s1[1]; }
  x = __builtin_bit_cast(double, a) + __builtin_bit_cast(double, b2);
  u = __builtin_bit_cast(v2u, x);
  { auto s0 = __builtin_amdgcn_permlane32_swap(u[0], u[0], false, false); auto s1 = __builtin_amdgcn_permlane32_swap(u[1], u[1], false, false);
    a[0] = s0[0]; a[1] = s1[0]; b2[0] = s0[1]; b2[1] = s1[1]; }
  return __builtin_bit_cast(double, a) + __builtin_bit_cast(double, b2);
}
__device__ __forceinline__ void lds_inc(unsigned char* smem, int idx, int lane) {
  LDSW();
  if (lane == 0) __hip_atomic_fetch_add((LAS unsigned*)(LAS unsigned char*)smem + (L_SYNC / 4 + idx), 1u, __ATOMIC_RELAXED, __HIP_MEMORY_SCOPE_WORKGROUP);
}
template <int SLEEP> __device__ __forceinline__ void lds_wait(unsigned char* smem, int idx, unsigned want) {
#pragma unroll 1
  for (unsigned spins = 0; spins < 8000000u / (1 + SLEEP); ++spins) {
    const unsigned v = __builtin_amdgcn_readfirstlane(*((volatile LAS unsigned*)(LAS unsigned char*)smem + (L_SYNC / 4 + idx)));
    if (v >= want) break;
    if (SLEEP) __builtin_amdgcn_s_sleep(SLEEP);
  }
  asm volatile("" ::: "memory");
}

__device__ __forceinline__ void fma8(float (&acc)[8], float xv, const v4u& w) {
#pragma unroll
  for (int k = 0; k < 4; ++k) { acc[2 * k] = fmaf(xv, bflo(w[k]), acc[2 * k]); acc[2 * k + 1] = fmaf(xv, bfhi(w[k]), acc[2 * k + 1]); }
}
template <bool BIT2> __device__ __forceinline__ f32x2 red8(const float (&acc)[8]) {
  float a[4];
#pragma unroll
  for (int j = 0; j < 4; ++j) a[j] = swap32_sum(acc[j], acc[j + 4]);
  float b0 = swap16_sum(a[0], a[2]), b1 = swap16_sum(a[1], a[3]);
  if (BIT2) { b0 += dpp<ROR4>(b0); b1 += dpp<ROR4>(b1); }
  b0 += dpp<ROR8>(b0); b1 += dpp<ROR8>(b1);
  f32x2 r; r[0] = b0; r[1] = b1; return r;
}
__device__ __forceinline__ float val(unsigned u) { return __uint_as_float(u & ~1u); }
__device__ __forceinline__ bool all4(const v4u& p) { return (p[0] & p[1] & p[2] & p[3] & 1u) != 0u; }

__global__ void __launch_bounds__(NTHR, 2)
k_fused(const unsigned short* __restrict__ x, const unsigned char* Kc, const unsigned char* Vc,
        const unsigned char* __restrict__ wq, const unsigned char* wo, const unsigned char* wg,
        const unsigned char* wu, const unsigned char* wd, const unsigned short* __restrict__ gw,
        unsigned char* ws, unsigned* out) {
  __shared__ __attribute__((aligned(16))) unsigned char smem[LDS_BYTES];
  const int tid = SWAPR ? (threadIdx.x ^ 256) : threadIdx.x, lane = tid & 63, w = __builtin_amdgcn_readfirstlane(tid >> 6);
  const int bid = blockIdx.x, grp = bid >> 3, sp = bid & 7, b = grp >> 4, kvh = grp & 15;
  const int g = lane >> 4, c = lane & 15;
  unsigned tsv = 0xffffffffu; (void)tsv;
  STAMP(0);
  const int voff = lane * 16;
  const int ridx = (grp + 4 * sp) & 31;
  const bool is_final = ridx < 9;
  const int fid = sp * 9 + ridx;
  const bool hi8 = (c & 8) != 0;
#define MAKE_MSK() v4u msk; _Pragma("unroll") for (int d = 0; d < 4; ++d) msk[d] = (((c & 7) >> 1) == d) ? ((c & 1) ? 0xffff0000u : 0x0000ffffu) : 0u;

  auto gather_o = [&](bool watch_flag) {
    const rsrc_t wsr = mk_rsrc(ws, WS_END);
    const int o_acc = WS_H1 + (grp * 8 + 4 * (c >> 3)) * H1_STRIDE + (32 * sp + 8 * g + (c & 7)) * 8;
    const int o_ml = WS_H1 + (grp * 8 + (c & 7)) * H1_STRIDE + 2048 + (sp >> 2) * 16;
    v2u pv[4];
    v4u pm = {0u, 0u, 0u, 0u};
#pragma unroll
    for (int i = 0; i < 4; ++i) pv[i] = v2u{0u, 0u};
    for (unsigned spins = 0;; ++spins) {
      if (H1MASK) {
        if (!(pv[0][0] & pv[1][0] & pv[2][0] & pv[3][0] & 1u)) {
#pragma unroll
          for (int i = 0; i < 4; ++i) pv[i] = __builtin_amdgcn_raw_buffer_load_b64(wsr, o_acc, i * H1_STRIDE, AUX_SC1);
        }
        if (!(pm[0] & pm[2] & 1u)) pm = __builtin_amdgcn_raw_buffer_load_b128(wsr, o_ml, 0, AUX_SC1);
      } else {
#pragma unroll
        for (int i = 0; i < 4; ++i) pv[i] = __builtin_amdgcn_raw_buffer_load_b64(wsr, o_acc, i * H1_STRIDE, AUX_SC1);
        pm = __builtin_amdgcn_raw_buffer_load_b128(wsr, o_ml, 0, AUX_SC1);
      }
      if (__all((pv[0][0] & pv[1][0] & pv[2][0] & pv[3][0] & pm[0] & pm[2] & 1u) != 0u) || spins > SPIN_CAP) break;
      if (watch_flag && __builtin_amdgcn_readfirstlane(*((volatile LAS unsigned*)(smem + L_SYNC) + 4)) != 0u) return;
      __builtin_amdgcn_s_sleep(PSLEEP);
    }
    STAMP(8);
    const float Ms = __uint_as_float(pm[1]);
    float Mx = fmaxf(Ms, dpp<XOR1>(Ms)); Mx = fmaxf(Mx, dpp<XOR2>(Mx)); Mx = fmaxf(Mx, dpp<HMIR>(Mx));
    const float wt = __builtin_amdgcn_exp2f(Ms - Mx);
    v2u lv; lv[0] = pm[2]; lv[1] = pm[3];
    double den = (double)wt * __builtin_bit_cast(double, lv);
    den += dppd<XOR1>(den); den += dppd<XOR2>(den); den += dppd<HMIR>(den);
    double num = 0.0;
#define ACC1(i) { const float wl_ = dpp<0x150 + (i)>(wt), wh_ = dpp<0x154 + (i)>(wt); num = __builtin_fma((double)(hi8 ? wh_ : wl_), __builtin_bit_cast(double, pv[i]), num); }
    ACC1(0) ACC1(1) ACC1(2) ACC1(3)
#undef ACC1
    num += dppd<ROR8>(num);
    const float ov = (float)num / (float)den;
    *(LAS unsigned*)(smem + L_OS + lane * 4) = pk_bf16(ov, ov);
    lds_inc(smem, 4, lane);
  };

  if (w >= 4) {
    const int sw = w - 4, stid = tid - 256;
    const rsrc_t kr = mk_rsrc(Kc + (size_t)grp * (SK * 256), SK * 256);
    const rsrc_t vr = mk_rsrc(Vc + (size_t)grp * (SK * 256), SK * 256);
    const rsrc_t wsr = mk_rsrc(ws, WS_END);
    const unsigned ubase = (unsigned)(sp * 128 + sw);
    v4u Ba[8], Bb[8], Be[NE][8];
    if (!LATE0) issue(Be[0], kr, voff, ubase);
    __builtin_amdgcn_sched_barrier(0);
    asm volatile("" ::: "memory");
    __builtin_amdgcn_s_barrier();
    asm volatile("" ::: "memory");
    if (LATE0) issue(Be[0], kr, voff, ubase);
    __builtin_amdgcn_sched_barrier(0);
    STAMP(1);
    MAKE_MSK();
#pragma unroll
    for (int e = 1; e < NE; ++e) {
      DRAIN();
      issue(Be[e], kr, voff, ubase + 4 * e);
      __builtin_amdgcn_sched_barrier(0);
    }
    DRAIN();
    issue(Ba, kr, voff, ubase + 4 * NE);
    __builtin_amdgcn_sched_barrier(0);
    DRAIN();
    issue(Bb, kr, voff, ubase + 4 * NE + 4);
    __builtin_amdgcn_sched_barrier(0);
    STAMP(2);
    lds_wait<1>(smem, 0, 1u);
    STAMP(3);
    const v4u q0 = *(const LAS v4u*)(smem + L_QS + c * 16), q1 = *(const LAS v4u*)(smem + L_QS + 256 + c * 16);
    const int sc_off = ((c >> 3) * 4 + g) * 16;
    const int sg_st = (c & 7) == 0 ? WS_SG + grp * 131072 + sc_off : OOB;
#define KSC(B) const v2u s0_ = scores(&B[0], q0, q1, hi8), s1_ = scores(&B[4], q0, q1, hi8);                        \
      v4u sc_; sc_[0] = s0_[0]; sc_[1] = s0_[1]; sc_[2] = s1_[0]; sc_[3] = s1_[1];
#define KSTEP(B, i)                                                                                                \
    {                                                                                                              \
      KSC(B)                                                                                                       \
      if ((c & 7) == 0) *(LAS v4u*)(smem + L_SC + (4 * (i) + sw) * 128 + sc_off) = sc_;                            \
      asm volatile("" ::: "memory");                                                                               \
      __builtin_amdgcn_sched_barrier(0);                                                                           \
    }
    int nsl = 0;
    unsigned uid = 0;
#define SFLUSH()                                                                                                   \
    {                                                                                                              \
      const v4u v_ = *(const LAS v4u*)(smem + L_SB + sw * 1024 + voff);                                            \
      __builtin_amdgcn_raw_buffer_store_b128(v_, wsr, (lane >> 3) < nsl ? WS_SG + grp * 131072 + (int)(uid * 128u) + (lane & 7) * 16 : OOB, 0, AUX_SC1); \
      nsl = 0;                                                                                                     \
    }
#define KSTEPG(B, u)                                                                                               \
    {                                                                                                              \
      KSC(B)                                                                                                       \
      if ((c & 7) == 0) *(LAS v4u*)(smem + L_SB + sw * 1024 + nsl * 128 + sc_off) = sc_;                           \
      uid = (lane >> 3) == nsl ? (u) : uid;                                                                        \
      ++nsl;                                                                                                       \
      if (nsl == 8) SFLUSH()                                                                                       \
      asm volatile("" ::: "memory");                                                                               \
      __builtin_amdgcn_sched_barrier(0);                                                                           \
    }
    const int cnt0 = lane == 0 ? WS_CNT + grp * 256 : OOB;
    unsigned vone = 1u, a = 0u;
    unsigned taddr = (unsigned)(size_t)(LAS unsigned char*)(smem + L_SYNC + 28);
    asm volatile("" : "+v"(vone), "+v"(taddr));
    KSTEP(Ba, NE);
    issue(Ba, kr, voff, ubase + 4 * (NE + 2));
    __builtin_amdgcn_sched_barrier(0);
#pragma unroll
    for (int e = 0; e < NE; ++e) KSTEP(Be[e], e);
    KSTEP(Bb, NE + 1);
    static_assert(NE % 2 == 0, "K steps left: odd");
#define PACE() do { if (PACET) { while ((int)((unsigned)__builtin_amdgcn_s_memrealtime() * 16u - due) < 0) __builtin_amdgcn_s_sleep(1); due += PACET; } } while (0)
    unsigned due = (unsigned)__builtin_amdgcn_s_memrealtime() * 16u;
#pragma unroll 1
    for (int i = NE + 2; i < (int)(NSU / 4) - 1; i += 2) {
      PACE();
      DRAIN();
      issue(Bb, kr, voff, ubase + 4 * (i + 1));
      __builtin_amdgcn_sched_barrier(0);
      KSTEP(Ba, i);
      PACE();
      DRAIN();
      a = atomic_inc(wsr, cnt0, i + 3 >= (int)(NSU / 4), 0);
      issue(Ba, kr, voff, ubase + 4 * (i + 2));
      __builtin_amdgcn_sched_barrier(0);
      KSTEP(Bb, i + 1);
    }
#define MAPD(d) (((d) & 7u) * 128u + NSU + ((d) >> 3))
#define WAITA() DRAIN()
    {
      unsigned ua, ub;
      WAITA();
      unsigned d = __builtin_amdgcn_readfirstlane(a);
      a = atomic_inc(wsr, cnt0, d < ND, 0);
      ub = MAPD(d);
      issue(Bb, d < ND ? kr : vr, voff, d < ND ? ub : ubase);
      __builtin_amdgcn_sched_barrier(0);
      KSTEP(Ba, NSU / 4 - 1);
      bool in_b = true;
      while (d < ND) {
        WAITA();
        d = __builtin_amdgcn_readfirstlane(a);
        a = atomic_inc(wsr, cnt0, d < ND, 0);
        ua = MAPD(d);
        issue(Ba, d < ND ? kr : vr, voff, d < ND ? ua : ubase);
        __builtin_amdgcn_sched_barrier(0);
        KSTEPG(Bb, ub);
        if (d >= ND) { in_b = false; break; }
        WAITA();
        d = __builtin_amdgcn_readfirstlane(a);
        a = atomic_inc(wsr, cnt0, d < ND, 0);
        ub = MAPD(d);
        issue(Bb, d < ND ? kr : vr, voff, d < ND ? ub : ubase);
        __builtin_amdgcn_sched_barrier(0);
        KSTEPG(Ba, ua);
      }
      STAMP(4);
      SFLUSH()
      DRAIN();
      if (in_b) {
#pragma unroll
        for (int t = 0; t < 8; ++t) Ba[t] = Bb[t];
      }
    }
#undef KSTEP
#undef KSTEPG
#undef SFLUSH
#undef KSC
    St st; st.m = -INFINITY; st.l = 0.0; st.acc = f64x4{0.0, 0.0, 0.0, 0.0};
#define LSC(i) (*(const LAS v4u*)(smem + L_SC + (4 * (i) + sw) * 128 + sc_off))
    lds_inc(smem, 7, lane);
    due = (unsigned)__builtin_amdgcn_s_memrealtime() * 16u;
#pragma unroll 1
    for (int i = 0; i < (int)(NSU / 4) - 1; i += 2) {
      PACE();
      DRAIN();
      issue(Bb, vr, voff, ubase + 4 * (i + 1));
      __builtin_amdgcn_sched_barrier(0);
      pv_unit(Ba, LSC(i), msk, st); pin(st);
      __builtin_amdgcn_sched_barrier(0);
      PACE();
      DRAIN();
      a = atomic_inc(wsr, cnt0, i + 3 >= (int)(NSU / 4), 4);
      issue(Ba, vr, voff, ubase + 4 * (i + 2));
      __builtin_amdgcn_sched_barrier(0);
      pv_unit(Bb, LSC(i + 1), msk, st); pin(st);
      __builtin_amdgcn_sched_barrier(0);
    }
    lds_wait<2>(smem, 9, 1u);
    v4u sa = LSC(NSU / 4 - 1), sb;
#undef LSC
    const int sg_ld = WS_SG + grp * 131072 + sc_off;
#define NEXT(nx)                                                                                                   \
    {                                                                                                              \
      const unsigned d_ = __builtin_amdgcn_readfirstlane(a);                                                       \
      nx = d_ < ND ? (d_ & 7u) * 128u + NSU + (d_ >> 3) : NONE;                                                    \
      if (d_ + NEARD >= ND) *((volatile LAS unsigned*)(smem + L_SYNC) + 2) = 1u;                                   \
      a = atomic_inc(wsr, cnt0, d_ < ND, 4);                                                               \
    }
    while (true) {
      unsigned nx;
      DRAIN();
      NEXT(nx);
      sb = __builtin_amdgcn_raw_buffer_load_b128(wsr, sg_ld, nx < NONE ? (int)(nx * 128u) : OOB, AUX_SC1);
      issue(Bb, vr, voff, nx);
      __builtin_amdgcn_sched_barrier(0);
      pv_unit(Ba, sa, msk, st); pin(st);
      __builtin_amdgcn_sched_barrier(0);
      if (nx >= NONE) break;
      DRAIN();
      NEXT(nx);
      sa = __builtin_amdgcn_raw_buffer_load_b128(wsr, sg_ld, nx < NONE ? (int)(nx * 128u) : OOB, AUX_SC1);
      issue(Ba, vr, voff, nx);
      __builtin_amdgcn_sched_barrier(0);
      pv_unit(Bb, sb, msk, st); pin(st);
      __builtin_amdgcn_sched_barrier(0);
      if (nx >= NONE) break;
    }
#undef NEXT
    STAMP(6);
    {
      const double l = xrow_sumd(st.l);
      const int h = c >> 3;
#pragma unroll
      for (int r = 0; r < 4; ++r) {
        const int dim = 8 * (4 * g + r) + (c & 7);
        *(LAS double*)(smem + L_MRG + (sw * 256 + h * 128 + (dim & 96) + (dim & 3) * 8 + ((dim >> 2) & 7)) * 8) = st.acc[r];
      }
      if (g == 0 && (c & 7) == 0) { *(LAS float*)(smem + L_ML + (h * 4 + sw) * 4) = st.m; *(LAS double*)(smem + L_ML + 32 + (h * 4 + sw) * 8) = l; }
    }
    lds_inc(smem, 3, lane);
    lds_wait<0>(smem, 3, 4u);
    STAMP(7);
    {
      const int hh = sw >> 1;
      const f32x4 m4 = *(const LAS f32x4*)(smem + L_ML + hh * 16);
      const f64x4 l4 = *(const LAS f64x4*)(smem + L_ML + 32 + hh * 32);
      double a4[4];
#pragma unroll
      for (int ww = 0; ww < 4; ++ww) a4[ww] = *(const LAS double*)(smem + L_MRG + (ww * 256 + stid) * 8);
      const float M = fmaxf(fmaxf(m4[0], m4[1]), fmaxf(m4[2], m4[3]));
      double L = 0.0, o = 0.0;
#pragma unroll
      for (int ww = 0; ww < 4; ++ww) {
        const double wt = (double)__builtin_amdgcn_exp2f(m4[ww] - M);
        L = __builtin_fma(wt, l4[ww], L); o = __builtin_fma(wt, a4[ww], o);
      }
      v2u ov = __builtin_bit_cast(v2u, o); ov[0] |= 1u;
      __builtin_amdgcn_raw_buffer_store_b64(ov, wsr, WS_H1 + bid * H1_STRIDE + stid * 8, 0, AUX_SC1);
      if ((stid & 127) == 0) {
        const v2u lv = __builtin_bit_cast(v2u, L);
        v4u ml; ml[0] = 1u; ml[1] = __float_as_uint(M); ml[2] = lv[0] | 1u; ml[3] = lv[1];
        __builtin_amdgcn_raw_buffer_store_b128(ml, wsr, WS_H1 + bid * H1_STRIDE + 2048 + hh * 16, 0, AUX_SC1);
      }
    }
    STAMP(14);
    if (H1P) {
      for (int i = 0; i < sw; ++i) __builtin_amdgcn_s_sleep(STAG);
      gather_o(true);
    }
    if (is_final) {
      const int src = WS_H2 + fid * 8192 + sw * 2048 + voff;
      lds_wait<8>(smem, 6, 1u);
      v4u p0, p1;
      for (unsigned spins = 0;; ++spins) {
        p0 = __builtin_amdgcn_raw_buffer_load_b128(wsr, src, 0, AUX_SC1);
        p1 = __builtin_amdgcn_raw_buffer_load_b128(wsr, src, 1024, AUX_SC1);
        const unsigned m = (p0[0] & p0[1] & p0[2] & p0[3]) & (p1[0] & p1[1] & p1[2] & p1[3]) & 1u;
        if (__all(m != 0u) || spins > SPIN_CAP) break;
        __builtin_amdgcn_s_sleep(PSLEEP);
      }
      STAMP(10);
      f32x4 t4;
#pragma unroll
      for (int j = 0; j < 4; ++j) {
        float t = val(p0[j]) + val(p1[j]);
        t += dpp<ROR4>(t); t += dpp<ROR8>(t);
        t4[j] = xrow_sum(t);
      }
      if (lane < 4) *(LAS f32x4*)(smem + L_FB + (sw * 4 + lane) * 16) = t4;
      LDSW();
      if (lds_ticket(taddr + 4, vone) == 3u && lane < 8) {
        f32x2 t = *(const LAS f32x2*)(smem + L_FB + lane * 8);
#pragma unroll
        for (int ww = 1; ww < 4; ++ww) t += *(const LAS f32x2*)(smem + L_FB + ww * 64 + lane * 8);
        out[fid * 8 + lane] = pk_bf16(t[0], t[1]);
      }
    }
  } else {
    const int hw = w;
    unsigned short xq[9];
    v4u wqv[9];
    {
      const int r = lane >> 3, row0 = 288 * (sp & 1) + 72 * hw;
      const rsrc_t qr = mk_rsrc(wq, D * 8192);
      const int base = (row0 + r) * 8192 + (kvh * 256 + (sp >> 1) * 64) * 2 + (lane & 7) * 16;
#pragma unroll
      for (int i = 0; i < 9; ++i) wqv[i] = __builtin_amdgcn_raw_buffer_load_b128(qr, base, i * 8 * 8192, AUX_NT);
#pragma unroll
      for (int i = 0; i < 9; ++i) xq[i] = x[b * D + row0 + 8 * i + r];
    }
    const int xi = tid < 144 ? tid : 143;
    const v2u xa = ((const v2u*)x)[xi], xb = ((const v2u*)x)[144 + xi];
    if (tid < 3) { const v4u z = {0u, 0u, 0u, 0u}; *(LAS v4u*)(smem + L_SYNC + tid * 16) = z; }
    __builtin_amdgcn_sched_barrier(0);
    BAR();
    STAMP(1);
    const rsrc_t wsr = mk_rsrc(ws, WS_END);
    v2u evv = {0u, 0u};
    if (hw == 3) {
      evv = __builtin_amdgcn_raw_buffer_load_b64(wsr, WS_E, 0, AUX_SC1);
      const v4u z = {0u, 0u, 0u, 0u};
      __builtin_amdgcn_raw_buffer_store_b128(z, wsr, WS_H1 + bid * H1_STRIDE + voff, 0, AUX_SC1);
      __builtin_amdgcn_raw_buffer_store_b128(z, wsr, WS_H1 + bid * H1_STRIDE + 1024 + voff, 0, AUX_SC1);
      const int h2o = WS_H2 + (b * 36 * 128 + (bid & 127)) * 64 + (lane >> 2) * 8192 + (lane & 3) * 16;
      __builtin_amdgcn_raw_buffer_store_b128(z, wsr, h2o, 0, AUX_SC1);
      __builtin_amdgcn_raw_buffer_store_b128(z, wsr, h2o, 16 * 8192, AUX_SC1);
      int off = h2o + 32 * 8192;
      if (lane >= 16) off = WS_H1 + bid * H1_STRIDE + 2048 + (lane & 1) * 16;
      if (lane >= 32) off = sp == 0 ? WS_CNT + grp * 256 : OOB;
      __builtin_amdgcn_raw_buffer_store_b128(z, wsr, off, 0, AUX_SC1);
    }
    __builtin_amdgcn_sched_barrier(0);
    {
      float acc[8];
#pragma unroll
      for (int j = 0; j < 8; ++j) acc[j] = 0.f;
#pragma unroll
      for (int i = 0; i < 9; ++i) fma8(acc, bflo(xq[i]), wqv[i]);
      const f32x2 r2 = red8<false>(acc);
      if ((lane & 8) == 0) *(LAS f32x2*)(smem + L_QP + (hw * 64 + (lane & 7) * 8 + 4 * (lane >> 5) + 2 * ((lane >> 4) & 1)) * 4) = r2;
      if (tid < 144) {
        v4u xs4;
        xs4[0] = (xa[0] & 0xffffu) | (xb[0] << 16); xs4[1] = (xa[0] >> 16) | (xb[0] & 0xffff0000u);
        xs4[2] = (xa[1] & 0xffffu) | (xb[1] << 16); xs4[3] = (xa[1] >> 16) | (xb[1] & 0xffff0000u);
        *(LAS v4u*)(smem + L_XS + tid * 16) = xs4;
      }
      lds_inc(smem, 5, lane);
      lds_wait<0>(smem, 5, 4u);
    }
    STAMP(12);
    MAKE_MSK();
    v4u WO[16];
    const rsrc_t wor = mk_rsrc(wo, 4096 * D * 2);
    const int wo_lo = (kvh * 256 + 32 * sp + g) * 1152 + hw * 256 + c * 16;
    const int wo_hi = (hw == 0 && c < 8) ? wo_lo + 1024 : OOB;
    if (hw == 3) {
      const unsigned long long ev = ((unsigned long long)(unsigned)__builtin_amdgcn_readfirstlane(evv[1]) << 32) | (unsigned)__builtin_amdgcn_readfirstlane(evv[0]);
      const unsigned long long tag64 = ev * 6364136223846793005ull + 1442695040888963407ull;
      const unsigned tag_hi = (unsigned)(tag64 >> 32);
      const unsigned tag_lo8 = ((unsigned)tag64 >> 24) & 0xffu;
      {
        float s = 0.f;
#pragma unroll
        for (int ww = 0; ww < 4; ++ww) s += *(const LAS float*)(smem + L_QP + (ww * 64 + lane) * 4);
        DRAIN();
        v2u gr; gr[0] = ((__float_as_uint(s) + 0x80u) & 0xffffff00u) | tag_lo8; gr[1] = tag_hi;
        __builtin_amdgcn_raw_buffer_store_b64(gr, wsr, WS_QG + ((grp * 2 + (sp & 1)) * 256 + (sp >> 1) * 64 + lane) * 8, 0, AUX_SC1);
        v2u fl; fl[0] = (unsigned)tag64; fl[1] = tag_hi;
        __builtin_amdgcn_raw_buffer_store_b64(fl, wsr, lane == 0 ? WS_ST + bid * 8 : OOB, 0, AUX_SC1);
      }
      STAMP(13);
      for (unsigned spins = 0;; ++spins) {
        v4u p[4];
#pragma unroll
        for (int i = 0; i < 4; ++i) p[i] = __builtin_amdgcn_raw_buffer_load_b128(wsr, WS_QG + grp * 4096 + lane * 32 + (i & 1) * 16 + (i >> 1) * 2048, 0, AUX_SC1);
        unsigned bad = 0;
#pragma unroll
        for (int i = 0; i < 4; ++i) bad |= (p[i][1] ^ tag_hi) | (p[i][3] ^ tag_hi) | ((p[i][0] ^ tag_lo8) & 0xffu) | ((p[i][2] ^ tag_lo8) & 0xffu);
        if (__all(bad == 0u) || spins > SPIN_CAP) {
          float qv[4];
#pragma unroll
          for (int i = 0; i < 4; ++i)
            qv[i] = __uint_as_float(p[i >> 1][(i & 1) * 2] & 0xffffff00u) + __uint_as_float(p[2 + (i >> 1)][(i & 1) * 2] & 0xffffff00u);
          v2u qq; qq[0] = pk_bf16(qv[0], qv[1]); qq[1] = pk_bf16(qv[2], qv[3]);
          *(LAS v2u*)(smem + L_QS + lane * 8) = qq;
          break;
        }
        __builtin_amdgcn_s_sleep(2);
      }
      lds_inc(smem, 0, lane);
      STAMP(3);
      if (lane == 0) { v2u t2; t2[0] = (unsigned)tag64; t2[1] = tag_hi; *(LAS v2u*)(smem + L_TAG) = t2; }
    }
    v4u G1[11], WD[18];
    const int cm = bid % 36, rb = bid / 36, mat1 = cm >= 18 ? 1 : 0, cblk = cm - 18 * mat1;
    const int hb = 2 * rb + (hw >> 1);
    const int rg0 = hb * 72 / 7, nrg = (hb + 1) * 72 / 7 - rg0;
    const bool has_s1 = bid < 252;
    const int u0 = 36 * (bid & 127);
    unsigned short gwv[9];
    {
      const rsrc_t gr = mk_rsrc(mat1 ? wu : wg, has_s1 ? D * F * 2 : 0);
      const int go = (4 * rg0 + g) * (F * 2) + cblk * 512 + (hw & 1) * 256 + c * 16;
#pragma unroll
      for (int i = 0; i < 11; ++i) G1[i] = __builtin_amdgcn_raw_buffer_load_b128(gr, go, (i < 10 || nrg == 11) ? i * 4 * (F * 2) : OOB, AUX_NT);
      const rsrc_t dr = mk_rsrc(wd, F * D * 2);
      const int wd_lo = (u0 + g) * 1152 + hw * 256 + c * 16;
      const int wd_hi = (hw == 0 && c < 8) ? wd_lo + 1024 : OOB;
#pragma unroll
      for (int i = 0; i < 9; ++i) WD[i] = __builtin_amdgcn_raw_buffer_load_b128(dr, wd_lo, i * 4608, AUX_NT);
#pragma unroll
      for (int i = 9; i < 18; ++i) WD[i] = __builtin_amdgcn_raw_buffer_load_b128(dr, wd_hi, (i - 9) * 4608, AUX_NT);
#pragma unroll
      for (int i = 0; i < 8; ++i) WO[i] = __builtin_amdgcn_raw_buffer_load_b128(wor, wo_lo, i * 4608, AUX_NT);
#pragma unroll
      for (int i = 8; i < 16; ++i) WO[i] = __builtin_amdgcn_raw_buffer_load_b128(wor, wo_hi, (i - 8) * 4608, AUX_NT);
#pragma unroll
      for (int i = 0; i < 9; ++i) gwv[i] = gw[i * 64 + lane];
    }
    __builtin_amdgcn_sched_barrier(0);
    {
      f32x4 ga = {0.f, 0.f, 0.f, 0.f};
#pragma unroll
      for (int i = 0; i < 11; ++i) {
        const int row = 4 * (rg0 + i) + g;
        const unsigned xv = *(const LAS unsigned*)(smem + L_XS + (row < D ? row : 0) * 4);
        const unsigned t_ = hi8 ? ((xv & 0xffff0000u) | (xv >> 16)) : ((xv << 16) | (xv & 0xffffu));
        v4u bb; bb[0] = t_ & msk[0]; bb[1] = t_ & msk[1]; bb[2] = t_ & msk[2]; bb[3] = t_ & msk[3];
        ga = mfma(G1[i], bb, ga);
      }
      if (hw < 2) {
        *(LAS f32x4*)(smem + L_GU + hw * 1024 + voff) = ga;
        lds_inc(smem, 10, lane);
      } else {
        lds_wait<2>(smem, 10, 2u);
        ga = *(const LAS f32x4*)(smem + L_GU + (hw & 1) * 1024 + voff) + ga;
        __builtin_amdgcn_raw_buffer_store_b128(__builtin_bit_cast(v4u, ga), wsr, has_s1 ? WS_G1 + (((cblk * 2 + (hw & 1)) * 2 + mat1) * 7 + rb) * 1024 + voff : OOB, 0, AUX_SC1);
        DRAIN();
        lds_inc(smem, 1, lane);
      }
    }
    float gt;
    {
      float z = 0.f;
#pragma unroll
      for (int i = 0; i < 9; ++i) {
        const unsigned xv = *(const LAS unsigned*)(smem + L_XS + (i * 64 + lane) * 4);
        z += rbf((b ? bfhi(xv) : bflo(xv)) * bflo(gwv[i]));
      }
      z += dpp<XOR1>(z); z += dpp<XOR2>(z); z += dpp<ROR4>(z); z += dpp<ROR8>(z);
      z = rbf(xrow_sum(z));
      gt = rbf(1.f / (1.f + __expf(-z)));
    }
    unsigned tag_lo = 0, tag_hi = 0;
    if (hw == 3) {
      const v2u tg = *(const LAS v2u*)(smem + L_TAG);
      tag_lo = __builtin_amdgcn_readfirstlane(tg[0]); tag_hi = __builtin_amdgcn_readfirstlane(tg[1]);
      lds_wait<4>(smem, 1, 2u);
      v2u fl; fl[0] = tag_lo; fl[1] = tag_hi;
      __builtin_amdgcn_raw_buffer_store_b64(fl, wsr, (lane == 0 && has_s1) ? WS_F1 + cblk * 128 + (mat1 * 7 + rb) * 8 : OOB, 0, AUX_SC1);
      __builtin_amdgcn_s_sleep(HDELAY);
      {
        const int fblk = lane < 32 ? (u0 >> 8) : ((u0 + 35) >> 8);
        const int fo = WS_F1 + fblk * 128 + ((lane & 31) < 14 ? (lane & 31) : 0) * 8;
        for (unsigned spins = 0;; ++spins) {
          const v2u p = __builtin_amdgcn_raw_buffer_load_b64(wsr, fo, 0, AUX_SC1);
          if (__all(((p[1] ^ tag_hi) | (p[0] ^ tag_lo)) == 0u) || spins > SPIN_CAP) break;
          __builtin_amdgcn_s_sleep(64);
        }
      }
      __builtin_amdgcn_fence(__ATOMIC_ACQUIRE, "wavefront");
      const int u = u0 + 4 * c + g;
      const int so = c < 9 ? WS_G1 + (u >> 7) * 14 * 1024 + (((u >> 5) & 3) * 16 + b * 8 + (u & 7)) * 16 + ((u >> 3) & 3) * 4 : OOB;
      float pv[14];
#pragma unroll
      for (int i = 0; i < 14; ++i) pv[i] = __uint_as_float(__builtin_amdgcn_raw_buffer_load_b32(wsr, so, i * 1024, AUX_SC1));
      const float gv = rbf(((pv[0] + pv[1]) + (pv[2] + pv[3])) + ((pv[4] + pv[5]) + pv[6]));
      const float uv = rbf(((pv[7] + pv[8]) + (pv[9] + pv[10])) + ((pv[11] + pv[12]) + pv[13]));
      const float inner = 0.7978845608028654f * (gv + 0.044715f * gv * gv * gv);
      const float hv = rbf(0.5f * gv * (1.f + tanhf(inner))) * uv;
      *(LAS unsigned*)(smem + L_FFN + lane * 4) = pk_bf16(hv, hv);
      lds_inc(smem, 11, lane);
    }
    f32x4 yf = {0.f, 0.f, 0.f, 0.f}, zf = {0.f, 0.f, 0.f, 0.f};
    {
      lds_wait<16>(smem, 11, 1u);
      const unsigned hp = *(const LAS unsigned*)(smem + L_FFN + lane * 4);
#define PRD(i) { const unsigned t_ = dppu<0x150 + (i)>(hp); \
                 v4u bb; bb[0] = t_ & msk[0]; bb[1] = t_ & msk[1]; bb[2] = t_ & msk[2]; bb[3] = t_ & msk[3]; \
                 yf = mfma(WD[i], bb, yf); zf = mfma(WD[9 + (i)], bb, zf); }
      PRD(0) PRD(1) PRD(2) PRD(3) PRD(4) PRD(5) PRD(6) PRD(7) PRD(8)
#undef PRD
      const float og = rbf(1.f - gt);
      yf *= og; zf *= og;
      asm volatile("" : "+v"(yf), "+v"(zf));
    }
    __builtin_amdgcn_sched_barrier(0);
    STAMP(5);
    if (hw == 3) {
      const unsigned long long tag64 = tag_lo;
      lds_wait<32>(smem, 7, 4u);
      if (lane == 0) {
        v2u fl; fl[0] = (unsigned)tag64; fl[1] = tag_hi;
        __builtin_amdgcn_raw_buffer_store_b64(fl, wsr, WS_SF + bid * 64, 0, AUX_SC1);
      }
      for (unsigned spins = 0;; ++spins) {
        const v2u p = __builtin_amdgcn_raw_buffer_load_b64(wsr, WS_SF + (grp * 8 + (lane & 7)) * 64, 0, AUX_SC1);
        if (__all(((p[1] ^ tag_hi) | (p[0] ^ (unsigned)tag64)) == 0u) || spins > SPIN_CAP) break;
        __builtin_amdgcn_s_sleep(32);
      }
      lds_inc(smem, 9, lane);
      if (is_final) {
        for (unsigned spins = 0;; ++spins) {
          unsigned bad = 0;
#pragma unroll
          for (int i = 0; i < 2; ++i) {
            const v4u p = __builtin_amdgcn_raw_buffer_load_b128(wsr, WS_ST + i * 1024 + voff, 0, AUX_SC1);
            bad |= (p[1] ^ tag_hi) | (p[0] ^ tag_lo) | (p[3] ^ tag_hi) | (p[2] ^ tag_lo);
          }
          if (__all(bad == 0u) || spins > SPIN_CAP) break;
          __builtin_amdgcn_s_sleep(64);
        }
        if (fid == 0 && lane == 0) {
          v2u e; e[0] = tag_lo; e[1] = tag_hi;
          __builtin_amdgcn_raw_buffer_store_b64(e, wsr, WS_E, 0, AUX_SC1);
        }
        lds_inc(smem, 6, lane);
      }
    }
    __builtin_amdgcn_sched_barrier(0);
    DRAIN();
    lds_wait<32>(smem, 2, 1u);
    if (!H1P && hw == 3) gather_o(false);
    lds_wait<0>(smem, 4, 1u);
    const unsigned pp = *(const LAS unsigned*)(smem + L_OS + lane * 4);
    STAMP(9);
    {
      f32x4 y0 = {0.f, 0.f, 0.f, 0.f}, y1 = {0.f, 0.f, 0.f, 0.f}, z0 = {0.f, 0.f, 0.f, 0.f};
#define PRJ(i, y) { const unsigned t_ = dppu<0x150 + (i)>(pp); v4u bb; bb[0] = t_ & msk[0]; bb[1] = t_ & msk[1]; bb[2] = t_ & msk[2]; bb[3] = t_ & msk[3]; \
                    y = mfma(WO[i], bb, y); z0 = mfma(WO[8 + (i)], bb, z0); }
      PRJ(0, y0) PRJ(1, y1) PRJ(2, y0) PRJ(3, y1) PRJ(4, y0) PRJ(5, y1) PRJ(6, y0) PRJ(7, y1)
#undef PRJ
      y0 = (y0 + y1) * gt + yf;
      z0 = z0 * gt + zf;
      const int dst = WS_H2 + ((b * 36 + hw * 8 + 2 * g) * 128 + (bid & 127)) * 64 + c * 4;
      __builtin_amdgcn_raw_buffer_store_b32(__float_as_uint(hi8 ? y0[1] : y0[0]) | 1u, wsr, dst, 0, AUX_SC1);
      __builtin_amdgcn_raw_buffer_store_b32(__float_as_uint(hi8 ? y0[3] : y0[2]) | 1u, wsr, dst, 8192, AUX_SC1);
      if (hw == 0) {
        const int dz = g < 2 ? dst + 32 * 8192 : OOB;
        __builtin_amdgcn_raw_buffer_store_b32(__float_as_uint(hi8 ? z0[1] : z0[0]) | 1u, wsr, dz, 0, AUX_SC1);
        __builtin_amdgcn_raw_buffer_store_b32(__float_as_uint(hi8 ? z0[3] : z0[2]) | 1u, wsr, dz, 8192, AUX_SC1);
      }
    }
    STAMP(15);
  }
  STAMP(11);
  STAMP_FLUSH();
}

extern "C" void kernel_launch(void* const* d_in, const int* in_sizes, int n_in, void* d_out, int out_size, void* d_ws, size_t ws_size,
                              hipStream_t stream) {
  k_fused<<<256, NTHR, 0, stream>>>((const unsigned short*)d_in[0], (const unsigned char*)d_in[1], (const unsigned char*)d_in[2],
                                    (const unsigned char*)d_in[3], (const unsigned char*)d_in[4], (const unsigned char*)d_in[6],
                                    (const unsigned char*)d_in[7], (const unsigned char*)d_in[8], (const unsigned short*)d_in[9],
                                    (unsigned char*)d_ws, (unsigned*)d_out);
}
```

```cpp
#include <hip/hip_runtime.h>
#include <hip/hip_bf16.h>
#include <stdint.h>

typedef unsigned v4u __attribute__((ext_vector_type(4)));
typedef unsigned v2u __attribute__((ext_vector_type(2)));
typedef float f32x4 __attribute__((ext_vector_type(4)));
typedef float f32x2 __attribute__((ext_vector_type(2)));
typedef short bf16x8 __attribute__((ext_vector_type(8)));
typedef __bf16 bf16x2 __attribute__((ext_vector_type(2)));
typedef __amdgpu_buffer_rsrc_t rsrc_t;
#define LAS __attribute__((address_space(3)))

constexpr int D = 576, SK = 32768, F = 4608;
constexpr int NW = 8, NTHR = NW * 64;
#define NSU_V 124
constexpr unsigned NSU = NSU_V;
constexpr unsigned ND = 8 * (128 - NSU);
constexpr unsigned NONE = 1024;
static_assert(NSU % 4 == 0 && (NSU / 4) % 2 == 1, "static V steps per wave: odd");
constexpr int AUX_NT = 2, AUX_SC1 = 16;
constexpr int OOB = 0x40000000;
constexpr unsigned SPIN_CAP = 200000u;
#define H1P 1
#define NE 2
#define STAG 4
#define H1MASK 1
#define TOUCH 0
#define LATE0 0
#define WTS 0
#define WTD 4
#define HDELAY 100
#define SWAPR 0
#define SBATCH 1
#define PACET 1756
#define PCRED 3500
#define KDYN 1
#define PTOUCH 1
#define NEARD 64
#define PSLEEP 2

constexpr int WS_E = 0;
constexpr int WS_CNT = 4096;
constexpr int WS_QG = 16384;
constexpr int WS_H1 = WS_QG + 131072;
constexpr int H1_STRIDE = 2176;
constexpr int WS_H2 = WS_H1 + 256 * H1_STRIDE;
constexpr int WS_ST = WS_H2 + 144 * 4096;
constexpr int WS_SF = WS_ST + 4096;
constexpr int WS_SG = WS_SF + 256 * 64;
constexpr int WS_F1 = WS_SG + 32 * 1024 * 128;
constexpr int WS_G1 = WS_F1 + 4096;
constexpr int WS_END = WS_G1 + 36 * 14 * 1024;

constexpr int L_XS = 0;
constexpr int L_QP = 2304;
constexpr int L_QS = L_QP + 2048;
constexpr int L_MRG = L_QS + 512;
constexpr int L_ML = L_MRG + 8192;
constexpr int L_OS = L_ML + 96;
constexpr int L_GU = L_OS + 256;
constexpr int L_TAG = L_GU + 2048;
constexpr int L_FFN = L_TAG + 16;
constexpr int L_FB = L_FFN + 256;
constexpr int L_SYNC = L_FB + 256;
constexpr int L_SC = L_SYNC + 48;
constexpr int L_SB = L_SC + 16384;
constexpr int L_END = L_SB + 4096;
constexpr int LDS_BYTES = 96 * 1024;
static_assert(L_END <= LDS_BYTES, "lds");

__device__ __forceinline__ float bflo(unsigned u) { return __builtin_bit_cast(float, u << 16); }
__device__ __forceinline__ float bfhi(unsigned u) { return __builtin_bit_cast(float, u & 0xffff0000u); }
__device__ __forceinline__ unsigned pk_bf16(float lo, float hi) {
  bf16x2 v; v[0] = (__bf16)lo; v[1] = (__bf16)hi; return __builtin_bit_cast(unsigned, v);
}
__device__ __forceinline__ float rbf(float f) { return bflo(pk_bf16(f, f)); }
__device__ __forceinline__ float dot2(unsigned a, unsigned b, float c) {
  return __builtin_amdgcn_fdot2_f32_bf16(__builtin_bit_cast(bf16x2, a), __builtin_bit_cast(bf16x2, b), c, false);
}
template <int CTRL> __device__ __forceinline__ float dpp(float x) {
  return __builtin_bit_cast(float, __builtin_amdgcn_mov_dpp(__builtin_bit_cast(int, x), CTRL, 0xf, 0xf, true));
}
constexpr int XOR1 = 0xB1, XOR2 = 0x4E, HMIR = 0x141, ROR4 = 0x124, ROR8 = 0x128;
__device__ __forceinline__ f32x4 mfma(v4u a, v4u b, f32x4 c) {
  return __builtin_amdgcn_mfma_f32_16x16x32_bf16(__builtin_bit_cast(bf16x8, a), __builtin_bit_cast(bf16x8, b), c, 0, 0, 0);
}
__device__ __forceinline__ float swap16_sum(float a, float b) {
  auto s = __builtin_amdgcn_permlane16_swap(__float_as_uint(a), __float_as_uint(b), false, false);
  return __uint_as_float(s[0]) + __uint_as_float(s[1]);
}
__device__ __forceinline__ float swap32_sum(float a, float b) {
  auto s = __builtin_amdgcn_permlane32_swap(__float_as_uint(a), __float_as_uint(b), false, false);
  return __uint_as_float(s[0]) + __uint_as_float(s[1]);
}
__device__ __forceinline__ float xrow_max(float x) {
  auto s = __builtin_amdgcn_permlane16_swap(__float_as_uint(x), __float_as_uint(x), false, false);
  x = fmaxf(__uint_as_float(s[0]), __uint_as_float(s[1]));
  auto t = __builtin_amdgcn_permlane32_swap(__float_as_uint(x), __float_as_uint(x), false, false);
  return fmaxf(__uint_as_float(t[0]), __uint_as_float(t[1]));
}
__device__ __forceinline__ float xrow_sum(float x) { x = swap16_sum(x, x); return swap32_sum(x, x); }
__device__ __forceinline__ rsrc_t mk_rsrc(const void* p, int bytes) { return __builtin_amdgcn_make_buffer_rsrc((void*)p, 0, bytes, 0x00020000); }
#define BAR() do { asm volatile("s_waitcnt lgkmcnt(0)" ::: "memory"); __builtin_amdgcn_s_barrier(); asm volatile("" ::: "memory"); } while (0)
#define DRAIN() do { __builtin_amdgcn_s_waitcnt(0x0F70); asm volatile("s_waitcnt vmcnt(0)" ::: "memory"); } while (0)
#define WAITV(n) do { __builtin_amdgcn_s_waitcnt(0x0F70 | ((n) & 15) | (((n) >> 4) << 14)); asm volatile("" ::: "memory"); } while (0)
#define LDSW() asm volatile("s_waitcnt lgkmcnt(0)" ::: "memory")

#define STAMP(i) do {} while (0)
#define STAMP_FLUSH() do {} while (0)
constexpr float SCALE = 0.08838834764831845f, L2E = 1.4426950408889634f;

typedef double f64x4 __attribute__((ext_vector_type(4)));
struct St { float m; double l; f64x4 acc; };
__device__ __forceinline__ void pin(St& st) { asm volatile("" : "+v"(st.m), "+v"(st.l), "+v"(st.acc) :: "memory"); }
__device__ __forceinline__ v4u diag(float v, const v4u& msk) {
  const unsigned pp = pk_bf16(v, v);
  v4u bb; bb[0] = pp & msk[0]; bb[1] = pp & msk[1]; bb[2] = pp & msk[2]; bb[3] = pp & msk[3];
  return bb;
}

__device__ __forceinline__ v2u scores(const v4u* KB, const v4u& q0, const v4u& q1, bool hi8) {
  float s[4];
#pragma unroll
  for (int t = 0; t < 4; ++t) {
    float a0 = dot2(KB[t][0], q0[0], 0.f), a1 = dot2(KB[t][0], q1[0], 0.f);
#pragma unroll
    for (int k = 1; k < 4; ++k) { a0 = dot2(KB[t][k], q0[k], a0); a1 = dot2(KB[t][k], q1[k], a1); }
    const float keep = hi8 ? a1 : a0, send = hi8 ? a0 : a1;
    float v = keep + dpp<ROR8>(send);
    v += dpp<HMIR>(v); v += dpp<XOR2>(v); v += dpp<XOR1>(v);
    s[t] = v;
  }
  const unsigned u0 = pk_bf16(s[0], s[1]), u1 = pk_bf16(s[2], s[3]);
  v2u r; r[0] = pk_bf16(bflo(u0) * SCALE, bfhi(u0) * SCALE); r[1] = pk_bf16(bflo(u1) * SCALE, bfhi(u1) * SCALE);
  return r;
}
__device__ __forceinline__ void pv_unit(const v4u (&VB)[8], const v4u sc, const v4u& msk, St& st) {
  float s[8];
#pragma unroll
  for (int t = 0; t < 4; ++t) { s[2 * t] = bflo(sc[t]); s[2 * t + 1] = bfhi(sc[t]); }
  float tm = fmaxf(fmaxf(fmaxf(s[0], s[1]), fmaxf(s[2], s[3])), fmaxf(fmaxf(s[4], s[5]), fmaxf(s[6], s[7])));
  tm = xrow_max(tm);
  const float tl = tm * L2E, mt = __builtin_ceilf(tl), fr = tl - mt;
  const float mn = fmaxf(st.m, mt);
  const double alpha = (double)__builtin_amdgcn_exp2f(st.m - mn);
  const float beta = __builtin_amdgcn_exp2f(mt - mn);
  st.m = mn;
  float p[8];
#pragma unroll
  for (int t = 0; t < 8; ++t) p[t] = __builtin_amdgcn_exp2f(fmaf(s[t] - tm, L2E, fr));
  f32x4 ta = {0.f, 0.f, 0.f, 0.f};
#pragma unroll
  for (int t = 0; t < 8; ++t) ta = mfma(VB[t], diag(p[t], msk), ta);
  st.l = __builtin_fma(st.l, alpha, (double)((((p[0] + p[1]) + (p[2] + p[3])) + ((p[4] + p[5]) + (p[6] + p[7]))) * beta));
#pragma unroll
  for (int r = 0; r < 4; ++r) st.acc[r] = __builtin_fma(st.acc[r], alpha, (double)(ta[r] * beta));
}
__device__ __forceinline__ void issue(v4u (&B)[8], rsrc_t r, int voff, unsigned unit) {
  const int so = (int)(unit * 8192u);
#pragma unroll
  for (int t = 0; t < 4; ++t) B[t] = __builtin_amdgcn_raw_buffer_load_b128(r, voff + t * 1024, so, AUX_NT);
#pragma unroll
  for (int t = 0; t < 4; ++t) B[4 + t] = __builtin_amdgcn_raw_buffer_load_b128(r, voff + t * 1024, so + 4096, AUX_NT);
}
__device__ __forceinline__ unsigned atomic_inc(rsrc_t r, int voff0, bool on, int so) {
  return (unsigned)__builtin_amdgcn_raw_ptr_buffer_atomic_add_i32(1, r, voff0, on ? so : OOB, 0);
}
__device__ __forceinline__ unsigned lds_ticket(unsigned lds_addr_v, unsigned vone) {
  unsigned long long keep; unsigned r, s;
  asm volatile("s_mov_b64 %1, exec\n\ts_mov_b64 exec, 1\n\tds_add_rtn_u32 %0, %3, %4\n\ts_mov_b64 exec, %1\n\ts_waitcnt lgkmcnt(0)\n\tv_readfirstlane_b32 %2, %0\n\ts_nop 1"
               : "=&v"(r), "=&s"(keep), "=s"(s) : "v"(lds_addr_v), "v"(vone) : "memory");
  return s;
}
template <int CTRL> __device__ __forceinline__ unsigned dppu(unsigned x) { return (unsigned)__builtin_amdgcn_mov_dpp((int)x, CTRL, 0xf, 0xf, true); }
template <int CTRL> __device__ __forceinline__ double dppd(double x) {
  const v2u u = __builtin_bit_cast(v2u, x);
  v2u r; r[0] = dppu<CTRL>(u[0]); r[1] = dppu<CTRL>(u[1]);
  return __builtin_bit_cast(double, r);
}
__device__ __forceinline__ double xrow_sumd(double x) {
  v2u u = __builtin_bit_cast(v2u, x), a, b2;
  { auto s0 = __builtin_amdgcn_permlane16_swap(u[0], u[0], false, false); auto s1 = __builtin_amdgcn_permlane16_swap(u[1], u[1], false, false);
    a[0] = s0[0]; a[1] = s1[0]; b2[0] = s0[1]; b2[1] = s1[1]; }
  x = __builtin_bit_cast(double, a) + __builtin_bit_cast(double, b2);
  u = __builtin_bit_cast(v2u, x);
  { auto s0 = __builtin_amdgcn_permlane32_swap(u[0], u[0], false, false); auto s1 = __builtin_amdgcn_permlane32_swap(u[1], u[1], false, false);
    a[0] = s0[0]; a[1] = s1[0]; b2[0] = s0[1]; b2[1] = s1[1]; }
  return __builtin_bit_cast(double, a) + __builtin_bit_cast(double, b2);
}
__device__ __forceinline__ void lds_inc(unsigned char* smem, int idx, int lane) {
  LDSW();
  if (lane == 0) __hip_atomic_fetch_add((LAS unsigned*)(LAS unsigned char*)smem + (L_SYNC / 4 + idx), 1u, __ATOMIC_RELAXED, __HIP_MEMORY_SCOPE_WORKGROUP);
}
template <int SLEEP> __device__ __forceinline__ void lds_wait(unsigned char* smem, int idx, unsigned want) {
#pragma unroll 1
  for (unsigned spins = 0; spins < 8000000u / (1 + SLEEP); ++spins) {
    const unsigned v = __builtin_amdgcn_readfirstlane(*((volatile LAS unsigned*)(LAS unsigned char*)smem + (L_SYNC / 4 + idx)));
    if (v >= want) break;
    if (SLEEP) __builtin_amdgcn_s_sleep(SLEEP);
  }
  asm volatile("" ::: "memory");
}

__device__ __forceinline__ void fma8(float (&acc)[8], float xv, const v4u& w) {
#pragma unroll
  for (int k = 0; k < 4; ++k) { acc[2 * k] = fmaf(xv, bflo(w[k]), acc[2 * k]); acc[2 * k + 1] = fmaf(xv, bfhi(w[k]), acc[2 * k + 1]); }
}
template <bool BIT2> __device__ __forceinline__ f32x2 red8(const float (&acc)[8]) {
  float a[4];
#pragma unroll
  for (int j = 0; j < 4; ++j) a[j] = swap32_sum(acc[j], acc[j + 4]);
  float b0 = swap16_sum(a[0], a[2]), b1 = swap16_sum(a[1], a[3]);
  if (BIT2) { b0 += dpp<ROR4>(b0); b1 += dpp<ROR4>(b1); }
  b0 += dpp<ROR8>(b0); b1 += dpp<ROR8>(b1);
  f32x2 r; r[0] = b0; r[1] = b1; return r;
}
__device__ __forceinline__ float val(unsigned u) { return __uint_as_float(u & ~1u); }
__device__ __forceinline__ bool all4(const v4u& p) { return (p[0] & p[1] & p[2] & p[3] & 1u) != 0u; }

__global__ void __launch_bounds__(NTHR, 2)
k_fused(const unsigned short* __restrict__ x, const unsigned char* Kc, const unsigned char* Vc,
        const unsigned char* __restrict__ wq, const unsigned char* wo, const unsigned char* wg,
        const unsigned char* wu, const unsigned char* wd, const unsigned short* __restrict__ gw,
        unsigned char* ws, unsigned* out) {
  __shared__ __attribute__((aligned(16))) unsigned char smem[LDS_BYTES];
  const int tid = SWAPR ? (threadIdx.x ^ 256) : threadIdx.x, lane = tid & 63, w = __builtin_amdgcn_readfirstlane(tid >> 6);
  const int bid = blockIdx.x, grp = bid >> 3, sp = bid & 7, b = grp >> 4, kvh = grp & 15;
  const int g = lane >> 4, c = lane & 15;
  unsigned tsv = 0xffffffffu; (void)tsv;
  STAMP(0);
  const int voff = lane * 16;
  const int ridx = (grp + 4 * sp) & 31;
  const bool is_final = ridx < 9;
  const int fid = sp * 9 + ridx;
  const bool hi8 = (c & 8) != 0;
#define MAKE_MSK() v4u msk; _Pragma("unroll") for (int d = 0; d < 4; ++d) msk[d] = (((c & 7) >> 1) == d) ? ((c & 1) ? 0xffff0000u : 0x0000ffffu) : 0u;

  auto gather_o = [&](bool watch_flag) {
    const rsrc_t wsr = mk_rsrc(ws, WS_END);
    const int o_acc = WS_H1 + (grp * 8 + 4 * (c >> 3)) * H1_STRIDE + (32 * sp + 8 * g + (c & 7)) * 8;
    const int o_ml = WS_H1 + (grp * 8 + (c & 7)) * H1_STRIDE + 2048 + (sp >> 2) * 16;
    v2u pv[4];
    v4u pm = {0u, 0u, 0u, 0u};
#pragma unroll
    for (int i = 0; i < 4; ++i) pv[i] = v2u{0u, 0u};
    for (unsigned spins = 0;; ++spins) {
      if (H1MASK) {
        if (!(pv[0][0] & pv[1][0] & pv[2][0] & pv[3][0] & 1u)) {
#pragma unroll
          for (int i = 0; i < 4; ++i) pv[i] = __builtin_amdgcn_raw_buffer_load_b64(wsr, o_acc, i * H1_STRIDE, AUX_SC1);
        }
        if (!(pm[0] & pm[2] & 1u)) pm = __builtin_amdgcn_raw_buffer_load_b128(wsr, o_ml, 0, AUX_SC1);
      } else {
#pragma unroll
        for (int i = 0; i < 4; ++i) pv[i] = __builtin_amdgcn_raw_buffer_load_b64(wsr, o_acc, i * H1_STRIDE, AUX_SC1);
        pm = __builtin_amdgcn_raw_buffer_load_b128(wsr, o_ml, 0, AUX_SC1);
      }
      if (__all((pv[0][0] & pv[1][0] & pv[2][0] & pv[3][0] & pm[0] & pm[2] & 1u) != 0u) || spins > SPIN_CAP) break;
      if (watch_flag && __builtin_amdgcn_readfirstlane(*((volatile LAS unsigned*)(smem + L_SYNC) + 4)) != 0u) return;
      __builtin_amdgcn_s_sleep(PSLEEP);
    }
    STAMP(8);
    const float Ms = __uint_as_float(pm[1]);
    float Mx = fmaxf(Ms, dpp<XOR1>(Ms)); Mx = fmaxf(Mx, dpp<XOR2>(Mx)); Mx = fmaxf(Mx, dpp<HMIR>(Mx));
    const float wt = __builtin_amdgcn_exp2f(Ms - Mx);
    v2u lv; lv[0] = pm[2]; lv[1] = pm[3];
    double den = (double)wt * __builtin_bit_cast(double, lv);
    den += dppd<XOR1>(den); den += dppd<XOR2>(den); den += dppd<HMIR>(den);
    double num = 0.0;
#define ACC1(i) { const float wl_ = dpp<0x150 + (i)>(wt), wh_ = dpp<0x154 + (i)>(wt); num = __builtin_fma((double)(hi8 ? wh_ : wl_), __builtin_bit_cast(double, pv[i]), num); }
    ACC1(0) ACC1(1) ACC1(2) ACC1(3)
#undef ACC1
    num += dppd<ROR8>(num);
    const float ov = (float)num / (float)den;
    *(LAS unsigned*)(smem + L_OS + lane * 4) = pk_bf16(ov, ov);
    lds_inc(smem, 4, lane);
  };

  if (w >= 4) {
    const int sw = w - 4, stid = tid - 256;
    const rsrc_t kr = mk_rsrc(Kc + (size_t)grp * (SK * 256), SK * 256);
    const rsrc_t vr = mk_rsrc(Vc + (size_t)grp * (SK * 256), SK * 256);
    const rsrc_t wsr = mk_rsrc(ws, WS_END);
    const unsigned ubase = (unsigned)(sp * 128 + sw);
    v4u Ba[8], Bb[8], Be[NE][8];
    if (!LATE0) issue(Be[0], kr, voff, ubase);
    __builtin_amdgcn_sched_barrier(0);
    asm volatile("" ::: "memory");
    __builtin_amdgcn_s_barrier();
    asm volatile("" ::: "memory");
    if (LATE0) issue(Be[0], kr, voff, ubase);
    __builtin_amdgcn_sched_barrier(0);
    STAMP(1);
    MAKE_MSK();
#pragma unroll
    for (int e = 1; e < NE; ++e) {
      DRAIN();
      issue(Be[e], kr, voff, ubase + 4 * e);
      __builtin_amdgcn_sched_barrier(0);
    }
    DRAIN();
    issue(Ba, kr, voff, ubase + 4 * NE);
    __builtin_amdgcn_sched_barrier(0);
    DRAIN();
    issue(Bb, kr, voff, ubase + 4 * NE + 4);
    __builtin_amdgcn_sched_barrier(0);
    STAMP(2);
    lds_wait<1>(smem, 0, 1u);
    STAMP(3);
    const v4u q0 = *(const LAS v4u*)(smem + L_QS + c * 16), q1 = *(const LAS v4u*)(smem + L_QS + 256 + c * 16);
    const int sc_off = ((c >> 3) * 4 + g) * 16;
    const int sg_st = (c & 7) == 0 ? WS_SG + grp * 131072 + sc_off : OOB;
#define KSC(B) const v2u s0_ = scores(&B[0], q0, q1, hi8), s1_ = scores(&B[4], q0, q1, hi8);                        \
      v4u sc_; sc_[0] = s0_[0]; sc_[1] = s0_[1]; sc_[2] = s1_[0]; sc_[3] = s1_[1];
#define KSTEP(B, i)                                                                                                \
    {                                                                                                              \
      KSC(B)                                                                                                       \
      if ((c & 7) == 0) *(LAS v4u*)(smem + L_SC + (4 * (i) + sw) * 128 + sc_off) = sc_;                            \
      asm volatile("" ::: "memory");                                                                               \
      __builtin_amdgcn_sched_barrier(0);                                                                           \
    }
    int nsl = 0;
    unsigned uid = 0;
#define SFLUSH()                                                                                                   \
    {                                                                                                              \
      const v4u v_ = *(const LAS v4u*)(smem + L_SB + sw * 1024 + voff);                                            \
      __builtin_amdgcn_raw_buffer_store_b128(v_, wsr, (lane >> 3) < nsl ? WS_SG + grp * 131072 + (int)(uid * 128u) + (lane & 7) * 16 : OOB, 0, AUX_SC1); \
      nsl = 0;                                                                                                     \
    }
#define KSTEPG(B, u)                                                                                               \
    {                                                                                                              \
      KSC(B)                                                                                                       \
      if ((c & 7) == 0) *(LAS v4u*)(smem + L_SB + sw * 1024 + nsl * 128 + sc_off) = sc_;                           \
      uid = (lane >> 3) == nsl ? (u) : uid;                                                                        \
      ++nsl;                                                                                                       \
      if (nsl == 8) SFLUSH()                                                                                       \
      asm volatile("" ::: "memory");                                                                               \
      __builtin_amdgcn_sched_barrier(0);                                                                           \
    }
    const int cnt0 = lane == 0 ? WS_CNT + grp * 256 : OOB;
    unsigned vone = 1u, a = 0u;
    unsigned taddr = (unsigned)(size_t)(LAS unsigned char*)(smem + L_SYNC + 28);
    asm volatile("" : "+v"(vone), "+v"(taddr));
    KSTEP(Ba, NE);
    issue(Ba, kr, voff, ubase + 4 * (NE + 2));
    __builtin_amdgcn_sched_barrier(0);
#pragma unroll
    for (int e = 0; e < NE; ++e) KSTEP(Be[e], e);
    KSTEP(Bb, NE + 1);
    static_assert(NE % 2 == 0, "K steps left: odd");
#define PACE() do { int lag_; while ((lag_ = (int)((unsigned)__builtin_amdgcn_s_memrealtime() * 16u - due)) < 0) __builtin_amdgcn_s_sleep(1); \
                    if (PCRED && lag_ > PCRED) due += (unsigned)(lag_ - PCRED); due += PACET; } while (0)
    unsigned due = (unsigned)__builtin_amdgcn_s_memrealtime() * 16u;
#pragma unroll 1
    for (int i = NE + 2; i < (int)(NSU / 4) - 1; i += 2) {
      PACE();
      DRAIN();
      issue(Bb, kr, voff, ubase + 4 * (i + 1));
      __builtin_amdgcn_sched_barrier(0);
      KSTEP(Ba, i);
      PACE();
      DRAIN();
      if (KDYN) a = atomic_inc(wsr, cnt0, i + 3 >= (int)(NSU / 4), 0);
      issue(Ba, kr, voff, ubase + 4 * (i + 2));
      __builtin_amdgcn_sched_barrier(0);
      KSTEP(Bb, i + 1);
    }
#define MAPD(d) (((d) & 7u) * 128u + NSU + ((d) >> 3))
#define WAITA() DRAIN()
    {
      unsigned ua, ub;
      if (!KDYN) PACE();
      WAITA();
      unsigned d = KDYN ? __builtin_amdgcn_readfirstlane(a) : (unsigned)(sw * 8 + sp);
      if (KDYN) a = atomic_inc(wsr, cnt0, d < ND, 0);
      ub = MAPD(d);
      issue(Bb, d < ND ? kr : vr, voff, d < ND ? ub : ubase);
      __builtin_amdgcn_sched_barrier(0);
      KSTEP(Ba, NSU / 4 - 1);
      bool in_b = true;
      while (d < ND) {
        if (!KDYN) PACE();
      WAITA();
        d = KDYN ? __builtin_amdgcn_readfirstlane(a) : d + 32u;
        if (KDYN) a = atomic_inc(wsr, cnt0, d < ND, 0);
        ua = MAPD(d);
        issue(Ba, d < ND ? kr : vr, voff, d < ND ? ua : ubase);
        __builtin_amdgcn_sched_barrier(0);
        KSTEPG(Bb, ub);
        if (d >= ND) { in_b = false; break; }
        if (!KDYN) PACE();
      WAITA();
        d = KDYN ? __builtin_amdgcn_readfirstlane(a) : d + 32u;
        if (KDYN) a = atomic_inc(wsr, cnt0, d < ND, 0);
        ub = MAPD(d);
        issue(Bb, d < ND ? kr : vr, voff, d < ND ? ub : ubase);
        __builtin_amdgcn_sched_barrier(0);
        KSTEPG(Ba, ua);
      }
      STAMP(4);
      SFLUSH()
      DRAIN();
      if (in_b) {
#pragma unroll
        for (int t = 0; t < 8; ++t) Ba[t] = Bb[t];
      }
    }
#undef KSTEP
#undef KSTEPG
#undef SFLUSH
#undef KSC
    St st; st.m = -INFINITY; st.l = 0.0; st.acc = f64x4{0.0, 0.0, 0.0, 0.0};
#define LSC(i) (*(const LAS v4u*)(smem + L_SC + (4 * (i) + sw) * 128 + sc_off))
    lds_inc(smem, 7, lane);
    if (KDYN) due = (unsigned)__builtin_amdgcn_s_memrealtime() * 16u;
#pragma unroll 1
    for (int i = 0; i < (int)(NSU / 4) - 1; i += 2) {
      PACE();
      DRAIN();
      issue(Bb, vr, voff, ubase + 4 * (i + 1));
      __builtin_amdgcn_sched_barrier(0);
      pv_unit(Ba, LSC(i), msk, st); pin(st);
      __builtin_amdgcn_sched_barrier(0);
      PACE();
      DRAIN();
      a = atomic_inc(wsr, cnt0, i + 3 >= (int)(NSU / 4), 4);
      issue(Ba, vr, voff, ubase + 4 * (i + 2));
      __builtin_amdgcn_sched_barrier(0);
      pv_unit(Bb, LSC(i + 1), msk, st); pin(st);
      __builtin_amdgcn_sched_barrier(0);
    }
    lds_wait<2>(smem, 9, 1u);
    v4u sa = LSC(NSU / 4 - 1), sb;
#undef LSC
    const int sg_ld = WS_SG + grp * 131072 + sc_off;
#define NEXT(nx)                                                                                                   \
    {                                                                                                              \
      const unsigned d_ = __builtin_amdgcn_readfirstlane(a);                                                       \
      nx = d_ < ND ? (d_ & 7u) * 128u + NSU + (d_ >> 3) : NONE;                                                    \
      if (d_ + NEARD >= ND) *((volatile LAS unsigned*)(smem + L_SYNC) + 2) = 1u;                                   \
      a = atomic_inc(wsr, cnt0, d_ < ND, 4);                                                               \
    }
    while (true) {
      unsigned nx;
      DRAIN();
      NEXT(nx);
      sb = __builtin_amdgcn_raw_buffer_load_b128(wsr, sg_ld, nx < NONE ? (int)(nx * 128u) : OOB, AUX_SC1);
      issue(Bb, vr, voff, nx);
      __builtin_amdgcn_sched_barrier(0);
      pv_unit(Ba, sa, msk, st); pin(st);
      __builtin_amdgcn_sched_barrier(0);
      if (nx >= NONE) break;
      DRAIN();
      NEXT(nx);
      sa = __builtin_amdgcn_raw_buffer_load_b128(wsr, sg_ld, nx < NONE ? (int)(nx * 128u) : OOB, AUX_SC1);
      issue(Ba, vr, voff, nx);
      __builtin_amdgcn_sched_barrier(0);
      pv_unit(Bb, sb, msk, st); pin(st);
      __builtin_amdgcn_sched_barrier(0);
      if (nx >= NONE) break;
    }
#undef NEXT
    STAMP(6);
    {
      const double l = xrow_sumd(st.l);
      const int h = c >> 3;
#pragma unroll
      for (int r = 0; r < 4; ++r) {
        const int dim = 8 * (4 * g + r) + (c & 7);
        *(LAS double*)(smem + L_MRG + (sw * 256 + h * 128 + (dim & 96) + (dim & 3) * 8 + ((dim >> 2) & 7)) * 8) = st.acc[r];
      }
      if (g == 0 && (c & 7) == 0) { *(LAS float*)(smem + L_ML + (h * 4 + sw) * 4) = st.m; *(LAS double*)(smem + L_ML + 32 + (h * 4 + sw) * 8) = l; }
    }
    lds_inc(smem, 3, lane);
    lds_wait<0>(smem, 3, 4u);
    STAMP(7);
    {
      const int hh = sw >> 1;
      const f32x4 m4 = *(const LAS f32x4*)(smem + L_ML + hh * 16);
      const f64x4 l4 = *(const LAS f64x4*)(smem + L_ML + 32 + hh * 32);
      double a4[4];
#pragma unroll
      for (int ww = 0; ww < 4; ++ww) a4[ww] = *(const LAS double*)(smem + L_MRG + (ww * 256 + stid) * 8);
      const float M = fmaxf(fmaxf(m4[0], m4[1]), fmaxf(m4[2], m4[3]));
      double L = 0.0, o = 0.0;
#pragma unroll
      for (int ww = 0; ww < 4; ++ww) {
        const double wt = (double)__builtin_amdgcn_exp2f(m4[ww] - M);
        L = __builtin_fma(wt, l4[ww], L); o = __builtin_fma(wt, a4[ww], o);
      }
      v2u ov = __builtin_bit_cast(v2u, o); ov[0] |= 1u;
      __builtin_amdgcn_raw_buffer_store_b64(ov, wsr, WS_H1 + bid * H1_STRIDE + stid * 8, 0, AUX_SC1);
      if ((stid & 127) == 0) {
        const v2u lv = __builtin_bit_cast(v2u, L);
        v4u ml; ml[0] = 1u; ml[1] = __float_as_uint(M); ml[2] = lv[0] | 1u; ml[3] = lv[1];
        __builtin_amdgcn_raw_buffer_store_b128(ml, wsr, WS_H1 + bid * H1_STRIDE + 2048 + hh * 16, 0, AUX_SC1);
      }
    }
    STAMP(14);
    if (H1P) {
      for (int i = 0; i < sw; ++i) __builtin_amdgcn_s_sleep(STAG);
      gather_o(true);
    }
    if (is_final) {
      const int src = WS_H2 + fid * 8192 + sw * 2048 + voff;
      lds_wait<8>(smem, 6, 1u);
      v4u p0, p1;
      for (unsigned spins = 0;; ++spins) {
        p0 = __builtin_amdgcn_raw_buffer_load_b128(wsr, src, 0, AUX_SC1);
        p1 = __builtin_amdgcn_raw_buffer_load_b128(wsr, src, 1024, AUX_SC1);
        const unsigned m = (p0[0] & p0[1] & p0[2] & p0[3]) & (p1[0] & p1[1] & p1[2] & p1[3]) & 1u;
        if (__all(m != 0u) || spins > SPIN_CAP) break;
        __builtin_amdgcn_s_sleep(PSLEEP);
      }
      STAMP(10);
      f32x4 t4;
#pragma unroll
      for (int j = 0; j < 4; ++j) {
        float t = val(p0[j]) + val(p1[j]);
        t += dpp<ROR4>(t); t += dpp<ROR8>(t);
        t4[j] = xrow_sum(t);
      }
      if (lane < 4) *(LAS f32x4*)(smem + L_FB + (sw * 4 + lane) * 16) = t4;
      LDSW();
      if (lds_ticket(taddr + 4, vone) == 3u && lane < 8) {
        f32x2 t = *(const LAS f32x2*)(smem + L_FB + lane * 8);
#pragma unroll
        for (int ww = 1; ww < 4; ++ww) t += *(const LAS f32x2*)(smem + L_FB + ww * 64 + lane * 8);
        out[fid * 8 + lane] = pk_bf16(t[0], t[1]);
      }
    }
  } else {
    const int hw = w;
    unsigned short xq[9];
    v4u wqv[9];
    {
      const int r = lane >> 3, row0 = 288 * (sp & 1) + 72 * hw;
      const rsrc_t qr = mk_rsrc(wq, D * 8192);
      const int base = (row0 + r) * 8192 + (kvh * 256 + (sp >> 1) * 64) * 2 + (lane & 7) * 16;
#pragma unroll
      for (int i = 0; i < 9; ++i) wqv[i] = __builtin_amdgcn_raw_buffer_load_b128(qr, base, i * 8 * 8192, AUX_NT);
#pragma unroll
      for (int i = 0; i < 9; ++i) xq[i] = x[b * D + row0 + 8 * i + r];
    }
    const int xi = tid < 144 ? tid : 143;
    const v2u xa = ((const v2u*)x)[xi], xb = ((const v2u*)x)[144 + xi];
    if (tid < 3) { const v4u z = {0u, 0u, 0u, 0u}; *(LAS v4u*)(smem + L_SYNC + tid * 16) = z; }
    __builtin_amdgcn_sched_barrier(0);
    BAR();
    STAMP(1);
    const rsrc_t wsr = mk_rsrc(ws, WS_END);
    unsigned tch = 0;
    if (PTOUCH && hw == 1) {
      const int cm_ = bid % 36, rb_ = bid / 36;
      const unsigned char* tp = Vc + (size_t)bid * (1 << 20);
      if (lane == 1) tp = (cm_ >= 18 ? wu : wg) + (size_t)(rb_ < 7 ? (2 * rb_) * 72 / 7 * 4 : 0) * (F * 2);
      if (lane == 2) tp = (cm_ >= 18 ? wu : wg) + (size_t)(rb_ < 7 ? (2 * rb_ + 2) * 72 / 7 * 4 - 1 : 0) * (F * 2);
      if (lane == 3) tp = wd + (size_t)(36 * (bid & 127)) * 1152;
      if (lane == 4) tp = wo + (size_t)(kvh * 256 + 32 * sp) * 1152;
      if (lane == 5) tp = ws + WS_G1;
      if (lane == 6) tp = (const unsigned char*)gw;
      if (lane < 7) tch = __builtin_nontemporal_load((const unsigned*)tp);
    }
    v2u evv = {0u, 0u};
    if (hw == 3) {
      evv = __builtin_amdgcn_raw_buffer_load_b64(wsr, WS_E, 0, AUX_SC1);
      const v4u z = {0u, 0u, 0u, 0u};
      __builtin_amdgcn_raw_buffer_store_b128(z, wsr, WS_H1 + bid * H1_STRIDE + voff, 0, AUX_SC1);
      __builtin_amdgcn_raw_buffer_store_b128(z, wsr, WS_H1 + bid * H1_STRIDE + 1024 + voff, 0, AUX_SC1);
      const int h2o = WS_H2 + (b * 36 * 128 + (bid & 127)) * 64 + (lane >> 2) * 8192 + (lane & 3) * 16;
      __builtin_amdgcn_raw_buffer_store_b128(z, wsr, h2o, 0, AUX_SC1);
      __builtin_amdgcn_raw_buffer_store_b128(z, wsr, h2o, 16 * 8192, AUX_SC1);
      int off = h2o + 32 * 8192;
      if (lane >= 16) off = WS_H1 + bid * H1_STRIDE + 2048 + (lane & 1) * 16;
      if (lane >= 32) off = sp == 0 ? WS_CNT + grp * 256 : OOB;
      __builtin_amdgcn_raw_buffer_store_b128(z, wsr, off, 0, AUX_SC1);
    }
    __builtin_amdgcn_sched_barrier(0);
    {
      float acc[8];
#pragma unroll
      for (int j = 0; j < 8; ++j) acc[j] = 0.f;
#pragma unroll
      for (int i = 0; i < 9; ++i) fma8(acc, bflo(xq[i]), wqv[i]);
      const f32x2 r2 = red8<false>(acc);
      if ((lane & 8) == 0) *(LAS f32x2*)(smem + L_QP + (hw * 64 + (lane & 7) * 8 + 4 * (lane >> 5) + 2 * ((lane >> 4) & 1)) * 4) = r2;
      if (tid < 144) {
        v4u xs4;
        xs4[0] = (xa[0] & 0xffffu) | (xb[0] << 16); xs4[1] = (xa[0] >> 16) | (xb[0] & 0xffff0000u);
        xs4[2] = (xa[1] & 0xffffu) | (xb[1] << 16); xs4[3] = (xa[1] >> 16) | (xb[1] & 0xffff0000u);
        *(LAS v4u*)(smem + L_XS + tid * 16) = xs4;
      }
      lds_inc(smem, 5, lane);
      lds_wait<0>(smem, 5, 4u);
    }
    STAMP(12);
    MAKE_MSK();
    v4u WO[16];
    const rsrc_t wor = mk_rsrc(wo, 4096 * D * 2);
    const int wo_lo = (kvh * 256 + 32 * sp + g) * 1152 + hw * 256 + c * 16;
    const int wo_hi = (hw == 0 && c < 8) ? wo_lo + 1024 : OOB;
    if (hw == 3) {
      const unsigned long long ev = ((unsigned long long)(unsigned)__builtin_amdgcn_readfirstlane(evv[1]) << 32) | (unsigned)__builtin_amdgcn_readfirstlane(evv[0]);
      const unsigned long long tag64 = ev * 6364136223846793005ull + 1442695040888963407ull;
      const unsigned tag_hi = (unsigned)(tag64 >> 32);
      const unsigned tag_lo8 = ((unsigned)tag64 >> 24) & 0xffu;
      {
        float s = 0.f;
#pragma unroll
        for (int ww = 0; ww < 4; ++ww) s += *(const LAS float*)(smem + L_QP + (ww * 64 + lane) * 4);
        DRAIN();
        v2u gr; gr[0] = ((__float_as_uint(s) + 0x80u) & 0xffffff00u) | tag_lo8; gr[1] = tag_hi;
        __builtin_amdgcn_raw_buffer_store_b64(gr, wsr, WS_QG + ((grp * 2 + (sp & 1)) * 256 + (sp >> 1) * 64 + lane) * 8, 0, AUX_SC1);
        v2u fl; fl[0] = (unsigned)tag64; fl[1] = tag_hi;
        __builtin_amdgcn_raw_buffer_store_b64(fl, wsr, lane == 0 ? WS_ST + bid * 8 : OOB, 0, AUX_SC1);
      }
      STAMP(13);
      for (unsigned spins = 0;; ++spins) {
        v4u p[4];
#pragma unroll
        for (int i = 0; i < 4; ++i) p[i] = __builtin_amdgcn_raw_buffer_load_b128(wsr, WS_QG + grp * 4096 + lane * 32 + (i & 1) * 16 + (i >> 1) * 2048, 0, AUX_SC1);
        unsigned bad = 0;
#pragma unroll
        for (int i = 0; i < 4; ++i) bad |= (p[i][1] ^ tag_hi) | (p[i][3] ^ tag_hi) | ((p[i][0] ^ tag_lo8) & 0xffu) | ((p[i][2] ^ tag_lo8) & 0xffu);
        if (__all(bad == 0u) || spins > SPIN_CAP) {
          float qv[4];
#pragma unroll
          for (int i = 0; i < 4; ++i)
            qv[i] = __uint_as_float(p[i >> 1][(i & 1) * 2] & 0xffffff00u) + __uint_as_float(p[2 + (i >> 1)][(i & 1) * 2] & 0xffffff00u);
          v2u qq; qq[0] = pk_bf16(qv[0], qv[1]); qq[1] = pk_bf16(qv[2], qv[3]);
          *(LAS v2u*)(smem + L_QS + lane * 8) = qq;
          break;
        }
        __builtin_amdgcn_s_sleep(2);
      }
      lds_inc(smem, 0, lane);
      STAMP(3);
      if (lane == 0) { v2u t2; t2[0] = (unsigned)tag64; t2[1] = tag_hi; *(LAS v2u*)(smem + L_TAG) = t2; }
    }
    v4u G1[11], WD[18];
    const int cm = bid % 36, rb = bid / 36, mat1 = cm >= 18 ? 1 : 0, cblk = cm - 18 * mat1;
    const int hb = 2 * rb + (hw >> 1);
    const int rg0 = hb * 72 / 7, nrg = (hb + 1) * 72 / 7 - rg0;
    const bool has_s1 = bid < 252;
    const int u0 = 36 * (bid & 127);
    unsigned short gwv[9];
    {
      const rsrc_t gr = mk_rsrc(mat1 ? wu : wg, has_s1 ? D * F * 2 : 0);
      const int go = (4 * rg0 + g) * (F * 2) + cblk * 512 + (hw & 1) * 256 + c * 16;
#pragma unroll
      for (int i = 0; i < 11; ++i) G1[i] = __builtin_amdgcn_raw_buffer_load_b128(gr, go, (i < 10 || nrg == 11) ? i * 4 * (F * 2) : OOB, AUX_NT);
      const rsrc_t dr = mk_rsrc(wd, F * D * 2);
      const int wd_lo = (u0 + g) * 1152 + hw * 256 + c * 16;
      const int wd_hi = (hw == 0 && c < 8) ? wd_lo + 1024 : OOB;
#pragma unroll
      for (int i = 0; i < 9; ++i) WD[i] = __builtin_amdgcn_raw_buffer_load_b128(dr, wd_lo, i * 4608, AUX_NT);
#pragma unroll
      for (int i = 9; i < 18; ++i) WD[i] = __builtin_amdgcn_raw_buffer_load_b128(dr, wd_hi, (i - 9) * 4608, AUX_NT);
#pragma unroll
      for (int i = 0; i < 8; ++i) WO[i] = __builtin_amdgcn_raw_buffer_load_b128(wor, wo_lo, i * 4608, AUX_NT);
#pragma unroll
      for (int i = 8; i < 16; ++i) WO[i] = __builtin_amdgcn_raw_buffer_load_b128(wor, wo_hi, (i - 8) * 4608, AUX_NT);
#pragma unroll
      for (int i = 0; i < 9; ++i) gwv[i] = gw[i * 64 + lane];
    }
    __builtin_amdgcn_sched_barrier(0);
    {
      f32x4 ga = {0.f, 0.f, 0.f, 0.f};
#pragma unroll
      for (int i = 0; i < 11; ++i) {
        const int row = 4 * (rg0 + i) + g;
        const unsigned xv = *(const LAS unsigned*)(smem + L_XS + (row < D ? row : 0) * 4);
        const unsigned t_ = hi8 ? ((xv & 0xffff0000u) | (xv >> 16)) : ((xv << 16) | (xv & 0xffffu));
        v4u bb; bb[0] = t_ & msk[0]; bb[1] = t_ & msk[1]; bb[2] = t_ & msk[2]; bb[3] = t_ & msk[3];
        ga = mfma(G1[i], bb, ga);
      }
      if (hw < 2) {
        *(LAS f32x4*)(smem + L_GU + hw * 1024 + voff) = ga;
        lds_inc(smem, 10, lane);
      } else {
        lds_wait<2>(smem, 10, 2u);
        ga = *(const LAS f32x4*)(smem + L_GU + (hw & 1) * 1024 + voff) + ga;
        __builtin_amdgcn_raw_buffer_store_b128(__builtin_bit_cast(v4u, ga), wsr, has_s1 ? WS_G1 + (((cblk * 2 + (hw & 1)) * 2 + mat1) * 7 + rb) * 1024 + voff : OOB, 0, AUX_SC1);
        DRAIN();
        lds_inc(smem, 1, lane);
      }
    }
    float gt;
    {
      float z = 0.f;
#pragma unroll
      for (int i = 0; i < 9; ++i) {
        const unsigned xv = *(const LAS unsigned*)(smem + L_XS + (i * 64 + lane) * 4);
        z += rbf((b ? bfhi(xv) : bflo(xv)) * bflo(gwv[i]));
      }
      z += dpp<XOR1>(z); z += dpp<XOR2>(z); z += dpp<ROR4>(z); z += dpp<ROR8>(z);
      z = rbf(xrow_sum(z));
      gt = rbf(1.f / (1.f + __expf(-z)));
    }
    unsigned tag_lo = 0, tag_hi = 0;
    if (hw == 3) {
      const v2u tg = *(const LAS v2u*)(smem + L_TAG);
      tag_lo = __builtin_amdgcn_readfirstlane(tg[0]); tag_hi = __builtin_amdgcn_readfirstlane(tg[1]);
      lds_wait<4>(smem, 1, 2u);
      v2u fl; fl[0] = tag_lo; fl[1] = tag_hi;
      __builtin_amdgcn_raw_buffer_store_b64(fl, wsr, (lane == 0 && has_s1) ? WS_F1 + cblk * 128 + (mat1 * 7 + rb) * 8 : OOB, 0, AUX_SC1);
      __builtin_amdgcn_s_sleep(HDELAY);
      {
        const int fblk = lane < 32 ? (u0 >> 8) : ((u0 + 35) >> 8);
        const int fo = WS_F1 + fblk * 128 + ((lane & 31) < 14 ? (lane & 31) : 0) * 8;
        for (unsigned spins = 0;; ++spins) {
          const v2u p = __builtin_amdgcn_raw_buffer_load_b64(wsr, fo, 0, AUX_SC1);
          if (__all(((p[1] ^ tag_hi) | (p[0] ^ tag_lo)) == 0u) || spins > SPIN_CAP) break;
          __builtin_amdgcn_s_sleep(64);
        }
      }
      __builtin_amdgcn_fence(__ATOMIC_ACQUIRE, "wavefront");
      const int u = u0 + 4 * c + g;
      const int so = c < 9 ? WS_G1 + (u >> 7) * 14 * 1024 + (((u >> 5) & 3) * 16 + b * 8 + (u & 7)) * 16 + ((u >> 3) & 3) * 4 : OOB;
      float pv[14];
#pragma unroll
      for (int i = 0; i < 14; ++i) pv[i] = __uint_as_float(__builtin_amdgcn_raw_buffer_load_b32(wsr, so, i * 1024, AUX_SC1));
      const float gv = rbf(((pv[0] + pv[1]) + (pv[2] + pv[3])) + ((pv[4] + pv[5]) + pv[6]));
      const float uv = rbf(((pv[7] + pv[8]) + (pv[9] + pv[10])) + ((pv[11] + pv[12]) + pv[13]));
      const float inner = 0.7978845608028654f * (gv + 0.044715f * gv * gv * gv);
      const float hv = rbf(0.5f * gv * (1.f + tanhf(inner))) * uv;
      *(LAS unsigned*)(smem + L_FFN + lane * 4) = pk_bf16(hv, hv);
      lds_inc(smem, 11, lane);
    }
    f32x4 yf = {0.f, 0.f, 0.f, 0.f}, zf = {0.f, 0.f, 0.f, 0.f};
    {
      lds_wait<16>(smem, 11, 1u);
      const unsigned hp = *(const LAS unsigned*)(smem + L_FFN + lane * 4);
#define PRD(i) { const unsigned t_ = dppu<0x150 + (i)>(hp); \
                 v4u bb; bb[0] = t_ & msk[0]; bb[1] = t_ & msk[1]; bb[2] = t_ & msk[2]; bb[3] = t_ & msk[3]; \
                 yf = mfma(WD[i], bb, yf); zf = mfma(WD[9 + (i)], bb, zf); }
      PRD(0) PRD(1) PRD(2) PRD(3) PRD(4) PRD(5) PRD(6) PRD(7) PRD(8)
#undef PRD
      const float og = rbf(1.f - gt);
      yf *= og; zf *= og;
      asm volatile("" : "+v"(yf), "+v"(zf));
    }
    __builtin_amdgcn_sched_barrier(0);
    asm volatile("" :: "v"(tch));
    STAMP(5);
    if (hw == 3) {
      const unsigned long long tag64 = tag_lo;
      lds_wait<32>(smem, 7, 4u);
      if (lane == 0) {
        v2u fl; fl[0] = (unsigned)tag64; fl[1] = tag_hi;
        __builtin_amdgcn_raw_buffer_store_b64(fl, wsr, WS_SF + bid * 64, 0, AUX_SC1);
      }
      for (unsigned spins = 0;; ++spins) {
        const v2u p = __builtin_amdgcn_raw_buffer_load_b64(wsr, WS_SF + (grp * 8 + (lane & 7)) * 64, 0, AUX_SC1);
        if (__all(((p[1] ^ tag_hi) | (p[0] ^ (unsigned)tag64)) == 0u) || spins > SPIN_CAP) break;
        __builtin_amdgcn_s_sleep(32);
      }
      lds_inc(smem, 9, lane);
      if (is_final) {
        for (unsigned spins = 0;; ++spins) {
          unsigned bad = 0;
#pragma unroll
          for (int i = 0; i < 2; ++i) {
            const v4u p = __builtin_amdgcn_raw_buffer_load_b128(wsr, WS_ST + i * 1024 + voff, 0, AUX_SC1);
            bad |= (p[1] ^ tag_hi) | (p[0] ^ tag_lo) | (p[3] ^ tag_hi) | (p[2] ^ tag_lo);
          }
          if (__all(bad == 0u) || spins > SPIN_CAP) break;
          __builtin_amdgcn_s_sleep(64);
        }
        if (fid == 0 && lane == 0) {
          v2u e; e[0] = tag_lo; e[1] = tag_hi;
          __builtin_amdgcn_raw_buffer_store_b64(e, wsr, WS_E, 0, AUX_SC1);
        }
        lds_inc(smem, 6, lane);
      }
    }
    __builtin_amdgcn_sched_barrier(0);
    DRAIN();
    lds_wait<32>(smem, 2, 1u);
    if (!H1P && hw == 3) gather_o(false);
    lds_wait<0>(smem, 4, 1u);
    const unsigned pp = *(const LAS unsigned*)(smem + L_OS + lane * 4);
    STAMP(9);
    {
      f32x4 y0 = {0.f, 0.f, 0.f, 0.f}, y1 = {0.f, 0.f, 0.f, 0.f}, z0 = {0.f, 0.f, 0.f, 0.f};
#define PRJ(i, y) { const unsigned t_ = dppu<0x150 + (i)>(pp); v4u bb; bb[0] = t_ & msk[0]; bb[1] = t_ & msk[1]; bb[2] = t_ & msk[2]; bb[3] = t_ & msk[3]; \
                    y = mfma(WO[i], bb, y); z0 = mfma(WO[8 + (i)], bb, z0); }
      PRJ(0, y0) PRJ(1, y1) PRJ(2, y0) PRJ(3, y1) PRJ(4, y0) PRJ(5, y1) PRJ(6, y0) PRJ(7, y1)
#undef PRJ
      y0 = (y0 + y1) * gt + yf;
      z0 = z0 * gt + zf;
      const int dst = WS_H2 + ((b * 36 + hw * 8 + 2 * g) * 128 + (bid & 127)) * 64 + c * 4;
      __builtin_amdgcn_raw_buffer_store_b32(__float_as_uint(hi8 ? y0[1] : y0[0]) | 1u, wsr, dst, 0, AUX_SC1);
      __builtin_amdgcn_raw_buffer_store_b32(__float_as_uint(hi8 ? y0[3] : y0[2]) | 1u, wsr, dst, 8192, AUX_SC1);
      if (hw == 0) {
        const int dz = g < 2 ? dst + 32 * 8192 : OOB;
        __builtin_amdgcn_raw_buffer_store_b32(__float_as_uint(hi8 ? z0[1] : z0[0]) | 1u, wsr, dz, 0, AUX_SC1);
        __builtin_amdgcn_raw_buffer_store_b32(__float_as_uint(hi8 ? z0[3] : z0[2]) | 1u, wsr, dz, 8192, AUX_SC1);
      }
    }
    STAMP(15);
  }
  STAMP(11);
  STAMP_FLUSH();
}

extern "C" void kernel_launch(void* const* d_in, const int* in_sizes, int n_in, void* d_out, int out_size, void* d_ws, size_t ws_size,
                              hipStream_t stream) {
  k_fused<<<256, NTHR, 0, stream>>>((const unsigned short*)d_in[0], (const unsigned char*)d_in[1], (const unsigned char*)d_in[2],
                                    (const unsigned char*)d_in[3], (const unsigned char*)d_in[4], (const unsigned char*)d_in[6],
                                    (const unsigned char*)d_in[7], (const unsigned char*)d_in[8], (const unsigned short*)d_in[9],
                                    (unsigned char*)d_ws, (unsigned*)d_out);
}
```

```cpp
#include <hip/hip_runtime.h>
#include <hip/hip_bf16.h>
#include <stdint.h>

typedef unsigned v4u __attribute__((ext_vector_type(4)));
typedef unsigned v2u __attribute__((ext_vector_type(2)));
typedef float f32x4 __attribute__((ext_vector_type(4)));
typedef float f32x2 __attribute__((ext_vector_type(2)));
typedef short bf16x8 __attribute__((ext_vector_type(8)));
typedef __bf16 bf16x2 __attribute__((ext_vector_type(2)));
typedef __amdgpu_buffer_rsrc_t rsrc_t;
#define LAS __attribute__((address_space(3)))

constexpr int D = 576, SK = 32768, F = 4608;
constexpr int NW = 8, NTHR = NW * 64;
#define NSU_V 124
constexpr unsigned NSU = NSU_V;
constexpr unsigned ND = 8 * (128 - NSU);
constexpr unsigned NONE = 1024;
static_assert(NSU % 4 == 0 && (NSU / 4) % 2 == 1, "static V steps per wave: odd");
constexpr int AUX_NT = 2, AUX_SC1 = 16;
constexpr int OOB = 0x40000000;
constexpr unsigned SPIN_CAP = 200000u;
#define H1P 1
#define NE 2
#define STAG 4
#define H1MASK 1
#define TOUCH 0
#define LATE0 0
#define WTS 0
#define WTD 4
#define HDELAY 100
#define SWAPR 0
#define SBATCH 1
#define PACET 1756
#define PCRED 3500
#define KDYN 1
#define PTOUCH 1
#define PTOUCH2 1
#define NEARD 64
#define PSLEEP 2

constexpr int WS_E = 0;
constexpr int WS_CNT = 4096;
constexpr int WS_QG = 16384;
constexpr int WS_H1 = WS_QG + 131072;
constexpr int H1_STRIDE = 2176;
constexpr int WS_H2 = WS_H1 + 256 * H1_STRIDE;
constexpr int WS_ST = WS_H2 + 144 * 4096;
constexpr int WS_SF = WS_ST + 4096;
constexpr int WS_SG = WS_SF + 256 * 64;
constexpr int WS_F1 = WS_SG + 32 * 1024 * 128;
constexpr int WS_G1 = WS_F1 + 4096;
constexpr int WS_END = WS_G1 + 36 * 14 * 1024;

constexpr int L_XS = 0;
constexpr int L_QP = 2304;
constexpr int L_QS = L_QP + 2048;
constexpr int L_MRG = L_QS + 512;
constexpr int L_ML = L_MRG + 8192;
constexpr int L_OS = L_ML + 96;
constexpr int L_GU = L_OS + 256;
constexpr int L_TAG = L_GU + 2048;
constexpr int L_FFN = L_TAG + 16;
constexpr int L_FB = L_FFN + 256;
constexpr int L_SYNC = L_FB + 256;
constexpr int L_SC = L_SYNC + 48;
constexpr int L_SB = L_SC + 16384;
constexpr int L_END = L_SB + 4096;
constexpr int LDS_BYTES = 96 * 1024;
static_assert(L_END <= LDS_BYTES, "lds");

__device__ __forceinline__ float bflo(unsigned u) { return __builtin_bit_cast(float, u << 16); }
__device__ __forceinline__ float bfhi(unsigned u) { return __builtin_bit_cast(float, u & 0xffff0000u); }
__device__ __forceinline__ unsigned pk_bf16(float lo, float hi) {
  bf16x2 v; v[0] = (__bf16)lo; v[1] = (__bf16)hi; return __builtin_bit_cast(unsigned, v);
}
__device__ __forceinline__ float rbf(float f) { return bflo(pk_bf16(f, f)); }
__device__ __forceinline__ float dot2(unsigned a, unsigned b, float c) {
  return __builtin_amdgcn_fdot2_f32_bf16(__builtin_bit_cast(bf16x2, a), __builtin_bit_cast(bf16x2, b), c, false);
}
template <int CTRL> __device__ __forceinline__ float dpp(float x) {
  return __builtin_bit_cast(float, __builtin_amdgcn_mov_dpp(__builtin_bit_cast(int, x), CTRL, 0xf, 0xf, true));
}
constexpr int XOR1 = 0xB1, XOR2 = 0x4E, HMIR = 0x141, ROR4 = 0x124, ROR8 = 0x128;
__device__ __forceinline__ f32x4 mfma(v4u a, v4u b, f32x4 c) {
  return __builtin_amdgcn_mfma_f32_16x16x32_bf16(__builtin_bit_cast(bf16x8, a), __builtin_bit_cast(bf16x8, b), c, 0, 0, 0);
}
__device__ __forceinline__ float swap16_sum(float a, float b) {
  auto s = __builtin_amdgcn_permlane16_swap(__float_as_uint(a), __float_as_uint(b), false, false);
  return __uint_as_float(s[0]) + __uint_as_float(s[1]);
}
__device__ __forceinline__ float swap32_sum(float a, float b) {
  auto s = __builtin_amdgcn_permlane32_swap(__float_as_uint(a), __float_as_uint(b), false, false);
  return __uint_as_float(s[0]) + __uint_as_float(s[1]);
}
__device__ __forceinline__ float xrow_max(float x) {
  auto s = __builtin_amdgcn_permlane16_swap(__float_as_uint(x), __float_as_uint(x), false, false);
  x = fmaxf(__uint_as_float(s[0]), __uint_as_float(s[1]));
  auto t = __builtin_amdgcn_permlane32_swap(__float_as_uint(x), __float_as_uint(x), false, false);
  return fmaxf(__uint_as_float(t[0]), __uint_as_float(t[1]));
}
__device__ __forceinline__ float xrow_sum(float x) { x = swap16_sum(x, x); return swap32_sum(x, x); }
__device__ __forceinline__ rsrc_t mk_rsrc(const void* p, int bytes) { return __builtin_amdgcn_make_buffer_rsrc((void*)p, 0, bytes, 0x00020000); }
#define BAR() do { asm volatile("s_waitcnt lgkmcnt(0)" ::: "memory"); __builtin_amdgcn_s_barrier(); asm volatile("" ::: "memory"); } while (0)
#define DRAIN() do { __builtin_amdgcn_s_waitcnt(0x0F70); asm volatile("s_waitcnt vmcnt(0)" ::: "memory"); } while (0)
#define WAITV(n) do { __builtin_amdgcn_s_waitcnt(0x0F70 | ((n) & 15) | (((n) >> 4) << 14)); asm volatile("" ::: "memory"); } while (0)
#define LDSW() asm volatile("s_waitcnt lgkmcnt(0)" ::: "memory")

#define STAMP(i) do {} while (0)
#define STAMP_FLUSH() do {} while (0)
constexpr float SCALE = 0.08838834764831845f, L2E = 1.4426950408889634f;

typedef double f64x4 __attribute__((ext_vector_type(4)));
struct St { float m; double l; f64x4 acc; };
__device__ __forceinline__ void pin(St& st) { asm volatile("" : "+v"(st.m), "+v"(st.l), "+v"(st.acc) :: "memory"); }
__device__ __forceinline__ v4u diag(float v, const v4u& msk) {
  const unsigned pp = pk_bf16(v, v);
  v4u bb; bb[0] = pp & msk[0]; bb[1] = pp & msk[1]; bb[2] = pp & msk[2]; bb[3] = pp & msk[3];
  return bb;
}

__device__ __forceinline__ v2u scores(const v4u* KB, const v4u& q0, const v4u& q1, bool hi8) {
  float s[4];
#pragma unroll
  for (int t = 0; t < 4; ++t) {
    float a0 = dot2(KB[t][0], q0[0], 0.f), a1 = dot2(KB[t][0], q1[0], 0.f);
#pragma unroll
    for (int k = 1; k < 4; ++k) { a0 = dot2(KB[t][k], q0[k], a0); a1 = dot2(KB[t][k], q1[k], a1); }
    const float keep = hi8 ? a1 : a0, send = hi8 ? a0 : a1;
    float v = keep + dpp<ROR8>(send);
    v += dpp<HMIR>(v); v += dpp<XOR2>(v); v += dpp<XOR1>(v);
    s[t] = v;
  }
  const unsigned u0 = pk_bf16(s[0], s[1]), u1 = pk_bf16(s[2], s[3]);
  v2u r; r[0] = pk_bf16(bflo(u0) * SCALE, bfhi(u0) * SCALE); r[1] = pk_bf16(bflo(u1) * SCALE, bfhi(u1) * SCALE);
  return r;
}
__device__ __forceinline__ void pv_unit(const v4u (&VB)[8], const v4u sc, const v4u& msk, St& st) {
  float s[8];
#pragma unroll
  for (int t = 0; t < 4; ++t) { s[2 * t] = bflo(sc[t]); s[2 * t + 1] = bfhi(sc[t]); }
  float tm = fmaxf(fmaxf(fmaxf(s[0], s[1]), fmaxf(s[2], s[3])), fmaxf(fmaxf(s[4], s[5]), fmaxf(s[6], s[7])));
  tm = xrow_max(tm);
  const float tl = tm * L2E, mt = __builtin_ceilf(tl), fr = tl - mt;
  const float mn = fmaxf(st.m, mt);
  const double alpha = (double)__builtin_amdgcn_exp2f(st.m - mn);
  const float beta = __builtin_amdgcn_exp2f(mt - mn);
  st.m = mn;
  float p[8];
#pragma unroll
  for (int t = 0; t < 8; ++t) p[t] = __builtin_amdgcn_exp2f(fmaf(s[t] - tm, L2E, fr));
  f32x4 ta = {0.f, 0.f, 0.f, 0.f};
#pragma unroll
  for (int t = 0; t < 8; ++t) ta = mfma(VB[t], diag(p[t], msk), ta);
  st.l = __builtin_fma(st.l, alpha, (double)((((p[0] + p[1]) + (p[2] + p[3])) + ((p[4] + p[5]) + (p[6] + p[7]))) * beta));
#pragma unroll
  for (int r = 0; r < 4; ++r) st.acc[r] = __builtin_fma(st.acc[r], alpha, (double)(ta[r] * beta));
}
__device__ __forceinline__ void issue(v4u (&B)[8], rsrc_t r, int voff, unsigned unit) {
  const int so = (int)(unit * 8192u);
#pragma unroll
  for (int t = 0; t < 4; ++t) B[t] = __builtin_amdgcn_raw_buffer_load_b128(r, voff + t * 1024, so, AUX_NT);
#pragma unroll
  for (int t = 0; t < 4; ++t) B[4 + t] = __builtin_amdgcn_raw_buffer_load_b128(r, voff + t * 1024, so + 4096, AUX_NT);
}
__device__ __forceinline__ unsigned atomic_inc(rsrc_t r, int voff0, bool on, int so) {
  return (unsigned)__builtin_amdgcn_raw_ptr_buffer_atomic_add_i32(1, r, voff0, on ? so : OOB, 0);
}
__device__ __forceinline__ unsigned lds_ticket(unsigned lds_addr_v, unsigned vone) {
  unsigned long long keep; unsigned r, s;
  asm volatile("s_mov_b64 %1, exec\n\ts_mov_b64 exec, 1\n\tds_add_rtn_u32 %0, %3, %4\n\ts_mov_b64 exec, %1\n\ts_waitcnt lgkmcnt(0)\n\tv_readfirstlane_b32 %2, %0\n\ts_nop 1"
               : "=&v"(r), "=&s"(keep), "=s"(s) : "v"(lds_addr_v), "v"(vone) : "memory");
  return s;
}
template <int CTRL> __device__ __forceinline__ unsigned dppu(unsigned x) { return (unsigned)__builtin_amdgcn_mov_dpp((int)x, CTRL, 0xf, 0xf, true); }
template <int CTRL> __device__ __forceinline__ double dppd(double x) {
  const v2u u = __builtin_bit_cast(v2u, x);
  v2u r; r[0] = dppu<CTRL>(u[0]); r[1] = dppu<CTRL>(u[1]);
  return __builtin_bit_cast(double, r);
}
__device__ __forceinline__ double xrow_sumd(double x) {
  v2u u = __builtin_bit_cast(v2u, x), a, b2;
  { auto s0 = __builtin_amdgcn_permlane16_swap(u[0], u[0], false, false); auto s1 = __builtin_amdgcn_permlane16_swap(u[1], u[1], false, false);
    a[0] = s0[0]; a[1] = s1[0]; b2[0] = s0[1]; b2[1] = s1[1]; }
  x = __builtin_bit_cast(double, a) + __builtin_bit_cast(double, b2);
  u = __builtin_bit_cast(v2u, x);
  { auto s0 = __builtin_amdgcn_permlane32_swap(u[0], u[0], false, false); auto s1 = __builtin_amdgcn_permlane32_swap(u[1], u[1], false, false);
    a[0] = s0[0]; a[1] = s1[0]; b2[0] = s0[1]; b2[1] = s1[1]; }
  return __builtin_bit_cast(double, a) + __builtin_bit_cast(double, b2);
}
__device__ __forceinline__ void lds_inc(unsigned char* smem, int idx, int lane) {
  LDSW();
  if (lane == 0) __hip_atomic_fetch_add((LAS unsigned*)(LAS unsigned char*)smem + (L_SYNC / 4 + idx), 1u, __ATOMIC_RELAXED, __HIP_MEMORY_SCOPE_WORKGROUP);
}
template <int SLEEP> __device__ __forceinline__ void lds_wait(unsigned char* smem, int idx, unsigned want) {
#pragma unroll 1
  for (unsigned spins = 0; spins < 8000000u / (1 + SLEEP); ++spins) {
    const unsigned v = __builtin_amdgcn_readfirstlane(*((volatile LAS unsigned*)(LAS unsigned char*)smem + (L_SYNC / 4 + idx)));
    if (v >= want) break;
    if (SLEEP) __builtin_amdgcn_s_sleep(SLEEP);
  }
  asm volatile("" ::: "memory");
}

__device__ __forceinline__ void fma8(float (&acc)[8], float xv, const v4u& w) {
#pragma unroll
  for (int k = 0; k < 4; ++k) { acc[2 * k] = fmaf(xv, bflo(w[k]), acc[2 * k]); acc[2 * k + 1] = fmaf(xv, bfhi(w[k]), acc[2 * k + 1]); }
}
template <bool BIT2> __device__ __forceinline__ f32x2 red8(const float (&acc)[8]) {
  float a[4];
#pragma unroll
  for (int j = 0; j < 4; ++j) a[j] = swap32_sum(acc[j], acc[j + 4]);
  float b0 = swap16_sum(a[0], a[2]), b1 = swap16_sum(a[1], a[3]);
  if (BIT2) { b0 += dpp<ROR4>(b0); b1 += dpp<ROR4>(b1); }
  b0 += dpp<ROR8>(b0); b1 += dpp<ROR8>(b1);
  f32x2 r; r[0] = b0; r[1] = b1; return r;
}
__device__ __forceinline__ float val(unsigned u) { return __uint_as_float(u & ~1u); }
__device__ __forceinline__ bool all4(const v4u& p) { return (p[0] & p[1] & p[2] & p[3] & 1u) != 0u; }

__global__ void __launch_bounds__(NTHR, 2)
k_fused(const unsigned short* __restrict__ x, const unsigned char* Kc, const unsigned char* Vc,
        const unsigned char* __restrict__ wq, const unsigned char* wo, const unsigned char* wg,
        const unsigned char* wu, const unsigned char* wd, const unsigned short* __restrict__ gw,
        unsigned char* ws, unsigned* out) {
  __shared__ __attribute__((aligned(16))) unsigned char smem[LDS_BYTES];
  const int tid = SWAPR ? (threadIdx.x ^ 256) : threadIdx.x, lane = tid & 63, w = __builtin_amdgcn_readfirstlane(tid >> 6);
  const int bid = blockIdx.x, grp = bid >> 3, sp = bid & 7, b = grp >> 4, kvh = grp & 15;
  const int g = lane >> 4, c = lane & 15;
  unsigned tsv = 0xffffffffu; (void)tsv;
  STAMP(0);
  const int voff = lane * 16;
  const int ridx = (grp + 4 * sp) & 31;
  const bool is_final = ridx < 9;
  const int fid = sp * 9 + ridx;
  const bool hi8 = (c & 8) != 0;
#define MAKE_MSK() v4u msk; _Pragma("unroll") for (int d = 0; d < 4; ++d) msk[d] = (((c & 7) >> 1) == d) ? ((c & 1) ? 0xffff0000u : 0x0000ffffu) : 0u;

  auto gather_o = [&](bool watch_flag) {
    const rsrc_t wsr = mk_rsrc(ws, WS_END);
    const int o_acc = WS_H1 + (grp * 8 + 4 * (c >> 3)) * H1_STRIDE + (32 * sp + 8 * g + (c & 7)) * 8;
    const int o_ml = WS_H1 + (grp * 8 + (c & 7)) * H1_STRIDE + 2048 + (sp >> 2) * 16;
    v2u pv[4];
    v4u pm = {0u, 0u, 0u, 0u};
#pragma unroll
    for (int i = 0; i < 4; ++i) pv[i] = v2u{0u, 0u};
    for (unsigned spins = 0;; ++spins) {
      if (H1MASK) {
        if (!(pv[0][0] & pv[1][0] & pv[2][0] & pv[3][0] & 1u)) {
#pragma unroll
          for (int i = 0; i < 4; ++i) pv[i] = __builtin_amdgcn_raw_buffer_load_b64(wsr, o_acc, i * H1_STRIDE, AUX_SC1);
        }
        if (!(pm[0] & pm[2] & 1u)) pm = __builtin_amdgcn_raw_buffer_load_b128(wsr, o_ml, 0, AUX_SC1);
      } else {
#pragma unroll
        for (int i = 0; i < 4; ++i) pv[i] = __builtin_amdgcn_raw_buffer_load_b64(wsr, o_acc, i * H1_STRIDE, AUX_SC1);
        pm = __builtin_amdgcn_raw_buffer_load_b128(wsr, o_ml, 0, AUX_SC1);
      }
      if (__all((pv[0][0] & pv[1][0] & pv[2][0] & pv[3][0] & pm[0] & pm[2] & 1u) != 0u) || spins > SPIN_CAP) break;
      if (watch_flag && __builtin_amdgcn_readfirstlane(*((volatile LAS unsigned*)(smem + L_SYNC) + 4)) != 0u) return;
      __builtin_amdgcn_s_sleep(PSLEEP);
    }
    STAMP(8);
    const float Ms = __uint_as_float(pm[1]);
    float Mx = fmaxf(Ms, dpp<XOR1>(Ms)); Mx = fmaxf(Mx, dpp<XOR2>(Mx)); Mx = fmaxf(Mx, dpp<HMIR>(Mx));
    const float wt = __builtin_amdgcn_exp2f(Ms - Mx);
    v2u lv; lv[0] = pm[2]; lv[1] = pm[3];
    double den = (double)wt * __builtin_bit_cast(double, lv);
    den += dppd<XOR1>(den); den += dppd<XOR2>(den); den += dppd<HMIR>(den);
    double num = 0.0;
#define ACC1(i) { const float wl_ = dpp<0x150 + (i)>(wt), wh_ = dpp<0x154 + (i)>(wt); num = __builtin_fma((double)(hi8 ? wh_ : wl_), __builtin_bit_cast(double, pv[i]), num); }
    ACC1(0) ACC1(1) ACC1(2) ACC1(3)
#undef ACC1
    num += dppd<ROR8>(num);
    const float ov = (float)num / (float)den;
    *(LAS unsigned*)(smem + L_OS + lane * 4) = pk_bf16(ov, ov);
    lds_inc(smem, 4, lane);
  };

  if (w >= 4) {
    const int sw = w - 4, stid = tid - 256;
    const rsrc_t kr = mk_rsrc(Kc + (size_t)grp * (SK * 256), SK * 256);
    const rsrc_t vr = mk_rsrc(Vc + (size_t)grp * (SK * 256), SK * 256);
    const rsrc_t wsr = mk_rsrc(ws, WS_END);
    const unsigned ubase = (unsigned)(sp * 128 + sw);
    v4u Ba[8], Bb[8], Be[NE][8];
    if (!LATE0) issue(Be[0], kr, voff, ubase);
    __builtin_amdgcn_sched_barrier(0);
    asm volatile("" ::: "memory");
    __builtin_amdgcn_s_barrier();
    asm volatile("" ::: "memory");
    if (LATE0) issue(Be[0], kr, voff, ubase);
    __builtin_amdgcn_sched_barrier(0);
    STAMP(1);
    MAKE_MSK();
#pragma unroll
    for (int e = 1; e < NE; ++e) {
      DRAIN();
      issue(Be[e], kr, voff, ubase + 4 * e);
      __builtin_amdgcn_sched_barrier(0);
    }
    DRAIN();
    issue(Ba, kr, voff, ubase + 4 * NE);
    __builtin_amdgcn_sched_barrier(0);
    DRAIN();
    issue(Bb, kr, voff, ubase + 4 * NE + 4);
    __builtin_amdgcn_sched_barrier(0);
    STAMP(2);
    lds_wait<1>(smem, 0, 1u);
    STAMP(3);
    const v4u q0 = *(const LAS v4u*)(smem + L_QS + c * 16), q1 = *(const LAS v4u*)(smem + L_QS + 256 + c * 16);
    const int sc_off = ((c >> 3) * 4 + g) * 16;
    const int sg_st = (c & 7) == 0 ? WS_SG + grp * 131072 + sc_off : OOB;
#define KSC(B) const v2u s0_ = scores(&B[0], q0, q1, hi8), s1_ = scores(&B[4], q0, q1, hi8);                        \
      v4u sc_; sc_[0] = s0_[0]; sc_[1] = s0_[1]; sc_[2] = s1_[0]; sc_[3] = s1_[1];
#define KSTEP(B, i)                                                                                                \
    {                                                                                                              \
      KSC(B)                                                                                                       \
      if ((c & 7) == 0) *(LAS v4u*)(smem + L_SC + (4 * (i) + sw) * 128 + sc_off) = sc_;                            \
      asm volatile("" ::: "memory");                                                                               \
      __builtin_amdgcn_sched_barrier(0);                                                                           \
    }
    int nsl = 0;
    unsigned uid = 0;
#define SFLUSH()                                                                                                   \
    {                                                                                                              \
      const v4u v_ = *(const LAS v4u*)(smem + L_SB + sw * 1024 + voff);                                            \
      __builtin_amdgcn_raw_buffer_store_b128(v_, wsr, (lane >> 3) < nsl ? WS_SG + grp * 131072 + (int)(uid * 128u) + (lane & 7) * 16 : OOB, 0, AUX_SC1); \
      nsl = 0;                                                                                                     \
    }
#define KSTEPG(B, u)                                                                                               \
    {                                                                                                              \
      KSC(B)                                                                                                       \
      if ((c & 7) == 0) *(LAS v4u*)(smem + L_SB + sw * 1024 + nsl * 128 + sc_off) = sc_;                           \
      uid = (lane >> 3) == nsl ? (u) : uid;                                                                        \
      ++nsl;                                                                                                       \
      if (nsl == 8) SFLUSH()                                                                                       \
      asm volatile("" ::: "memory");                                                                               \
      __builtin_amdgcn_sched_barrier(0);                                                                           \
    }
    const int cnt0 = lane == 0 ? WS_CNT + grp * 256 : OOB;
    unsigned vone = 1u, a = 0u;
    unsigned taddr = (unsigned)(size_t)(LAS unsigned char*)(smem + L_SYNC + 28);
    asm volatile("" : "+v"(vone), "+v"(taddr));
    KSTEP(Ba, NE);
    issue(Ba, kr, voff, ubase + 4 * (NE + 2));
    __builtin_amdgcn_sched_barrier(0);
#pragma unroll
    for (int e = 0; e < NE; ++e) KSTEP(Be[e], e);
    KSTEP(Bb, NE + 1);
    static_assert(NE % 2 == 0, "K steps left: odd");
#define PACE() do { int lag_; while ((lag_ = (int)((unsigned)__builtin_amdgcn_s_memrealtime() * 16u - due)) < 0) __builtin_amdgcn_s_sleep(1); \
                    if (PCRED && lag_ > PCRED) due += (unsigned)(lag_ - PCRED); due += PACET; } while (0)
    unsigned due = (unsigned)__builtin_amdgcn_s_memrealtime() * 16u;
#pragma unroll 1
    for (int i = NE + 2; i < (int)(NSU / 4) - 1; i += 2) {
      PACE();
      DRAIN();
      issue(Bb, kr, voff, ubase + 4 * (i + 1));
      __builtin_amdgcn_sched_barrier(0);
      KSTEP(Ba, i);
      PACE();
      DRAIN();
      if (KDYN) a = atomic_inc(wsr, cnt0, i + 3 >= (int)(NSU / 4), 0);
      issue(Ba, kr, voff, ubase + 4 * (i + 2));
      __builtin_amdgcn_sched_barrier(0);
      KSTEP(Bb, i + 1);
    }
#define MAPD(d) (((d) & 7u) * 128u + NSU + ((d) >> 3))
#define WAITA() DRAIN()
    {
      unsigned ua, ub;
      if (!KDYN) PACE();
      WAITA();
      unsigned d = KDYN ? __builtin_amdgcn_readfirstlane(a) : (unsigned)(sw * 8 + sp);
      if (KDYN) a = atomic_inc(wsr, cnt0, d < ND, 0);
      ub = MAPD(d);
      issue(Bb, d < ND ? kr : vr, voff, d < ND ? ub : ubase);
      __builtin_amdgcn_sched_barrier(0);
      KSTEP(Ba, NSU / 4 - 1);
      bool in_b = true;
      while (d < ND) {
        if (!KDYN) PACE();
      WAITA();
        d = KDYN ? __builtin_amdgcn_readfirstlane(a) : d + 32u;
        if (KDYN) a = atomic_inc(wsr, cnt0, d < ND, 0);
        ua = MAPD(d);
        issue(Ba, d < ND ? kr : vr, voff, d < ND ? ua : ubase);
        __builtin_amdgcn_sched_barrier(0);
        KSTEPG(Bb, ub);
        if (d >= ND) { in_b = false; break; }
        if (!KDYN) PACE();
      WAITA();
        d = KDYN ? __builtin_amdgcn_readfirstlane(a) : d + 32u;
        if (KDYN) a = atomic_inc(wsr, cnt0, d < ND, 0);
        ub = MAPD(d);
        issue(Bb, d < ND ? kr : vr, voff, d < ND ? ub : ubase);
        __builtin_amdgcn_sched_barrier(0);
        KSTEPG(Ba, ua);
      }
      STAMP(4);
      SFLUSH()
      DRAIN();
      if (in_b) {
#pragma unroll
        for (int t = 0; t < 8; ++t) Ba[t] = Bb[t];
      }
    }
#undef KSTEP
#undef KSTEPG
#undef SFLUSH
#undef KSC
    St st; st.m = -INFINITY; st.l = 0.0; st.acc = f64x4{0.0, 0.0, 0.0, 0.0};
#define LSC(i) (*(const LAS v4u*)(smem + L_SC + (4 * (i) + sw) * 128 + sc_off))
    lds_inc(smem, 7, lane);
    if (KDYN) due = (unsigned)__builtin_amdgcn_s_memrealtime() * 16u;
#pragma unroll 1
    for (int i = 0; i < (int)(NSU / 4) - 1; i += 2) {
      PACE();
      DRAIN();
      issue(Bb, vr, voff, ubase + 4 * (i + 1));
      __builtin_amdgcn_sched_barrier(0);
      pv_unit(Ba, LSC(i), msk, st); pin(st);
      __builtin_amdgcn_sched_barrier(0);
      PACE();
      DRAIN();
      a = atomic_inc(wsr, cnt0, i + 3 >= (int)(NSU / 4), 4);
      issue(Ba, vr, voff, ubase + 4 * (i + 2));
      __builtin_amdgcn_sched_barrier(0);
      pv_unit(Bb, LSC(i + 1), msk, st); pin(st);
      __builtin_amdgcn_sched_barrier(0);
    }
    lds_wait<2>(smem, 9, 1u);
    v4u sa = LSC(NSU / 4 - 1), sb;
#undef LSC
    const int sg_ld = WS_SG + grp * 131072 + sc_off;
#define NEXT(nx)                                                                                                   \
    {                                                                                                              \
      const unsigned d_ = __builtin_amdgcn_readfirstlane(a);                                                       \
      nx = d_ < ND ? (d_ & 7u) * 128u + NSU + (d_ >> 3) : NONE;                                                    \
      if (d_ + NEARD >= ND) *((volatile LAS unsigned*)(smem + L_SYNC) + 2) = 1u;                                   \
      a = atomic_inc(wsr, cnt0, d_ < ND, 4);                                                               \
    }
    while (true) {
      unsigned nx;
      DRAIN();
      NEXT(nx);
      sb = __builtin_amdgcn_raw_buffer_load_b128(wsr, sg_ld, nx < NONE ? (int)(nx * 128u) : OOB, AUX_SC1);
      issue(Bb, vr, voff, nx);
      __builtin_amdgcn_sched_barrier(0);
      pv_unit(Ba, sa, msk, st); pin(st);
      __builtin_amdgcn_sched_barrier(0);
      if (nx >= NONE) break;
      DRAIN();
      NEXT(nx);
      sa = __builtin_amdgcn_raw_buffer_load_b128(wsr, sg_ld, nx < NONE ? (int)(nx * 128u) : OOB, AUX_SC1);
      issue(Ba, vr, voff, nx);
      __builtin_amdgcn_sched_barrier(0);
      pv_unit(Bb, sb, msk, st); pin(st);
      __builtin_amdgcn_sched_barrier(0);
      if (nx >= NONE) break;
    }
#undef NEXT
    STAMP(6);
    {
      const double l = xrow_sumd(st.l);
      const int h = c >> 3;
#pragma unroll
      for (int r = 0; r < 4; ++r) {
        const int dim = 8 * (4 * g + r) + (c & 7);
        *(LAS double*)(smem + L_MRG + (sw * 256 + h * 128 + (dim & 96) + (dim & 3) * 8 + ((dim >> 2) & 7)) * 8) = st.acc[r];
      }
      if (g == 0 && (c & 7) == 0) { *(LAS float*)(smem + L_ML + (h * 4 + sw) * 4) = st.m; *(LAS double*)(smem + L_ML + 32 + (h * 4 + sw) * 8) = l; }
    }
    lds_inc(smem, 3, lane);
    lds_wait<0>(smem, 3, 4u);
    STAMP(7);
    {
      const int hh = sw >> 1;
      const f32x4 m4 = *(const LAS f32x4*)(smem + L_ML + hh * 16);
      const f64x4 l4 = *(const LAS f64x4*)(smem + L_ML + 32 + hh * 32);
      double a4[4];
#pragma unroll
      for (int ww = 0; ww < 4; ++ww) a4[ww] = *(const LAS double*)(smem + L_MRG + (ww * 256 + stid) * 8);
      const float M = fmaxf(fmaxf(m4[0], m4[1]), fmaxf(m4[2], m4[3]));
      double L = 0.0, o = 0.0;
#pragma unroll
      for (int ww = 0; ww < 4; ++ww) {
        const double wt = (double)__builtin_amdgcn_exp2f(m4[ww] - M);
        L = __builtin_fma(wt, l4[ww], L); o = __builtin_fma(wt, a4[ww], o);
      }
      v2u ov = __builtin_bit_cast(v2u, o); ov[0] |= 1u;
      __builtin_amdgcn_raw_buffer_store_b64(ov, wsr, WS_H1 + bid * H1_STRIDE + stid * 8, 0, AUX_SC1);
      if ((stid & 127) == 0) {
        const v2u lv = __builtin_bit_cast(v2u, L);
        v4u ml; ml[0] = 1u; ml[1] = __float_as_uint(M); ml[2] = lv[0] | 1u; ml[3] = lv[1];
        __builtin_amdgcn_raw_buffer_store_b128(ml, wsr, WS_H1 + bid * H1_STRIDE + 2048 + hh * 16, 0, AUX_SC1);
      }
    }
    STAMP(14);
    if (H1P) {
      for (int i = 0; i < sw; ++i) __builtin_amdgcn_s_sleep(STAG);
      gather_o(true);
    }
    if (is_final) {
      const int src = WS_H2 + fid * 8192 + sw * 2048 + voff;
      lds_wait<8>(smem, 6, 1u);
      v4u p0, p1;
      for (unsigned spins = 0;; ++spins) {
        p0 = __builtin_amdgcn_raw_buffer_load_b128(wsr, src, 0, AUX_SC1);
        p1 = __builtin_amdgcn_raw_buffer_load_b128(wsr, src, 1024, AUX_SC1);
        const unsigned m = (p0[0] & p0[1] & p0[2] & p0[3]) & (p1[0] & p1[1] & p1[2] & p1[3]) & 1u;
        if (__all(m != 0u) || spins > SPIN_CAP) break;
        __builtin_amdgcn_s_sleep(PSLEEP);
      }
      STAMP(10);
      f32x4 t4;
#pragma unroll
      for (int j = 0; j < 4; ++j) {
        float t = val(p0[j]) + val(p1[j]);
        t += dpp<ROR4>(t); t += dpp<ROR8>(t);
        t4[j] = xrow_sum(t);
      }
      if (lane < 4) *(LAS f32x4*)(smem + L_FB + (sw * 4 + lane) * 16) = t4;
      LDSW();
      if (lds_ticket(taddr + 4, vone) == 3u && lane < 8) {
        f32x2 t = *(const LAS f32x2*)(smem + L_FB + lane * 8);
#pragma unroll
        for (int ww = 1; ww < 4; ++ww) t += *(const LAS f32x2*)(smem + L_FB + ww * 64 + lane * 8);
        out[fid * 8 + lane] = pk_bf16(t[0], t[1]);
      }
    }
  } else {
    const int hw = w;
    unsigned short xq[9];
    v4u wqv[9];
    {
      const int r = lane >> 3, row0 = 288 * (sp & 1) + 72 * hw;
      const rsrc_t qr = mk_rsrc(wq, D * 8192);
      const int base = (row0 + r) * 8192 + (kvh * 256 + (sp >> 1) * 64) * 2 + (lane & 7) * 16;
#pragma unroll
      for (int i = 0; i < 9; ++i) wqv[i] = __builtin_amdgcn_raw_buffer_load_b128(qr, base, i * 8 * 8192, AUX_NT);
#pragma unroll
      for (int i = 0; i < 9; ++i) xq[i] = x[b * D + row0 + 8 * i + r];
    }
    const int xi = tid < 144 ? tid : 143;
    const v2u xa = ((const v2u*)x)[xi], xb = ((const v2u*)x)[144 + xi];
    if (tid < 3) { const v4u z = {0u, 0u, 0u, 0u}; *(LAS v4u*)(smem + L_SYNC + tid * 16) = z; }
    __builtin_amdgcn_sched_barrier(0);
    BAR();
    STAMP(1);
    const rsrc_t wsr = mk_rsrc(ws, WS_END);
    unsigned tch = 0;
    if (PTOUCH && hw == 1) {
      const int cm_ = bid % 36, rb_ = bid / 36;
      const unsigned char* tp = Vc + (size_t)bid * (1 << 20);
      if (lane == 1) tp = (cm_ >= 18 ? wu : wg) + (size_t)(rb_ < 7 ? (2 * rb_) * 72 / 7 * 4 : 0) * (F * 2);
      if (lane == 2) tp = (cm_ >= 18 ? wu : wg) + (size_t)(rb_ < 7 ? (2 * rb_ + 2) * 72 / 7 * 4 - 1 : 0) * (F * 2);
      if (lane == 3) tp = wd + (size_t)(36 * (bid & 127)) * 1152;
      if (lane == 4) tp = wo + (size_t)(kvh * 256 + 32 * sp) * 1152;
      if (lane == 5) tp = ws + WS_G1;
      if (lane == 6) tp = (const unsigned char*)gw;
      if (lane < 7) tch = __builtin_nontemporal_load((const unsigned*)tp);
    }
    v2u evv = {0u, 0u};
    if (hw == 3) {
      evv = __builtin_amdgcn_raw_buffer_load_b64(wsr, WS_E, 0, AUX_SC1);
      const v4u z = {0u, 0u, 0u, 0u};
      __builtin_amdgcn_raw_buffer_store_b128(z, wsr, WS_H1 + bid * H1_STRIDE + voff, 0, AUX_SC1);
      __builtin_amdgcn_raw_buffer_store_b128(z, wsr, WS_H1 + bid * H1_STRIDE + 1024 + voff, 0, AUX_SC1);
      const int h2o = WS_H2 + (b * 36 * 128 + (bid & 127)) * 64 + (lane >> 2) * 8192 + (lane & 3) * 16;
      __builtin_amdgcn_raw_buffer_store_b128(z, wsr, h2o, 0, AUX_SC1);
      __builtin_amdgcn_raw_buffer_store_b128(z, wsr, h2o, 16 * 8192, AUX_SC1);
      int off = h2o + 32 * 8192;
      if (lane >= 16) off = WS_H1 + bid * H1_STRIDE + 2048 + (lane & 1) * 16;
      if (lane >= 32) off = sp == 0 ? WS_CNT + grp * 256 : OOB;
      __builtin_amdgcn_raw_buffer_store_b128(z, wsr, off, 0, AUX_SC1);
    }
    __builtin_amdgcn_sched_barrier(0);
    {
      float acc[8];
#pragma unroll
      for (int j = 0; j < 8; ++j) acc[j] = 0.f;
#pragma unroll
      for (int i = 0; i < 9; ++i) fma8(acc, bflo(xq[i]), wqv[i]);
      const f32x2 r2 = red8<false>(acc);
      if ((lane & 8) == 0) *(LAS f32x2*)(smem + L_QP + (hw * 64 + (lane & 7) * 8 + 4 * (lane >> 5) + 2 * ((lane >> 4) & 1)) * 4) = r2;
      if (tid < 144) {
        v4u xs4;
        xs4[0] = (xa[0] & 0xffffu) | (xb[0] << 16); xs4[1] = (xa[0] >> 16) | (xb[0] & 0xffff0000u);
        xs4[2] = (xa[1] & 0xffffu) | (xb[1] << 16); xs4[3] = (xa[1] >> 16) | (xb[1] & 0xffff0000u);
        *(LAS v4u*)(smem + L_XS + tid * 16) = xs4;
      }
      lds_inc(smem, 5, lane);
      lds_wait<0>(smem, 5, 4u);
    }
    STAMP(12);
    MAKE_MSK();
    v4u WO[16];
    const rsrc_t wor = mk_rsrc(wo, 4096 * D * 2);
    const int wo_lo = (kvh * 256 + 32 * sp + g) * 1152 + hw * 256 + c * 16;
    const int wo_hi = (hw == 0 && c < 8) ? wo_lo + 1024 : OOB;
    if (hw == 3) {
      const unsigned long long ev = ((unsigned long long)(unsigned)__builtin_amdgcn_readfirstlane(evv[1]) << 32) | (unsigned)__builtin_amdgcn_readfirstlane(evv[0]);
      const unsigned long long tag64 = ev * 6364136223846793005ull + 1442695040888963407ull;
      const unsigned tag_hi = (unsigned)(tag64 >> 32);
      const unsigned tag_lo8 = ((unsigned)tag64 >> 24) & 0xffu;
      {
        float s = 0.f;
#pragma unroll
        for (int ww = 0; ww < 4; ++ww) s += *(const LAS float*)(smem + L_QP + (ww * 64 + lane) * 4);
        DRAIN();
        v2u gr; gr[0] = ((__float_as_uint(s) + 0x80u) & 0xffffff00u) | tag_lo8; gr[1] = tag_hi;
        __builtin_amdgcn_raw_buffer_store_b64(gr, wsr, WS_QG + ((grp * 2 + (sp & 1)) * 256 + (sp >> 1) * 64 + lane) * 8, 0, AUX_SC1);
        v2u fl; fl[0] = (unsigned)tag64; fl[1] = tag_hi;
        __builtin_amdgcn_raw_buffer_store_b64(fl, wsr, lane == 0 ? WS_ST + bid * 8 : OOB, 0, AUX_SC1);
      }
      STAMP(13);
      for (unsigned spins = 0;; ++spins) {
        v4u p[4];
#pragma unroll
        for (int i = 0; i < 4; ++i) p[i] = __builtin_amdgcn_raw_buffer_load_b128(wsr, WS_QG + grp * 4096 + lane * 32 + (i & 1) * 16 + (i >> 1) * 2048, 0, AUX_SC1);
        unsigned bad = 0;
#pragma unroll
        for (int i = 0; i < 4; ++i) bad |= (p[i][1] ^ tag_hi) | (p[i][3] ^ tag_hi) | ((p[i][0] ^ tag_lo8) & 0xffu) | ((p[i][2] ^ tag_lo8) & 0xffu);
        if (__all(bad == 0u) || spins > SPIN_CAP) {
          float qv[4];
#pragma unroll
          for (int i = 0; i < 4; ++i)
            qv[i] = __uint_as_float(p[i >> 1][(i & 1) * 2] & 0xffffff00u) + __uint_as_float(p[2 + (i >> 1)][(i & 1) * 2] & 0xffffff00u);
          v2u qq; qq[0] = pk_bf16(qv[0], qv[1]); qq[1] = pk_bf16(qv[2], qv[3]);
          *(LAS v2u*)(smem + L_QS + lane * 8) = qq;
          break;
        }
        __builtin_amdgcn_s_sleep(2);
      }
      lds_inc(smem, 0, lane);
      STAMP(3);
      if (lane == 0) { v2u t2; t2[0] = (unsigned)tag64; t2[1] = tag_hi; *(LAS v2u*)(smem + L_TAG) = t2; }
    }
    v4u G1[11], WD[18];
    const int cm = bid % 36, rb = bid / 36, mat1 = cm >= 18 ? 1 : 0, cblk = cm - 18 * mat1;
    const int hb = 2 * rb + (hw >> 1);
    const int rg0 = hb * 72 / 7, nrg = (hb + 1) * 72 / 7 - rg0;
    const bool has_s1 = bid < 252;
    const int u0 = 36 * (bid & 127);
    unsigned short gwv[9];
    {
      const rsrc_t gr = mk_rsrc(mat1 ? wu : wg, has_s1 ? D * F * 2 : 0);
      const int go = (4 * rg0 + g) * (F * 2) + cblk * 512 + (hw & 1) * 256 + c * 16;
#pragma unroll
      for (int i = 0; i < 11; ++i) G1[i] = __builtin_amdgcn_raw_buffer_load_b128(gr, go, (i < 10 || nrg == 11) ? i * 4 * (F * 2) : OOB, AUX_NT);
      const rsrc_t dr = mk_rsrc(wd, F * D * 2);
      const int wd_lo = (u0 + g) * 1152 + hw * 256 + c * 16;
      const int wd_hi = (hw == 0 && c < 8) ? wd_lo + 1024 : OOB;
#pragma unroll
      for (int i = 0; i < 9; ++i) WD[i] = __builtin_amdgcn_raw_buffer_load_b128(dr, wd_lo, i * 4608, AUX_NT);
#pragma unroll
      for (int i = 9; i < 18; ++i) WD[i] = __builtin_amdgcn_raw_buffer_load_b128(dr, wd_hi, (i - 9) * 4608, AUX_NT);
#pragma unroll
      for (int i = 0; i < 8; ++i) WO[i] = __builtin_amdgcn_raw_buffer_load_b128(wor, wo_lo, i * 4608, AUX_NT);
#pragma unroll
      for (int i = 8; i < 16; ++i) WO[i] = __builtin_amdgcn_raw_buffer_load_b128(wor, wo_hi, (i - 8) * 4608, AUX_NT);
#pragma unroll
      for (int i = 0; i < 9; ++i) gwv[i] = gw[i * 64 + lane];
    }
    __builtin_amdgcn_sched_barrier(0);
    {
      f32x4 ga = {0.f, 0.f, 0.f, 0.f};
#pragma unroll
      for (int i = 0; i < 11; ++i) {
        const int row = 4 * (rg0 + i) + g;
        const unsigned xv = *(const LAS unsigned*)(smem + L_XS + (row < D ? row : 0) * 4);
        const unsigned t_ = hi8 ? ((xv & 0xffff0000u) | (xv >> 16)) : ((xv << 16) | (xv & 0xffffu));
        v4u bb; bb[0] = t_ & msk[0]; bb[1] = t_ & msk[1]; bb[2] = t_ & msk[2]; bb[3] = t_ & msk[3];
        ga = mfma(G1[i], bb, ga);
      }
      if (hw < 2) {
        *(LAS f32x4*)(smem + L_GU + hw * 1024 + voff) = ga;
        lds_inc(smem, 10, lane);
      } else {
        lds_wait<2>(smem, 10, 2u);
        ga = *(const LAS f32x4*)(smem + L_GU + (hw & 1) * 1024 + voff) + ga;
        __builtin_amdgcn_raw_buffer_store_b128(__builtin_bit_cast(v4u, ga), wsr, has_s1 ? WS_G1 + (((cblk * 2 + (hw & 1)) * 2 + mat1) * 7 + rb) * 1024 + voff : OOB, 0, AUX_SC1);
        DRAIN();
        lds_inc(smem, 1, lane);
      }
    }
    float gt;
    {
      float z = 0.f;
#pragma unroll
      for (int i = 0; i < 9; ++i) {
        const unsigned xv = *(const LAS unsigned*)(smem + L_XS + (i * 64 + lane) * 4);
        z += rbf((b ? bfhi(xv) : bflo(xv)) * bflo(gwv[i]));
      }
      z += dpp<XOR1>(z); z += dpp<XOR2>(z); z += dpp<ROR4>(z); z += dpp<ROR8>(z);
      z = rbf(xrow_sum(z));
      gt = rbf(1.f / (1.f + __expf(-z)));
    }
    unsigned tag_lo = 0, tag_hi = 0;
    if (hw == 3) {
      const v2u tg = *(const LAS v2u*)(smem + L_TAG);
      tag_lo = __builtin_amdgcn_readfirstlane(tg[0]); tag_hi = __builtin_amdgcn_readfirstlane(tg[1]);
      lds_wait<4>(smem, 1, 2u);
      v2u fl; fl[0] = tag_lo; fl[1] = tag_hi;
      __builtin_amdgcn_raw_buffer_store_b64(fl, wsr, (lane == 0 && has_s1) ? WS_F1 + cblk * 128 + (mat1 * 7 + rb) * 8 : OOB, 0, AUX_SC1);
      __builtin_amdgcn_s_sleep(HDELAY);
      {
        const int fblk = lane < 32 ? (u0 >> 8) : ((u0 + 35) >> 8);
        const int fo = WS_F1 + fblk * 128 + ((lane & 31) < 14 ? (lane & 31) : 0) * 8;
        for (unsigned spins = 0;; ++spins) {
          const v2u p = __builtin_amdgcn_raw_buffer_load_b64(wsr, fo, 0, AUX_SC1);
          if (__all(((p[1] ^ tag_hi) | (p[0] ^ tag_lo)) == 0u) || spins > SPIN_CAP) break;
          __builtin_amdgcn_s_sleep(64);
        }
      }
      __builtin_amdgcn_fence(__ATOMIC_ACQUIRE, "wavefront");
      const int u = u0 + 4 * c + g;
      const int so = c < 9 ? WS_G1 + (u >> 7) * 14 * 1024 + (((u >> 5) & 3) * 16 + b * 8 + (u & 7)) * 16 + ((u >> 3) & 3) * 4 : OOB;
      float pv[14];
#pragma unroll
      for (int i = 0; i < 14; ++i) pv[i] = __uint_as_float(__builtin_amdgcn_raw_buffer_load_b32(wsr, so, i * 1024, AUX_SC1));
      const float gv = rbf(((pv[0] + pv[1]) + (pv[2] + pv[3])) + ((pv[4] + pv[5]) + pv[6]));
      const float uv = rbf(((pv[7] + pv[8]) + (pv[9] + pv[10])) + ((pv[11] + pv[12]) + pv[13]));
      const float inner = 0.7978845608028654f * (gv + 0.044715f * gv * gv * gv);
      const float hv = rbf(0.5f * gv * (1.f + tanhf(inner))) * uv;
      *(LAS unsigned*)(smem + L_FFN + lane * 4) = pk_bf16(hv, hv);
      lds_inc(smem, 11, lane);
    }
    f32x4 yf = {0.f, 0.f, 0.f, 0.f}, zf = {0.f, 0.f, 0.f, 0.f};
    {
      lds_wait<16>(smem, 11, 1u);
      const unsigned hp = *(const LAS unsigned*)(smem + L_FFN + lane * 4);
#define PRD(i) { const unsigned t_ = dppu<0x150 + (i)>(hp); \
                 v4u bb; bb[0] = t_ & msk[0]; bb[1] = t_ & msk[1]; bb[2] = t_ & msk[2]; bb[3] = t_ & msk[3]; \
                 yf = mfma(WD[i], bb, yf); zf = mfma(WD[9 + (i)], bb, zf); }
      PRD(0) PRD(1) PRD(2) PRD(3) PRD(4) PRD(5) PRD(6) PRD(7) PRD(8)
#undef PRD
      const float og = rbf(1.f - gt);
      yf *= og; zf *= og;
      asm volatile("" : "+v"(yf), "+v"(zf));
    }
    __builtin_amdgcn_sched_barrier(0);
    if (PTOUCH2 && hw == 2) {
      const unsigned char* tp = ((lane & 8) ? Vc : Kc) + (size_t)grp * (SK * 256) + (size_t)(lane & 7) * (1 << 20) + NSU * 8192;
      if (lane == 16) tp = (const unsigned char*)out;
      if (lane == 17) tp = ws + WS_SG + grp * 131072;
      if (lane == 18) tp = ws + WS_SG + grp * 131072 + 131068;
      if (lane < 19) tch = __builtin_nontemporal_load((const unsigned*)tp);
    }
    asm volatile("" :: "v"(tch));
    STAMP(5);
    if (hw == 3) {
      const unsigned long long tag64 = tag_lo;
      lds_wait<32>(smem, 7, 4u);
      if (lane == 0) {
        v2u fl; fl[0] = (unsigned)tag64; fl[1] = tag_hi;
        __builtin_amdgcn_raw_buffer_store_b64(fl, wsr, WS_SF + bid * 64, 0, AUX_SC1);
      }
      for (unsigned spins = 0;; ++spins) {
        const v2u p = __builtin_amdgcn_raw_buffer_load_b64(wsr, WS_SF + (grp * 8 + (lane & 7)) * 64, 0, AUX_SC1);
        if (__all(((p[1] ^ tag_hi) | (p[0] ^ (unsigned)tag64)) == 0u) || spins > SPIN_CAP) break;
        __builtin_amdgcn_s_sleep(32);
      }
      lds_inc(smem, 9, lane);
      if (is_final) {
        for (unsigned spins = 0;; ++spins) {
          unsigned bad = 0;
#pragma unroll
          for (int i = 0; i < 2; ++i) {
            const v4u p = __builtin_amdgcn_raw_buffer_load_b128(wsr, WS_ST + i * 1024 + voff, 0, AUX_SC1);
            bad |= (p[1] ^ tag_hi) | (p[0] ^ tag_lo) | (p[3] ^ tag_hi) | (p[2] ^ tag_lo);
          }
          if (__all(bad == 0u) || spins > SPIN_CAP) break;
          __builtin_amdgcn_s_sleep(64);
        }
        if (fid == 0 && lane == 0) {
          v2u e; e[0] = tag_lo; e[1] = tag_hi;
          __builtin_amdgcn_raw_buffer_store_b64(e, wsr, WS_E, 0, AUX_SC1);
        }
        lds_inc(smem, 6, lane);
      }
    }
    __builtin_amdgcn_sched_barrier(0);
    DRAIN();
    lds_wait<32>(smem, 2, 1u);
    if (!H1P && hw == 3) gather_o(false);
    lds_wait<0>(smem, 4, 1u);
    const unsigned pp = *(const LAS unsigned*)(smem + L_OS + lane * 4);
    STAMP(9);
    {
      f32x4 y0 = {0.f, 0.f, 0.f, 0.f}, y1 = {0.f, 0.f, 0.f, 0.f}, z0 = {0.f, 0.f, 0.f, 0.f};
#define PRJ(i, y) { const unsigned t_ = dppu<0x150 + (i)>(pp); v4u bb; bb[0] = t_ & msk[0]; bb[1] = t_ & msk[1]; bb[2] = t_ & msk[2]; bb[3] = t_ & msk[3]; \
                    y = mfma(WO[i], bb, y); z0 = mfma(WO[8 + (i)], bb, z0); }
      PRJ(0, y0) PRJ(1, y1) PRJ(2, y0) PRJ(3, y1) PRJ(4, y0) PRJ(5, y1) PRJ(6, y0) PRJ(7, y1)
#undef PRJ
      y0 = (y0 + y1) * gt + yf;
      z0 = z0 * gt + zf;
      const int dst = WS_H2 + ((b * 36 + hw * 8 + 2 * g) * 128 + (bid & 127)) * 64 + c * 4;
      __builtin_amdgcn_raw_buffer_store_b32(__float_as_uint(hi8 ? y0[1] : y0[0]) | 1u, wsr, dst, 0, AUX_SC1);
      __builtin_amdgcn_raw_buffer_store_b32(__float_as_uint(hi8 ? y0[3] : y0[2]) | 1u, wsr, dst, 8192, AUX_SC1);
      if (hw == 0) {
        const int dz = g < 2 ? dst + 32 * 8192 : OOB;
        __builtin_amdgcn_raw_buffer_store_b32(__float_as_uint(hi8 ? z0[1] : z0[0]) | 1u, wsr, dz, 0, AUX_SC1);
        __builtin_amdgcn_raw_buffer_store_b32(__float_as_uint(hi8 ? z0[3] : z0[2]) | 1u, wsr, dz, 8192, AUX_SC1);
      }
    }
    STAMP(15);
  }
  STAMP(11);
  STAMP_FLUSH();
}

extern "C" void kernel_launch(void* const* d_in, const int* in_sizes, int n_in, void* d_out, int out_size, void* d_ws, size_t ws_size,
                              hipStream_t stream) {
  k_fused<<<256, NTHR, 0, stream>>>((const unsigned short*)d_in[0], (const unsigned char*)d_in[1], (const unsigned char*)d_in[2],
                                    (const unsigned char*)d_in[3], (const unsigned char*)d_in[4], (const unsigned char*)d_in[6],
                                    (const unsigned char*)d_in[7], (const unsigned char*)d_in[8], (const unsigned short*)d_in[9],
                                    (unsigned char*)d_ws, (unsigned*)d_out);
}
```

```cpp
#include <hip/hip_runtime.h>
#include <hip/hip_bf16.h>
#include <stdint.h>

typedef unsigned v4u __attribute__((ext_vector_type(4)));
typedef unsigned v2u __attribute__((ext_vector_type(2)));
typedef float f32x4 __attribute__((ext_vector_type(4)));
typedef float f32x2 __attribute__((ext_vector_type(2)));
typedef short bf16x8 __attribute__((ext_vector_type(8)));
typedef __bf16 bf16x2 __attribute__((ext_vector_type(2)));
typedef __amdgpu_buffer_rsrc_t rsrc_t;
#define LAS __attribute__((address_space(3)))

constexpr int D = 576, SK = 32768, F = 4608;
constexpr int NW = 8, NTHR = NW * 64;
#define NSU_V 124
constexpr unsigned NSU = NSU_V;
constexpr unsigned ND = 8 * (128 - NSU);
constexpr unsigned NONE = 1024;
static_assert(NSU % 4 == 0 && (NSU / 4) % 2 == 1, "static V steps per wave: odd");
constexpr int AUX_NT = 2, AUX_SC1 = 16;
constexpr int OOB = 0x40000000;
constexpr unsigned SPIN_CAP = 200000u;
#define H1P 1
#define NE 2
#define STAG 4
#define H1MASK 1
#define TOUCH 0
#define LATE0 0
#define WTS 0
#define WTD 4
#define HDELAY 100
#define SWAPR 0
#define SBATCH 1
#define PACET 1756
#define PCRED 3500
#define KDYN 1
#define PTOUCH 2
#define PTOUCH2 1
#define CPF 1
#define CODE_BYTES 27136
#define NEARD 64
#define PSLEEP 2

constexpr int WS_E = 0;
constexpr int WS_CNT = 4096;
constexpr int WS_QG = 16384;
constexpr int WS_H1 = WS_QG + 131072;
constexpr int H1_STRIDE = 2176;
constexpr int WS_H2 = WS_H1 + 256 * H1_STRIDE;
constexpr int WS_ST = WS_H2 + 144 * 4096;
constexpr int WS_SF = WS_ST + 4096;
constexpr int WS_SG = WS_SF + 256 * 64;
constexpr int WS_F1 = WS_SG + 32 * 1024 * 128;
constexpr int WS_G1 = WS_F1 + 4096;
constexpr int WS_END = WS_G1 + 36 * 14 * 1024;

constexpr int L_XS = 0;
constexpr int L_QP = 2304;
constexpr int L_QS = L_QP + 2048;
constexpr int L_MRG = L_QS + 512;
constexpr int L_ML = L_MRG + 8192;
constexpr int L_OS = L_ML + 96;
constexpr int L_GU = L_OS + 256;
constexpr int L_TAG = L_GU + 2048;
constexpr int L_FFN = L_TAG + 16;
constexpr int L_FB = L_FFN + 256;
constexpr int L_SYNC = L_FB + 256;
constexpr int L_SC = L_SYNC + 48;
constexpr int L_SB = L_SC + 16384;
constexpr int L_END = L_SB + 4096;
constexpr int LDS_BYTES = 96 * 1024;
static_assert(L_END <= LDS_BYTES, "lds");

__device__ __forceinline__ float bflo(unsigned u) { return __builtin_bit_cast(float, u << 16); }
__device__ __forceinline__ float bfhi(unsigned u) { return __builtin_bit_cast(float, u & 0xffff0000u); }
__device__ __forceinline__ unsigned pk_bf16(float lo, float hi) {
  bf16x2 v; v[0] = (__bf16)lo; v[1] = (__bf16)hi; return __builtin_bit_cast(unsigned, v);
}
__device__ __forceinline__ float rbf(float f) { return bflo(pk_bf16(f, f)); }
__device__ __forceinline__ float dot2(unsigned a, unsigned b, float c) {
  return __builtin_amdgcn_fdot2_f32_bf16(__builtin_bit_cast(bf16x2, a), __builtin_bit_cast(bf16x2, b), c, false);
}
template <int CTRL> __device__ __forceinline__ float dpp(float x) {
  return __builtin_bit_cast(float, __builtin_amdgcn_mov_dpp(__builtin_bit_cast(int, x), CTRL, 0xf, 0xf, true));
}
constexpr int XOR1 = 0xB1, XOR2 = 0x4E, HMIR = 0x141, ROR4 = 0x124, ROR8 = 0x128;
__device__ __forceinline__ f32x4 mfma(v4u a, v4u b, f32x4 c) {
  return __builtin_amdgcn_mfma_f32_16x16x32_bf16(__builtin_bit_cast(bf16x8, a), __builtin_bit_cast(bf16x8, b), c, 0, 0, 0);
}
__device__ __forceinline__ float swap16_sum(float a, float b) {
  auto s = __builtin_amdgcn_permlane16_swap(__float_as_uint(a), __float_as_uint(b), false, false);
  return __uint_as_float(s[0]) + __uint_as_float(s[1]);
}
__device__ __forceinline__ float swap32_sum(float a, float b) {
  auto s = __builtin_amdgcn_permlane32_swap(__float_as_uint(a), __float_as_uint(b), false, false);
  return __uint_as_float(s[0]) + __uint_as_float(s[1]);
}
__device__ __forceinline__ float xrow_max(float x) {
  auto s = __builtin_amdgcn_permlane16_swap(__float_as_uint(x), __float_as_uint(x), false, false);
  x = fmaxf(__uint_as_float(s[0]), __uint_as_float(s[1]));
  auto t = __builtin_amdgcn_permlane32_swap(__float_as_uint(x), __float_as_uint(x), false, false);
  return fmaxf(__uint_as_float(t[0]), __uint_as_float(t[1]));
}
__device__ __forceinline__ float xrow_sum(float x) { x = swap16_sum(x, x); return swap32_sum(x, x); }
__device__ __forceinline__ rsrc_t mk_rsrc(const void* p, int bytes) { return __builtin_amdgcn_make_buffer_rsrc((void*)p, 0, bytes, 0x00020000); }
#define BAR() do { asm volatile("s_waitcnt lgkmcnt(0)" ::: "memory"); __builtin_amdgcn_s_barrier(); asm volatile("" ::: "memory"); } while (0)
#define DRAIN() do { __builtin_amdgcn_s_waitcnt(0x0F70); asm volatile("s_waitcnt vmcnt(0)" ::: "memory"); } while (0)
#define WAITV(n) do { __builtin_amdgcn_s_waitcnt(0x0F70 | ((n) & 15) | (((n) >> 4) << 14)); asm volatile("" ::: "memory"); } while (0)
#define LDSW() asm volatile("s_waitcnt lgkmcnt(0)" ::: "memory")

#define STAMP(i) do {} while (0)
#define STAMP_FLUSH() do {} while (0)
constexpr float SCALE = 0.08838834764831845f, L2E = 1.4426950408889634f;

typedef double f64x4 __attribute__((ext_vector_type(4)));
struct St { float m; double l; f64x4 acc; };
__device__ __forceinline__ void pin(St& st) { asm volatile("" : "+v"(st.m), "+v"(st.l), "+v"(st.acc) :: "memory"); }
__device__ __forceinline__ v4u diag(float v, const v4u& msk) {
  const unsigned pp = pk_bf16(v, v);
  v4u bb; bb[0] = pp & msk[0]; bb[1] = pp & msk[1]; bb[2] = pp & msk[2]; bb[3] = pp & msk[3];
  return bb;
}

__device__ __forceinline__ v2u scores(const v4u* KB, const v4u& q0, const v4u& q1, bool hi8) {
  float s[4];
#pragma unroll
  for (int t = 0; t < 4; ++t) {
    float a0 = dot2(KB[t][0], q0[0], 0.f), a1 = dot2(KB[t][0], q1[0], 0.f);
#pragma unroll
    for (int k = 1; k < 4; ++k) { a0 = dot2(KB[t][k], q0[k], a0); a1 = dot2(KB[t][k], q1[k], a1); }
    const float keep = hi8 ? a1 : a0, send = hi8 ? a0 : a1;
    float v = keep + dpp<ROR8>(send);
    v += dpp<HMIR>(v); v += dpp<XOR2>(v); v += dpp<XOR1>(v);
    s[t] = v;
  }
  const unsigned u0 = pk_bf16(s[0], s[1]), u1 = pk_bf16(s[2], s[3]);
  v2u r; r[0] = pk_bf16(bflo(u0) * SCALE, bfhi(u0) * SCALE); r[1] = pk_bf16(bflo(u1) * SCALE, bfhi(u1) * SCALE);
  return r;
}
__device__ __forceinline__ void pv_unit(const v4u (&VB)[8], const v4u sc, const v4u& msk, St& st) {
  float s[8];
#pragma unroll
  for (int t = 0; t < 4; ++t) { s[2 * t] = bflo(sc[t]); s[2 * t + 1] = bfhi(sc[t]); }
  float tm = fmaxf(fmaxf(fmaxf(s[0], s[1]), fmaxf(s[2], s[3])), fmaxf(fmaxf(s[4], s[5]), fmaxf(s[6], s[7])));
  tm = xrow_max(tm);
  const float tl = tm * L2E, mt = __builtin_ceilf(tl), fr = tl - mt;
  const float mn = fmaxf(st.m, mt);
  const double alpha = (double)__builtin_amdgcn_exp2f(st.m - mn);
  const float beta = __builtin_amdgcn_exp2f(mt - mn);
  st.m = mn;
  float p[8];
#pragma unroll
  for (int t = 0; t < 8; ++t) p[t] = __builtin_amdgcn_exp2f(fmaf(s[t] - tm, L2E, fr));
  f32x4 ta = {0.f, 0.f, 0.f, 0.f};
#pragma unroll
  for (int t = 0; t < 8; ++t) ta = mfma(VB[t], diag(p[t], msk), ta);
  st.l = __builtin_fma(st.l, alpha, (double)((((p[0] + p[1]) + (p[2] + p[3])) + ((p[4] + p[5]) + (p[6] + p[7]))) * beta));
#pragma unroll
  for (int r = 0; r < 4; ++r) st.acc[r] = __builtin_fma(st.acc[r], alpha, (double)(ta[r] * beta));
}
__device__ __forceinline__ void issue(v4u (&B)[8], rsrc_t r, int voff, unsigned unit) {
  const int so = (int)(unit * 8192u);
#pragma unroll
  for (int t = 0; t < 4; ++t) B[t] = __builtin_amdgcn_raw_buffer_load_b128(r, voff + t * 1024, so, AUX_NT);
#pragma unroll
  for (int t = 0; t < 4; ++t) B[4 + t] = __builtin_amdgcn_raw_buffer_load_b128(r, voff + t * 1024, so + 4096, AUX_NT);
}
__device__ __forceinline__ unsigned atomic_inc(rsrc_t r, int voff0, bool on, int so) {
  return (unsigned)__builtin_amdgcn_raw_ptr_buffer_atomic_add_i32(1, r, voff0, on ? so : OOB, 0);
}
__device__ __forceinline__ unsigned lds_ticket(unsigned lds_addr_v, unsigned vone) {
  unsigned long long keep; unsigned r, s;
  asm volatile("s_mov_b64 %1, exec\n\ts_mov_b64 exec, 1\n\tds_add_rtn_u32 %0, %3, %4\n\ts_mov_b64 exec, %1\n\ts_waitcnt lgkmcnt(0)\n\tv_readfirstlane_b32 %2, %0\n\ts_nop 1"
               : "=&v"(r), "=&s"(keep), "=s"(s) : "v"(lds_addr_v), "v"(vone) : "memory");
  return s;
}
template <int CTRL> __device__ __forceinline__ unsigned dppu(unsigned x) { return (unsigned)__builtin_amdgcn_mov_dpp((int)x, CTRL, 0xf, 0xf, true); }
template <int CTRL> __device__ __forceinline__ double dppd(double x) {
  const v2u u = __builtin_bit_cast(v2u, x);
  v2u r; r[0] = dppu<CTRL>(u[0]); r[1] = dppu<CTRL>(u[1]);
  return __builtin_bit_cast(double, r);
}
__device__ __forceinline__ double xrow_sumd(double x) {
  v2u u = __builtin_bit_cast(v2u, x), a, b2;
  { auto s0 = __builtin_amdgcn_permlane16_swap(u[0], u[0], false, false); auto s1 = __builtin_amdgcn_permlane16_swap(u[1], u[1], false, false);
    a[0] = s0[0]; a[1] = s1[0]; b2[0] = s0[1]; b2[1] = s1[1]; }
  x = __builtin_bit_cast(double, a) + __builtin_bit_cast(double, b2);
  u = __builtin_bit_cast(v2u, x);
  { auto s0 = __builtin_amdgcn_permlane32_swap(u[0], u[0], false, false); auto s1 = __builtin_amdgcn_permlane32_swap(u[1], u[1], false, false);
    a[0] = s0[0]; a[1] = s1[0]; b2[0] = s0[1]; b2[1] = s1[1]; }
  return __builtin_bit_cast(double, a) + __builtin_bit_cast(double, b2);
}
__device__ __forceinline__ void lds_inc(unsigned char* smem, int idx, int lane) {
  LDSW();
  if (lane == 0) __hip_atomic_fetch_add((LAS unsigned*)(LAS unsigned char*)smem + (L_SYNC / 4 + idx), 1u, __ATOMIC_RELAXED, __HIP_MEMORY_SCOPE_WORKGROUP);
}
template <int SLEEP> __device__ __forceinline__ void lds_wait(unsigned char* smem, int idx, unsigned want) {
#pragma unroll 1
  for (unsigned spins = 0; spins < 8000000u / (1 + SLEEP); ++spins) {
    const unsigned v = __builtin_amdgcn_readfirstlane(*((volatile LAS unsigned*)(LAS unsigned char*)smem + (L_SYNC / 4 + idx)));
    if (v >= want) break;
    if (SLEEP) __builtin_amdgcn_s_sleep(SLEEP);
  }
  asm volatile("" ::: "memory");
}

__device__ __forceinline__ void fma8(float (&acc)[8], float xv, const v4u& w) {
#pragma unroll
  for (int k = 0; k < 4; ++k) { acc[2 * k] = fmaf(xv, bflo(w[k]), acc[2 * k]); acc[2 * k + 1] = fmaf(xv, bfhi(w[k]), acc[2 * k + 1]); }
}
template <bool BIT2> __device__ __forceinline__ f32x2 red8(const float (&acc)[8]) {
  float a[4];
#pragma unroll
  for (int j = 0; j < 4; ++j) a[j] = swap32_sum(acc[j], acc[j + 4]);
  float b0 = swap16_sum(a[0], a[2]), b1 = swap16_sum(a[1], a[3]);
  if (BIT2) { b0 += dpp<ROR4>(b0); b1 += dpp<ROR4>(b1); }
  b0 += dpp<ROR8>(b0); b1 += dpp<ROR8>(b1);
  f32x2 r; r[0] = b0; r[1] = b1; return r;
}
__device__ __forceinline__ float val(unsigned u) { return __uint_as_float(u & ~1u); }
__device__ __forceinline__ bool all4(const v4u& p) { return (p[0] & p[1] & p[2] & p[3] & 1u) != 0u; }

__global__ void __launch_bounds__(NTHR, 2)
k_fused(const unsigned short* __restrict__ x, const unsigned char* Kc, const unsigned char* Vc,
        const unsigned char* __restrict__ wq, const unsigned char* wo, const unsigned char* wg,
        const unsigned char* wu, const unsigned char* wd, const unsigned short* __restrict__ gw,
        unsigned char* ws, unsigned* out) {
  __shared__ __attribute__((aligned(16))) unsigned char smem[LDS_BYTES];
  const int tid = SWAPR ? (threadIdx.x ^ 256) : threadIdx.x, lane = tid & 63, w = __builtin_amdgcn_readfirstlane(tid >> 6);
  const int bid = blockIdx.x, grp = bid >> 3, sp = bid & 7, b = grp >> 4, kvh = grp & 15;
  const int g = lane >> 4, c = lane & 15;
  unsigned tsv = 0xffffffffu; (void)tsv;
  STAMP(0);
  const int voff = lane * 16;
  v4u cpf = {0u, 0u, 0u, 0u};
  if (CPF) {
    const unsigned long long pc0 = __builtin_amdgcn_s_getpc() & ~255ull;
    const rsrc_t cr = mk_rsrc((const void*)pc0, CODE_BYTES);
    cpf = __builtin_amdgcn_raw_buffer_load_b128(cr, (w == 0) ? grp * 1024 + voff : OOB, 0, 0);
  }
  const int ridx = (grp + 4 * sp) & 31;
  const bool is_final = ridx < 9;
  const int fid = sp * 9 + ridx;
  const bool hi8 = (c & 8) != 0;
#define MAKE_MSK() v4u msk; _Pragma("unroll") for (int d = 0; d < 4; ++d) msk[d] = (((c & 7) >> 1) == d) ? ((c & 1) ? 0xffff0000u : 0x0000ffffu) : 0u;

  auto gather_o = [&](bool watch_flag) {
    const rsrc_t wsr = mk_rsrc(ws, WS_END);
    const int o_acc = WS_H1 + (grp * 8 + 4 * (c >> 3)) * H1_STRIDE + (32 * sp + 8 * g + (c & 7)) * 8;
    const int o_ml = WS_H1 + (grp * 8 + (c & 7)) * H1_STRIDE + 2048 + (sp >> 2) * 16;
    v2u pv[4];
    v4u pm = {0u, 0u, 0u, 0u};
#pragma unroll
    for (int i = 0; i < 4; ++i) pv[i] = v2u{0u, 0u};
    for (unsigned spins = 0;; ++spins) {
      if (H1MASK) {
        if (!(pv[0][0] & pv[1][0] & pv[2][0] & pv[3][0] & 1u)) {
#pragma unroll
          for (int i = 0; i < 4; ++i) pv[i] = __builtin_amdgcn_raw_buffer_load_b64(wsr, o_acc, i * H1_STRIDE, AUX_SC1);
        }
        if (!(pm[0] & pm[2] & 1u)) pm = __builtin_amdgcn_raw_buffer_load_b128(wsr, o_ml, 0, AUX_SC1);
      } else {
#pragma unroll
        for (int i = 0; i < 4; ++i) pv[i] = __builtin_amdgcn_raw_buffer_load_b64(wsr, o_acc, i * H1_STRIDE, AUX_SC1);
        pm = __builtin_amdgcn_raw_buffer_load_b128(wsr, o_ml, 0, AUX_SC1);
      }
      if (__all((pv[0][0] & pv[1][0] & pv[2][0] & pv[3][0] & pm[0] & pm[2] & 1u) != 0u) || spins > SPIN_CAP) break;
      if (watch_flag && __builtin_amdgcn_readfirstlane(*((volatile LAS unsigned*)(smem + L_SYNC) + 4)) != 0u) return;
      __builtin_amdgcn_s_sleep(PSLEEP);
    }
    STAMP(8);
    const float Ms = __uint_as_float(pm[1]);
    float Mx = fmaxf(Ms, dpp<XOR1>(Ms)); Mx = fmaxf(Mx, dpp<XOR2>(Mx)); Mx = fmaxf(Mx, dpp<HMIR>(Mx));
    const float wt = __builtin_amdgcn_exp2f(Ms - Mx);
    v2u lv; lv[0] = pm[2]; lv[1] = pm[3];
    double den = (double)wt * __builtin_bit_cast(double, lv);
    den += dppd<XOR1>(den); den += dppd<XOR2>(den); den += dppd<HMIR>(den);
    double num = 0.0;
#define ACC1(i) { const float wl_ = dpp<0x150 + (i)>(wt), wh_ = dpp<0x154 + (i)>(wt); num = __builtin_fma((double)(hi8 ? wh_ : wl_), __builtin_bit_cast(double, pv[i]), num); }
    ACC1(0) ACC1(1) ACC1(2) ACC1(3)
#undef ACC1
    num += dppd<ROR8>(num);
    const float ov = (float)num / (float)den;
    *(LAS unsigned*)(smem + L_OS + lane * 4) = pk_bf16(ov, ov);
    lds_inc(smem, 4, lane);
  };

  if (w >= 4) {
    const int sw = w - 4, stid = tid - 256;
    const rsrc_t kr = mk_rsrc(Kc + (size_t)grp * (SK * 256), SK * 256);
    const rsrc_t vr = mk_rsrc(Vc + (size_t)grp * (SK * 256), SK * 256);
    const rsrc_t wsr = mk_rsrc(ws, WS_END);
    const unsigned ubase = (unsigned)(sp * 128 + sw);
    v4u Ba[8], Bb[8], Be[NE][8];
    if (!LATE0) issue(Be[0], kr, voff, ubase);
    __builtin_amdgcn_sched_barrier(0);
    asm volatile("" ::: "memory");
    __builtin_amdgcn_s_barrier();
    asm volatile("" ::: "memory");
    if (LATE0) issue(Be[0], kr, voff, ubase);
    __builtin_amdgcn_sched_barrier(0);
    STAMP(1);
    unsigned tch = 0;
    if (PTOUCH == 2 && sw == 0) {
      const int cm_ = bid % 36, rb_ = bid / 36;
      const unsigned char* tp = Vc + (size_t)bid * (1 << 20);
      if (lane == 1) tp = (cm_ >= 18 ? wu : wg) + (size_t)(rb_ < 7 ? (2 * rb_) * 72 / 7 * 4 : 0) * (F * 2);
      if (lane == 2) tp = (cm_ >= 18 ? wu : wg) + (size_t)(rb_ < 7 ? (2 * rb_ + 2) * 72 / 7 * 4 - 1 : 0) * (F * 2);
      if (lane == 3) tp = wd + (size_t)(36 * (bid & 127)) * 1152;
      if (lane == 4) tp = wo + (size_t)(kvh * 256 + 32 * sp) * 1152;
      if (lane == 5) tp = ws + WS_G1;
      if (lane == 6) tp = (const unsigned char*)gw;
      if (lane < 7) tch = __builtin_nontemporal_load((const unsigned*)tp);
    }
    asm volatile("" :: "v"(tch));
    MAKE_MSK();
#pragma unroll
    for (int e = 1; e < NE; ++e) {
      DRAIN();
      issue(Be[e], kr, voff, ubase + 4 * e);
      __builtin_amdgcn_sched_barrier(0);
    }
    DRAIN();
    issue(Ba, kr, voff, ubase + 4 * NE);
    __builtin_amdgcn_sched_barrier(0);
    DRAIN();
    issue(Bb, kr, voff, ubase + 4 * NE + 4);
    __builtin_amdgcn_sched_barrier(0);
    STAMP(2);
    lds_wait<1>(smem, 0, 1u);
    STAMP(3);
    const v4u q0 = *(const LAS v4u*)(smem + L_QS + c * 16), q1 = *(const LAS v4u*)(smem + L_QS + 256 + c * 16);
    const int sc_off = ((c >> 3) * 4 + g) * 16;
    const int sg_st = (c & 7) == 0 ? WS_SG + grp * 131072 + sc_off : OOB;
#define KSC(B) const v2u s0_ = scores(&B[0], q0, q1, hi8), s1_ = scores(&B[4], q0, q1, hi8);                        \
      v4u sc_; sc_[0] = s0_[0]; sc_[1] = s0_[1]; sc_[2] = s1_[0]; sc_[3] = s1_[1];
#define KSTEP(B, i)                                                                                                \
    {                                                                                                              \
      KSC(B)                                                                                                       \
      if ((c & 7) == 0) *(LAS v4u*)(smem + L_SC + (4 * (i) + sw) * 128 + sc_off) = sc_;                            \
      asm volatile("" ::: "memory");                                                                               \
      __builtin_amdgcn_sched_barrier(0);                                                                           \
    }
    int nsl = 0;
    unsigned uid = 0;
#define SFLUSH()                                                                                                   \
    {                                                                                                              \
      const v4u v_ = *(const LAS v4u*)(smem + L_SB + sw * 1024 + voff);                                            \
      __builtin_amdgcn_raw_buffer_store_b128(v_, wsr, (lane >> 3) < nsl ? WS_SG + grp * 131072 + (int)(uid * 128u) + (lane & 7) * 16 : OOB, 0, AUX_SC1); \
      nsl = 0;                                                                                                     \
    }
#define KSTEPG(B, u)                                                                                               \
    {                                                                                                              \
      KSC(B)                                                                                                       \
      if ((c & 7) == 0) *(LAS v4u*)(smem + L_SB + sw * 1024 + nsl * 128 + sc_off) = sc_;                           \
      uid = (lane >> 3) == nsl ? (u) : uid;                                                                        \
      ++nsl;                                                                                                       \
      if (nsl == 8) SFLUSH()                                                                                       \
      asm volatile("" ::: "memory");                                                                               \
      __builtin_amdgcn_sched_barrier(0);                                                                           \
    }
    const int cnt0 = lane == 0 ? WS_CNT + grp * 256 : OOB;
    unsigned vone = 1u, a = 0u;
    unsigned taddr = (unsigned)(size_t)(LAS unsigned char*)(smem + L_SYNC + 28);
    asm volatile("" : "+v"(vone), "+v"(taddr));
    KSTEP(Ba, NE);
    issue(Ba, kr, voff, ubase + 4 * (NE + 2));
    __builtin_amdgcn_sched_barrier(0);
#pragma unroll
    for (int e = 0; e < NE; ++e) KSTEP(Be[e], e);
    KSTEP(Bb, NE + 1);
    static_assert(NE % 2 == 0, "K steps left: odd");
#define PACE() do { int lag_; while ((lag_ = (int)((unsigned)__builtin_amdgcn_s_memrealtime() * 16u - due)) < 0) __builtin_amdgcn_s_sleep(1); \
                    if (PCRED && lag_ > PCRED) due += (unsigned)(lag_ - PCRED); due += PACET; } while (0)
    unsigned due = (unsigned)__builtin_amdgcn_s_memrealtime() * 16u;
#pragma unroll 1
    for (int i = NE + 2; i < (int)(NSU / 4) - 1; i += 2) {
      PACE();
      DRAIN();
      issue(Bb, kr, voff, ubase + 4 * (i + 1));
      __builtin_amdgcn_sched_barrier(0);
      KSTEP(Ba, i);
      PACE();
      DRAIN();
      if (KDYN) a = atomic_inc(wsr, cnt0, i + 3 >= (int)(NSU / 4), 0);
      issue(Ba, kr, voff, ubase + 4 * (i + 2));
      __builtin_amdgcn_sched_barrier(0);
      KSTEP(Bb, i + 1);
    }
#define MAPD(d) (((d) & 7u) * 128u + NSU + ((d) >> 3))
#define WAITA() DRAIN()
    {
      unsigned ua, ub;
      if (!KDYN) PACE();
      WAITA();
      unsigned d = KDYN ? __builtin_amdgcn_readfirstlane(a) : (unsigned)(sw * 8 + sp);
      if (KDYN) a = atomic_inc(wsr, cnt0, d < ND, 0);
      ub = MAPD(d);
      issue(Bb, d < ND ? kr : vr, voff, d < ND ? ub : ubase);
      __builtin_amdgcn_sched_barrier(0);
      KSTEP(Ba, NSU / 4 - 1);
      bool in_b = true;
      while (d < ND) {
        if (!KDYN) PACE();
      WAITA();
        d = KDYN ? __builtin_amdgcn_readfirstlane(a) : d + 32u;
        if (KDYN) a = atomic_inc(wsr, cnt0, d < ND, 0);
        ua = MAPD(d);
        issue(Ba, d < ND ? kr : vr, voff, d < ND ? ua : ubase);
        __builtin_amdgcn_sched_barrier(0);
        KSTEPG(Bb, ub);
        if (d >= ND) { in_b = false; break; }
        if (!KDYN) PACE();
      WAITA();
        d = KDYN ? __builtin_amdgcn_readfirstlane(a) : d + 32u;
        if (KDYN) a = atomic_inc(wsr, cnt0, d < ND, 0);
        ub = MAPD(d);
        issue(Bb, d < ND ? kr : vr, voff, d < ND ? ub : ubase);
        __builtin_amdgcn_sched_barrier(0);
        KSTEPG(Ba, ua);
      }
      STAMP(4);
      SFLUSH()
      DRAIN();
      if (in_b) {
#pragma unroll
        for (int t = 0; t < 8; ++t) Ba[t] = Bb[t];
      }
    }
#undef KSTEP
#undef KSTEPG
#undef SFLUSH
#undef KSC
    St st; st.m = -INFINITY; st.l = 0.0; st.acc = f64x4{0.0, 0.0, 0.0, 0.0};
#define LSC(i) (*(const LAS v4u*)(smem + L_SC + (4 * (i) + sw) * 128 + sc_off))
    lds_inc(smem, 7, lane);
    if (KDYN) due = (unsigned)__builtin_amdgcn_s_memrealtime() * 16u;
#pragma unroll 1
    for (int i = 0; i < (int)(NSU / 4) - 1; i += 2) {
      PACE();
      DRAIN();
      issue(Bb, vr, voff, ubase + 4 * (i + 1));
      __builtin_amdgcn_sched_barrier(0);
      pv_unit(Ba, LSC(i), msk, st); pin(st);
      __builtin_amdgcn_sched_barrier(0);
      PACE();
      DRAIN();
      a = atomic_inc(wsr, cnt0, i + 3 >= (int)(NSU / 4), 4);
      issue(Ba, vr, voff, ubase + 4 * (i + 2));
      __builtin_amdgcn_sched_barrier(0);
      pv_unit(Bb, LSC(i + 1), msk, st); pin(st);
      __builtin_amdgcn_sched_barrier(0);
    }
    lds_wait<2>(smem, 9, 1u);
    v4u sa = LSC(NSU / 4 - 1), sb;
#undef LSC
    const int sg_ld = WS_SG + grp * 131072 + sc_off;
#define NEXT(nx)                                                                                                   \
    {                                                                                                              \
      const unsigned d_ = __builtin_amdgcn_readfirstlane(a);                                                       \
      nx = d_ < ND ? (d_ & 7u) * 128u + NSU + (d_ >> 3) : NONE;                                                    \
      if (d_ + NEARD >= ND) *((volatile LAS unsigned*)(smem + L_SYNC) + 2) = 1u;                                   \
      a = atomic_inc(wsr, cnt0, d_ < ND, 4);                                                               \
    }
    while (true) {
      unsigned nx;
      DRAIN();
      NEXT(nx);
      sb = __builtin_amdgcn_raw_buffer_load_b128(wsr, sg_ld, nx < NONE ? (int)(nx * 128u) : OOB, AUX_SC1);
      issue(Bb, vr, voff, nx);
      __builtin_amdgcn_sched_barrier(0);
      pv_unit(Ba, sa, msk, st); pin(st);
      __builtin_amdgcn_sched_barrier(0);
      if (nx >= NONE) break;
      DRAIN();
      NEXT(nx);
      sa = __builtin_amdgcn_raw_buffer_load_b128(wsr, sg_ld, nx < NONE ? (int)(nx * 128u) : OOB, AUX_SC1);
      issue(Ba, vr, voff, nx);
      __builtin_amdgcn_sched_barrier(0);
      pv_unit(Bb, sb, msk, st); pin(st);
      __builtin_amdgcn_sched_barrier(0);
      if (nx >= NONE) break;
    }
#undef NEXT
    STAMP(6);
    {
      const double l = xrow_sumd(st.l);
      const int h = c >> 3;
#pragma unroll
      for (int r = 0; r < 4; ++r) {
        const int dim = 8 * (4 * g + r) + (c & 7);
        *(LAS double*)(smem + L_MRG + (sw * 256 + h * 128 + (dim & 96) + (dim & 3) * 8 + ((dim >> 2) & 7)) * 8) = st.acc[r];
      }
      if (g == 0 && (c & 7) == 0) { *(LAS float*)(smem + L_ML + (h * 4 + sw) * 4) = st.m; *(LAS double*)(smem + L_ML + 32 + (h * 4 + sw) * 8) = l; }
    }
    lds_inc(smem, 3, lane);
    lds_wait<0>(smem, 3, 4u);
    STAMP(7);
    {
      const int hh = sw >> 1;
      const f32x4 m4 = *(const LAS f32x4*)(smem + L_ML + hh * 16);
      const f64x4 l4 = *(const LAS f64x4*)(smem + L_ML + 32 + hh * 32);
      double a4[4];
#pragma unroll
      for (int ww = 0; ww < 4; ++ww) a4[ww] = *(const LAS double*)(smem + L_MRG + (ww * 256 + stid) * 8);
      const float M = fmaxf(fmaxf(m4[0], m4[1]), fmaxf(m4[2], m4[3]));
      double L = 0.0, o = 0.0;
#pragma unroll
      for (int ww = 0; ww < 4; ++ww) {
        const double wt = (double)__builtin_amdgcn_exp2f(m4[ww] - M);
        L = __builtin_fma(wt, l4[ww], L); o = __builtin_fma(wt, a4[ww], o);
      }
      v2u ov = __builtin_bit_cast(v2u, o); ov[0] |= 1u;
      __builtin_amdgcn_raw_buffer_store_b64(ov, wsr, WS_H1 + bid * H1_STRIDE + stid * 8, 0, AUX_SC1);
      if ((stid & 127) == 0) {
        const v2u lv = __builtin_bit_cast(v2u, L);
        v4u ml; ml[0] = 1u; ml[1] = __float_as_uint(M); ml[2] = lv[0] | 1u; ml[3] = lv[1];
        __builtin_amdgcn_raw_buffer_store_b128(ml, wsr, WS_H1 + bid * H1_STRIDE + 2048 + hh * 16, 0, AUX_SC1);
      }
    }
    STAMP(14);
    if (H1P) {
      for (int i = 0; i < sw; ++i) __builtin_amdgcn_s_sleep(STAG);
      gather_o(true);
    }
    if (is_final) {
      const int src = WS_H2 + fid * 8192 + sw * 2048 + voff;
      lds_wait<8>(smem, 6, 1u);
      v4u p0, p1;
      for (unsigned spins = 0;; ++spins) {
        p0 = __builtin_amdgcn_raw_buffer_load_b128(wsr, src, 0, AUX_SC1);
        p1 = __builtin_amdgcn_raw_buffer_load_b128(wsr, src, 1024, AUX_SC1);
        const unsigned m = (p0[0] & p0[1] & p0[2] & p0[3]) & (p1[0] & p1[1] & p1[2] & p1[3]) & 1u;
        if (__all(m != 0u) || spins > SPIN_CAP) break;
        __builtin_amdgcn_s_sleep(PSLEEP);
      }
      STAMP(10);
      f32x4 t4;
#pragma unroll
      for (int j = 0; j < 4; ++j) {
        float t = val(p0[j]) + val(p1[j]);
        t += dpp<ROR4>(t); t += dpp<ROR8>(t);
        t4[j] = xrow_sum(t);
      }
      if (lane < 4) *(LAS f32x4*)(smem + L_FB + (sw * 4 + lane) * 16) = t4;
      LDSW();
      if (lds_ticket(taddr + 4, vone) == 3u && lane < 8) {
        f32x2 t = *(const LAS f32x2*)(smem + L_FB + lane * 8);
#pragma unroll
        for (int ww = 1; ww < 4; ++ww) t += *(const LAS f32x2*)(smem + L_FB + ww * 64 + lane * 8);
        out[fid * 8 + lane] = pk_bf16(t[0], t[1]);
      }
    }
  } else {
    const int hw = w;
    unsigned short xq[9];
    v4u wqv[9];
    {
      const int r = lane >> 3, row0 = 288 * (sp & 1) + 72 * hw;
      const rsrc_t qr = mk_rsrc(wq, D * 8192);
      const int base = (row0 + r) * 8192 + (kvh * 256 + (sp >> 1) * 64) * 2 + (lane & 7) * 16;
#pragma unroll
      for (int i = 0; i < 9; ++i) wqv[i] = __builtin_amdgcn_raw_buffer_load_b128(qr, base, i * 8 * 8192, AUX_NT);
#pragma unroll
      for (int i = 0; i < 9; ++i) xq[i] = x[b * D + row0 + 8 * i + r];
    }
    const int xi = tid < 144 ? tid : 143;
    const v2u xa = ((const v2u*)x)[xi], xb = ((const v2u*)x)[144 + xi];
    if (tid < 3) { const v4u z = {0u, 0u, 0u, 0u}; *(LAS v4u*)(smem + L_SYNC + tid * 16) = z; }
    __builtin_amdgcn_sched_barrier(0);
    BAR();
    STAMP(1);
    const rsrc_t wsr = mk_rsrc(ws, WS_END);
    unsigned tch = 0;
    if (PTOUCH == 1 && hw == 1) {
      const int cm_ = bid % 36, rb_ = bid / 36;
      const unsigned char* tp = Vc + (size_t)bid * (1 << 20);
      if (lane == 1) tp = (cm_ >= 18 ? wu : wg) + (size_t)(rb_ < 7 ? (2 * rb_) * 72 / 7 * 4 : 0) * (F * 2);
      if (lane == 2) tp = (cm_ >= 18 ? wu : wg) + (size_t)(rb_ < 7 ? (2 * rb_ + 2) * 72 / 7 * 4 - 1 : 0) * (F * 2);
      if (lane == 3) tp = wd + (size_t)(36 * (bid & 127)) * 1152;
      if (lane == 4) tp = wo + (size_t)(kvh * 256 + 32 * sp) * 1152;
      if (lane == 5) tp = ws + WS_G1;
      if (lane == 6) tp = (const unsigned char*)gw;
      if (lane < 7) tch = __builtin_nontemporal_load((const unsigned*)tp);
    }
    v2u evv = {0u, 0u};
    if (hw == 3) {
      evv = __builtin_amdgcn_raw_buffer_load_b64(wsr, WS_E, 0, AUX_SC1);
      const v4u z = {0u, 0u, 0u, 0u};
      __builtin_amdgcn_raw_buffer_store_b128(z, wsr, WS_H1 + bid * H1_STRIDE + voff, 0, AUX_SC1);
      __builtin_amdgcn_raw_buffer_store_b128(z, wsr, WS_H1 + bid * H1_STRIDE + 1024 + voff, 0, AUX_SC1);
      const int h2o = WS_H2 + (b * 36 * 128 + (bid & 127)) * 64 + (lane >> 2) * 8192 + (lane & 3) * 16;
      __builtin_amdgcn_raw_buffer_store_b128(z, wsr, h2o, 0, AUX_SC1);
      __builtin_amdgcn_raw_buffer_store_b128(z, wsr, h2o, 16 * 8192, AUX_SC1);
      int off = h2o + 32 * 8192;
      if (lane >= 16) off = WS_H1 + bid * H1_STRIDE + 2048 + (lane & 1) * 16;
      if (lane >= 32) off = sp == 0 ? WS_CNT + grp * 256 : OOB;
      __builtin_amdgcn_raw_buffer_store_b128(z, wsr, off, 0, AUX_SC1);
    }
    __builtin_amdgcn_sched_barrier(0);
    {
      float acc[8];
#pragma unroll
      for (int j = 0; j < 8; ++j) acc[j] = 0.f;
#pragma unroll
      for (int i = 0; i < 9; ++i) fma8(acc, bflo(xq[i]), wqv[i]);
      const f32x2 r2 = red8<false>(acc);
      if ((lane & 8) == 0) *(LAS f32x2*)(smem + L_QP + (hw * 64 + (lane & 7) * 8 + 4 * (lane >> 5) + 2 * ((lane >> 4) & 1)) * 4) = r2;
      if (tid < 144) {
        v4u xs4;
        xs4[0] = (xa[0] & 0xffffu) | (xb[0] << 16); xs4[1] = (xa[0] >> 16) | (xb[0] & 0xffff0000u);
        xs4[2] = (xa[1] & 0xffffu) | (xb[1] << 16); xs4[3] = (xa[1] >> 16) | (xb[1] & 0xffff0000u);
        *(LAS v4u*)(smem + L_XS + tid * 16) = xs4;
      }
      lds_inc(smem, 5, lane);
      lds_wait<0>(smem, 5, 4u);
    }
    STAMP(12);
    MAKE_MSK();
    v4u WO[16];
    const rsrc_t wor = mk_rsrc(wo, 4096 * D * 2);
    const int wo_lo = (kvh * 256 + 32 * sp + g) * 1152 + hw * 256 + c * 16;
    const int wo_hi = (hw == 0 && c < 8) ? wo_lo + 1024 : OOB;
    if (hw == 3) {
      const unsigned long long ev = ((unsigned long long)(unsigned)__builtin_amdgcn_readfirstlane(evv[1]) << 32) | (unsigned)__builtin_amdgcn_readfirstlane(evv[0]);
      const unsigned long long tag64 = ev * 6364136223846793005ull + 1442695040888963407ull;
      const unsigned tag_hi = (unsigned)(tag64 >> 32);
      const unsigned tag_lo8 = ((unsigned)tag64 >> 24) & 0xffu;
      {
        float s = 0.f;
#pragma unroll
        for (int ww = 0; ww < 4; ++ww) s += *(const LAS float*)(smem + L_QP + (ww * 64 + lane) * 4);
        DRAIN();
        v2u gr; gr[0] = ((__float_as_uint(s) + 0x80u) & 0xffffff00u) | tag_lo8; gr[1] = tag_hi;
        __builtin_amdgcn_raw_buffer_store_b64(gr, wsr, WS_QG + ((grp * 2 + (sp & 1)) * 256 + (sp >> 1) * 64 + lane) * 8, 0, AUX_SC1);
        v2u fl; fl[0] = (unsigned)tag64; fl[1] = tag_hi;
        __builtin_amdgcn_raw_buffer_store_b64(fl, wsr, lane == 0 ? WS_ST + bid * 8 : OOB, 0, AUX_SC1);
      }
      STAMP(13);
      for (unsigned spins = 0;; ++spins) {
        v4u p[4];
#pragma unroll
        for (int i = 0; i < 4; ++i) p[i] = __builtin_amdgcn_raw_buffer_load_b128(wsr, WS_QG + grp * 4096 + lane * 32 + (i & 1) * 16 + (i >> 1) * 2048, 0, AUX_SC1);
        unsigned bad = 0;
#pragma unroll
        for (int i = 0; i < 4; ++i) bad |= (p[i][1] ^ tag_hi) | (p[i][3] ^ tag_hi) | ((p[i][0] ^ tag_lo8) & 0xffu) | ((p[i][2] ^ tag_lo8) & 0xffu);
        if (__all(bad == 0u) || spins > SPIN_CAP) {
          float qv[4];
#pragma unroll
          for (int i = 0; i < 4; ++i)
            qv[i] = __uint_as_float(p[i >> 1][(i & 1) * 2] & 0xffffff00u) + __uint_as_float(p[2 + (i >> 1)][(i & 1) * 2] & 0xffffff00u);
          v2u qq; qq[0] = pk_bf16(qv[0], qv[1]); qq[1] = pk_bf16(qv[2], qv[3]);
          *(LAS v2u*)(smem + L_QS + lane * 8) = qq;
          break;
        }
        __builtin_amdgcn_s_sleep(2);
      }
      lds_inc(smem, 0, lane);
      STAMP(3);
      if (lane == 0) { v2u t2; t2[0] = (unsigned)tag64; t2[1] = tag_hi; *(LAS v2u*)(smem + L_TAG) = t2; }
    }
    v4u G1[11], WD[18];
    const int cm = bid % 36, rb = bid / 36, mat1 = cm >= 18 ? 1 : 0, cblk = cm - 18 * mat1;
    const int hb = 2 * rb + (hw >> 1);
    const int rg0 = hb * 72 / 7, nrg = (hb + 1) * 72 / 7 - rg0;
    const bool has_s1 = bid < 252;
    const int u0 = 36 * (bid & 127);
    unsigned short gwv[9];
    {
      const rsrc_t gr = mk_rsrc(mat1 ? wu : wg, has_s1 ? D * F * 2 : 0);
      const int go = (4 * rg0 + g) * (F * 2) + cblk * 512 + (hw & 1) * 256 + c * 16;
#pragma unroll
      for (int i = 0; i < 11; ++i) G1[i] = __builtin_amdgcn_raw_buffer_load_b128(gr, go, (i < 10 || nrg == 11) ? i * 4 * (F * 2) : OOB, AUX_NT);
      const rsrc_t dr = mk_rsrc(wd, F * D * 2);
      const int wd_lo = (u0 + g) * 1152 + hw * 256 + c * 16;
      const int wd_hi = (hw == 0 && c < 8) ? wd_lo + 1024 : OOB;
#pragma unroll
      for (int i = 0; i < 9; ++i) WD[i] = __builtin_amdgcn_raw_buffer_load_b128(dr, wd_lo, i * 4608, AUX_NT);
#pragma unroll
      for (int i = 9; i < 18; ++i) WD[i] = __builtin_amdgcn_raw_buffer_load_b128(dr, wd_hi, (i - 9) * 4608, AUX_NT);
#pragma unroll
      for (int i = 0; i < 8; ++i) WO[i] = __builtin_amdgcn_raw_buffer_load_b128(wor, wo_lo, i * 4608, AUX_NT);
#pragma unroll
      for (int i = 8; i < 16; ++i) WO[i] = __builtin_amdgcn_raw_buffer_load_b128(wor, wo_hi, (i - 8) * 4608, AUX_NT);
#pragma unroll
      for (int i = 0; i < 9; ++i) gwv[i] = gw[i * 64 + lane];
    }
    __builtin_amdgcn_sched_barrier(0);
    {
      f32x4 ga = {0.f, 0.f, 0.f, 0.f};
#pragma unroll
      for (int i = 0; i < 11; ++i) {
        const int row = 4 * (rg0 + i) + g;
        const unsigned xv = *(const LAS unsigned*)(smem + L_XS + (row < D ? row : 0) * 4);
        const unsigned t_ = hi8 ? ((xv & 0xffff0000u) | (xv >> 16)) : ((xv << 16) | (xv & 0xffffu));
        v4u bb; bb[0] = t_ & msk[0]; bb[1] = t_ & msk[1]; bb[2] = t_ & msk[2]; bb[3] = t_ & msk[3];
        ga = mfma(G1[i], bb, ga);
      }
      if (hw < 2) {
        *(LAS f32x4*)(smem + L_GU + hw * 1024 + voff) = ga;
        lds_inc(smem, 10, lane);
      } else {
        lds_wait<2>(smem, 10, 2u);
        ga = *(const LAS f32x4*)(smem + L_GU + (hw & 1) * 1024 + voff) + ga;
        __builtin_amdgcn_raw_buffer_store_b128(__builtin_bit_cast(v4u, ga), wsr, has_s1 ? WS_G1 + (((cblk * 2 + (hw & 1)) * 2 + mat1) * 7 + rb) * 1024 + voff : OOB, 0, AUX_SC1);
        DRAIN();
        lds_inc(smem, 1, lane);
      }
    }
    float gt;
    {
      float z = 0.f;
#pragma unroll
      for (int i = 0; i < 9; ++i) {
        const unsigned xv = *(const LAS unsigned*)(smem + L_XS + (i * 64 + lane) * 4);
        z += rbf((b ? bfhi(xv) : bflo(xv)) * bflo(gwv[i]));
      }
      z += dpp<XOR1>(z); z += dpp<XOR2>(z); z += dpp<ROR4>(z); z += dpp<ROR8>(z);
      z = rbf(xrow_sum(z));
      gt = rbf(1.f / (1.f + __expf(-z)));
    }
    unsigned tag_lo = 0, tag_hi = 0;
    if (hw == 3) {
      const v2u tg = *(const LAS v2u*)(smem + L_TAG);
      tag_lo = __builtin_amdgcn_readfirstlane(tg[0]); tag_hi = __builtin_amdgcn_readfirstlane(tg[1]);
      lds_wait<4>(smem, 1, 2u);
      v2u fl; fl[0] = tag_lo; fl[1] = tag_hi;
      __builtin_amdgcn_raw_buffer_store_b64(fl, wsr, (lane == 0 && has_s1) ? WS_F1 + cblk * 128 + (mat1 * 7 + rb) * 8 : OOB, 0, AUX_SC1);
      __builtin_amdgcn_s_sleep(HDELAY);
      {
        const int fblk = lane < 32 ? (u0 >> 8) : ((u0 + 35) >> 8);
        const int fo = WS_F1 + fblk * 128 + ((lane & 31) < 14 ? (lane & 31) : 0) * 8;
        for (unsigned spins = 0;; ++spins) {
          const v2u p = __builtin_amdgcn_raw_buffer_load_b64(wsr, fo, 0, AUX_SC1);
          if (__all(((p[1] ^ tag_hi) | (p[0] ^ tag_lo)) == 0u) || spins > SPIN_CAP) break;
          __builtin_amdgcn_s_sleep(64);
        }
      }
      __builtin_amdgcn_fence(__ATOMIC_ACQUIRE, "wavefront");
      const int u = u0 + 4 * c + g;
      const int so = c < 9 ? WS_G1 + (u >> 7) * 14 * 1024 + (((u >> 5) & 3) * 16 + b * 8 + (u & 7)) * 16 + ((u >> 3) & 3) * 4 : OOB;
      float pv[14];
#pragma unroll
      for (int i = 0; i < 14; ++i) pv[i] = __uint_as_float(__builtin_amdgcn_raw_buffer_load_b32(wsr, so, i * 1024, AUX_SC1));
      const float gv = rbf(((pv[0] + pv[1]) + (pv[2] + pv[3])) + ((pv[4] + pv[5]) + pv[6]));
      const float uv = rbf(((pv[7] + pv[8]) + (pv[9] + pv[10])) + ((pv[11] + pv[12]) + pv[13]));
      const float inner = 0.7978845608028654f * (gv + 0.044715f * gv * gv * gv);
      const float hv = rbf(0.5f * gv * (1.f + tanhf(inner))) * uv;
      *(LAS unsigned*)(smem + L_FFN + lane * 4) = pk_bf16(hv, hv);
      lds_inc(smem, 11, lane);
    }
    f32x4 yf = {0.f, 0.f, 0.f, 0.f}, zf = {0.f, 0.f, 0.f, 0.f};
    {
      lds_wait<16>(smem, 11, 1u);
      const unsigned hp = *(const LAS unsigned*)(smem + L_FFN + lane * 4);
#define PRD(i) { const unsigned t_ = dppu<0x150 + (i)>(hp); \
                 v4u bb; bb[0] = t_ & msk[0]; bb[1] = t_ & msk[1]; bb[2] = t_ & msk[2]; bb[3] = t_ & msk[3]; \
                 yf = mfma(WD[i], bb, yf); zf = mfma(WD[9 + (i)], bb, zf); }
      PRD(0) PRD(1) PRD(2) PRD(3) PRD(4) PRD(5) PRD(6) PRD(7) PRD(8)
#undef PRD
      const float og = rbf(1.f - gt);
      yf *= og; zf *= og;
      asm volatile("" : "+v"(yf), "+v"(zf));
    }
    __builtin_amdgcn_sched_barrier(0);
    if (PTOUCH2 && hw == 2) {
      const unsigned char* tp = ((lane & 8) ? Vc : Kc) + (size_t)grp * (SK * 256) + (size_t)(lane & 7) * (1 << 20) + NSU * 8192;
      if (lane == 16) tp = (const unsigned char*)out;
      if (lane == 17) tp = ws + WS_SG + grp * 131072;
      if (lane == 18) tp = ws + WS_SG + grp * 131072 + 131068;
      if (lane < 19) tch = __builtin_nontemporal_load((const unsigned*)tp);
    }
    asm volatile("" :: "v"(tch), "v"(cpf));
    STAMP(5);
    if (hw == 3) {
      const unsigned long long tag64 = tag_lo;
      lds_wait<32>(smem, 7, 4u);
      if (lane == 0) {
        v2u fl; fl[0] = (unsigned)tag64; fl[1] = tag_hi;
        __builtin_amdgcn_raw_buffer_store_b64(fl, wsr, WS_SF + bid * 64, 0, AUX_SC1);
      }
      for (unsigned spins = 0;; ++spins) {
        const v2u p = __builtin_amdgcn_raw_buffer_load_b64(wsr, WS_SF + (grp * 8 + (lane & 7)) * 64, 0, AUX_SC1);
        if (__all(((p[1] ^ tag_hi) | (p[0] ^ (unsigned)tag64)) == 0u) || spins > SPIN_CAP) break;
        __builtin_amdgcn_s_sleep(32);
      }
      lds_inc(smem, 9, lane);
      if (is_final) {
        for (unsigned spins = 0;; ++spins) {
          unsigned bad = 0;
#pragma unroll
          for (int i = 0; i < 2; ++i) {
            const v4u p = __builtin_amdgcn_raw_buffer_load_b128(wsr, WS_ST + i * 1024 + voff, 0, AUX_SC1);
            bad |= (p[1] ^ tag_hi) | (p[0] ^ tag_lo) | (p[3] ^ tag_hi) | (p[2] ^ tag_lo);
          }
          if (__all(bad == 0u) || spins > SPIN_CAP) break;
          __builtin_amdgcn_s_sleep(64);
        }
        if (fid == 0 && lane == 0) {
          v2u e; e[0] = tag_lo; e[1] = tag_hi;
          __builtin_amdgcn_raw_buffer_store_b64(e, wsr, WS_E, 0, AUX_SC1);
        }
        lds_inc(smem, 6, lane);
      }
    }
    __builtin_amdgcn_sched_barrier(0);
    DRAIN();
    lds_wait<32>(smem, 2, 1u);
    if (!H1P && hw == 3) gather_o(false);
    lds_wait<0>(smem, 4, 1u);
    const unsigned pp = *(const LAS unsigned*)(smem + L_OS + lane * 4);
    STAMP(9);
    {
      f32x4 y0 = {0.f, 0.f, 0.f, 0.f}, y1 = {0.f, 0.f, 0.f, 0.f}, z0 = {0.f, 0.f, 0.f, 0.f};
#define PRJ(i, y) { const unsigned t_ = dppu<0x150 + (i)>(pp); v4u bb; bb[0] = t_ & msk[0]; bb[1] = t_ & msk[1]; bb[2] = t_ & msk[2]; bb[3] = t_ & msk[3]; \
                    y = mfma(WO[i], bb, y); z0 = mfma(WO[8 + (i)], bb, z0); }
      PRJ(0, y0) PRJ(1, y1) PRJ(2, y0) PRJ(3, y1) PRJ(4, y0) PRJ(5, y1) PRJ(6, y0) PRJ(7, y1)
#undef PRJ
      y0 = (y0 + y1) * gt + yf;
      z0 = z0 * gt + zf;
      const int dst = WS_H2 + ((b * 36 + hw * 8 + 2 * g) * 128 + (bid & 127)) * 64 + c * 4;
      __builtin_amdgcn_raw_buffer_store_b32(__float_as_uint(hi8 ? y0[1] : y0[0]) | 1u, wsr, dst, 0, AUX_SC1);
      __builtin_amdgcn_raw_buffer_store_b32(__float_as_uint(hi8 ? y0[3] : y0[2]) | 1u, wsr, dst, 8192, AUX_SC1);
      if (hw == 0) {
        const int dz = g < 2 ? dst + 32 * 8192 : OOB;
        __builtin_amdgcn_raw_buffer_store_b32(__float_as_uint(hi8 ? z0[1] : z0[0]) | 1u, wsr, dz, 0, AUX_SC1);
        __builtin_amdgcn_raw_buffer_store_b32(__float_as_uint(hi8 ? z0[3] : z0[2]) | 1u, wsr, dz, 8192, AUX_SC1);
      }
    }
    STAMP(15);
  }
  STAMP(11);
  STAMP_FLUSH();
}

extern "C" void kernel_launch(void* const* d_in, const int* in_sizes, int n_in, void* d_out, int out_size, void* d_ws, size_t ws_size,
                              hipStream_t stream) {
  k_fused<<<256, NTHR, 0, stream>>>((const unsigned short*)d_in[0], (const unsigned char*)d_in[1], (const unsigned char*)d_in[2],
                                    (const unsigned char*)d_in[3], (const unsigned char*)d_in[4], (const unsigned char*)d_in[6],
                                    (const unsigned char*)d_in[7], (const unsigned char*)d_in[8], (const unsigned short*)d_in[9],
                                    (unsigned char*)d_ws, (unsigned*)d_out);
}
```

```cpp
#include <hip/hip_runtime.h>
#include <hip/hip_bf16.h>
#include <stdint.h>

typedef unsigned v4u __attribute__((ext_vector_type(4)));
typedef unsigned v2u __attribute__((ext_vector_type(2)));
typedef float f32x4 __attribute__((ext_vector_type(4)));
typedef float f32x2 __attribute__((ext_vector_type(2)));
typedef double f64x4 __attribute__((ext_vector_type(4)));
typedef short bf16x8 __attribute__((ext_vector_type(8)));
typedef __bf16 bf16x2 __attribute__((ext_vector_type(2)));
typedef __amdgpu_buffer_rsrc_t rsrc_t;
#define LAS __attribute__((address_space(3)))

constexpr int D = 576, SK = 32768, F = 4608;
constexpr int NTHR = 512;
constexpr unsigned NSU = 124;
constexpr unsigned ND = 8 * (128 - NSU);
constexpr unsigned NONE = 1024;
constexpr int NST = NSU / 4;
static_assert(NSU % 4 == 0 && NST % 2 == 1, "own steps per wave: odd");
constexpr int AUX_NT = 2, AUX_SC1 = 16;
constexpr int OOB = 0x40000000;
constexpr unsigned SPIN_CAP = 200000u;
constexpr int PACE_T = 1756;
constexpr int PACE_CREDIT = 3500;
constexpr int CODE_BYTES = 25600;

constexpr int WS_E = 0;
constexpr int WS_CNT = 4096;
constexpr int WS_QG = 16384;
constexpr int WS_H1 = WS_QG + 131072;
constexpr int H1_STRIDE = 2176;
constexpr int WS_H2 = WS_H1 + 256 * H1_STRIDE;
constexpr int WS_ST = WS_H2 + 72 * 8192;
constexpr int WS_SF = WS_ST + 4096;
constexpr int WS_SG = WS_SF + 256 * 64;
constexpr int WS_F1 = WS_SG + 32 * (int)ND * 128;
constexpr int WS_G1 = WS_F1 + 4096;
constexpr int WS_END = WS_G1 + 36 * 14 * 1024;

constexpr int L_XS = 0;
constexpr int L_QP = 2304;
constexpr int L_QS = L_QP + 1024;
constexpr int L_MRG = L_QS + 512;
constexpr int L_ML = L_MRG + 8192;
constexpr int L_OS = L_ML + 96;
constexpr int L_GU = L_OS + 256;
constexpr int L_HS = L_GU + 2048;
constexpr int L_FB = L_HS + 256;
constexpr int L_SYNC = L_FB + 256;
constexpr int L_SC = L_SYNC + 48;
constexpr int L_SB = L_SC + 16384;
constexpr int L_END = L_SB + 4096;
constexpr int LDS_BYTES = 96 * 1024;
static_assert(L_END <= LDS_BYTES, "lds");
enum { S_Q = 0, S_G1 = 1, S_NEAR = 2, S_MRG = 3, S_O = 4, S_QP = 5, S_ALL = 6, S_KDONE = 7, S_FIN = 8, S_SHARED = 9, S_GU = 10, S_H = 11 };

__device__ __forceinline__ float bflo(unsigned u) { return __builtin_bit_cast(float, u << 16); }
__device__ __forceinline__ float bfhi(unsigned u) { return __builtin_bit_cast(float, u & 0xffff0000u); }
__device__ __forceinline__ unsigned pk_bf16(float lo, float hi) {
  bf16x2 v; v[0] = (__bf16)lo; v[1] = (__bf16)hi; return __builtin_bit_cast(unsigned, v);
}
__device__ __forceinline__ float rbf(float f) { return bflo(pk_bf16(f, f)); }
__device__ __forceinline__ float dot2(unsigned a, unsigned b, float c) {
  return __builtin_amdgcn_fdot2_f32_bf16(__builtin_bit_cast(bf16x2, a), __builtin_bit_cast(bf16x2, b), c, false);
}
constexpr int XOR1 = 0xB1, XOR2 = 0x4E, HMIR = 0x141, ROR4 = 0x124, ROR8 = 0x128, BCAST = 0x150;
template <int CTRL> __device__ __forceinline__ unsigned dppu(unsigned x) { return (unsigned)__builtin_amdgcn_mov_dpp((int)x, CTRL, 0xf, 0xf, true); }
template <int CTRL> __device__ __forceinline__ float dpp(float x) { return __uint_as_float(dppu<CTRL>(__float_as_uint(x))); }
template <int CTRL> __device__ __forceinline__ double dppd(double x) {
  const v2u u = __builtin_bit_cast(v2u, x);
  v2u r; r[0] = dppu<CTRL>(u[0]); r[1] = dppu<CTRL>(u[1]);
  return __builtin_bit_cast(double, r);
}
__device__ __forceinline__ f32x4 mfma(v4u a, v4u b, f32x4 c) {
  return __builtin_amdgcn_mfma_f32_16x16x32_bf16(__builtin_bit_cast(bf16x8, a), __builtin_bit_cast(bf16x8, b), c, 0, 0, 0);
}
__device__ __forceinline__ float swap16_sum(float a, float b) {
  auto s = __builtin_amdgcn_permlane16_swap(__float_as_uint(a), __float_as_uint(b), false, false);
  return __uint_as_float(s[0]) + __uint_as_float(s[1]);
}
__device__ __forceinline__ float swap32_sum(float a, float b) {
  auto s = __builtin_amdgcn_permlane32_swap(__float_as_uint(a), __float_as_uint(b), false, false);
  return __uint_as_float(s[0]) + __uint_as_float(s[1]);
}
__device__ __forceinline__ float xrow_max(float x) {
  auto s = __builtin_amdgcn_permlane16_swap(__float_as_uint(x), __float_as_uint(x), false, false);
  x = fmaxf(__uint_as_float(s[0]), __uint_as_float(s[1]));
  auto t = __builtin_amdgcn_permlane32_swap(__float_as_uint(x), __float_as_uint(x), false, false);
  return fmaxf(__uint_as_float(t[0]), __uint_as_float(t[1]));
}
__device__ __forceinline__ float xrow_sum(float x) { x = swap16_sum(x, x); return swap32_sum(x, x); }
__device__ __forceinline__ double xrow_sumd(double x) {
  v2u u = __builtin_bit_cast(v2u, x), a, b2;
  { auto s0 = __builtin_amdgcn_permlane16_swap(u[0], u[0], false, false); auto s1 = __builtin_amdgcn_permlane16_swap(u[1], u[1], false, false);
    a[0] = s0[0]; a[1] = s1[0]; b2[0] = s0[1]; b2[1] = s1[1]; }
  x = __builtin_bit_cast(double, a) + __builtin_bit_cast(double, b2);
  u = __builtin_bit_cast(v2u, x);
  { auto s0 = __builtin_amdgcn_permlane32_swap(u[0], u[0], false, false); auto s1 = __builtin_amdgcn_permlane32_swap(u[1], u[1], false, false);
    a[0] = s0[0]; a[1] = s1[0]; b2[0] = s0[1]; b2[1] = s1[1]; }
  return __builtin_bit_cast(double, a) + __builtin_bit_cast(double, b2);
}
__device__ __forceinline__ rsrc_t mk_rsrc(const void* p, int bytes) { return __builtin_amdgcn_make_buffer_rsrc((void*)p, 0, bytes, 0x00020000); }
#define DRAIN() do { __builtin_amdgcn_s_waitcnt(0x0F70); asm volatile("s_waitcnt vmcnt(0)" ::: "memory"); } while (0)
#define LDSW() asm volatile("s_waitcnt lgkmcnt(0)" ::: "memory")
#define FENCE() __builtin_amdgcn_sched_barrier(0)

constexpr float SCALE = 0.08838834764831845f, L2E = 1.4426950408889634f;

struct St { float m; double l; f64x4 acc; };
__device__ __forceinline__ void pin(St& st) { asm volatile("" : "+v"(st.m), "+v"(st.l), "+v"(st.acc) :: "memory"); }
__device__ __forceinline__ v4u diag(unsigned pp, const v4u& msk) {
  v4u bb; bb[0] = pp & msk[0]; bb[1] = pp & msk[1]; bb[2] = pp & msk[2]; bb[3] = pp & msk[3];
  return bb;
}

__device__ __forceinline__ v2u scores(const v4u* KB, const v4u& q0, const v4u& q1, bool hi8) {
  float s[4];
#pragma unroll
  for (int t = 0; t < 4; ++t) {
    float a0 = dot2(KB[t][0], q0[0], 0.f), a1 = dot2(KB[t][0], q1[0], 0.f);
#pragma unroll
    for (int k = 1; k < 4; ++k) { a0 = dot2(KB[t][k], q0[k], a0); a1 = dot2(KB[t][k], q1[k], a1); }
    const float keep = hi8 ? a1 : a0, send = hi8 ? a0 : a1;
    float v = keep + dpp<ROR8>(send);
    v += dpp<HMIR>(v); v += dpp<XOR2>(v); v += dpp<XOR1>(v);
    s[t] = v;
  }
  const unsigned u0 = pk_bf16(s[0], s[1]), u1 = pk_bf16(s[2], s[3]);
  v2u r; r[0] = pk_bf16(bflo(u0) * SCALE, bfhi(u0) * SCALE); r[1] = pk_bf16(bflo(u1) * SCALE, bfhi(u1) * SCALE);
  return r;
}
__device__ __forceinline__ void pv_unit(const v4u (&VB)[8], const v4u sc, const v4u& msk, St& st) {
  float s[8];
#pragma unroll
  for (int t = 0; t < 4; ++t) { s[2 * t] = bflo(sc[t]); s[2 * t + 1] = bfhi(sc[t]); }
  float tm = fmaxf(fmaxf(fmaxf(s[0], s[1]), fmaxf(s[2], s[3])), fmaxf(fmaxf(s[4], s[5]), fmaxf(s[6], s[7])));
  tm = xrow_max(tm);
  const float tl = tm * L2E, mt = __builtin_ceilf(tl), fr = tl - mt;
  const float mn = fmaxf(st.m, mt);
  const double alpha = (double)__builtin_amdgcn_exp2f(st.m - mn);
  const float beta = __builtin_amdgcn_exp2f(mt - mn);
  st.m = mn;
  float p[8];
#pragma unroll
  for (int t = 0; t < 8; ++t) p[t] = __builtin_amdgcn_exp2f(fmaf(s[t] - tm, L2E, fr));
  f32x4 ta = {0.f, 0.f, 0.f, 0.f};
#pragma unroll
  for (int t = 0; t < 8; ++t) ta = mfma(VB[t], diag(pk_bf16(p[t], p[t]), msk), ta);
  st.l = __builtin_fma(st.l, alpha, (double)((((p[0] + p[1]) + (p[2] + p[3])) + ((p[4] + p[5]) + (p[6] + p[7]))) * beta));
#pragma unroll
  for (int r = 0; r < 4; ++r) st.acc[r] = __builtin_fma(st.acc[r], alpha, (double)(ta[r] * beta));
}
__device__ __forceinline__ void issue(v4u (&B)[8], rsrc_t r, int voff, unsigned unit) {
  const int so = (int)(unit * 8192u);
#pragma unroll
  for (int t = 0; t < 4; ++t) B[t] = __builtin_amdgcn_raw_buffer_load_b128(r, voff + t * 1024, so, AUX_NT);
#pragma unroll
  for (int t = 0; t < 4; ++t) B[4 + t] = __builtin_amdgcn_raw_buffer_load_b128(r, voff + t * 1024, so + 4096, AUX_NT);
  FENCE();
}
__device__ __forceinline__ unsigned atomic_inc(rsrc_t r, int voff0, bool on, int so) {
  return (unsigned)__builtin_amdgcn_raw_ptr_buffer_atomic_add_i32(1, r, voff0, on ? so : OOB, 0);
}
__device__ __forceinline__ unsigned lds_inc(unsigned char* smem, int idx, int lane) {
  LDSW();
  unsigned old = 0;
  if (lane == 0) old = __hip_atomic_fetch_add((LAS unsigned*)(LAS unsigned char*)smem + (L_SYNC / 4 + idx), 1u, __ATOMIC_RELAXED, __HIP_MEMORY_SCOPE_WORKGROUP);
  return old;
}
template <int SLEEP> __device__ __forceinline__ void lds_wait(unsigned char* smem, int idx, unsigned want) {
#pragma unroll 1
  for (unsigned spins = 0; spins < 8000000u / (1 + SLEEP); ++spins) {
    const unsigned v = __builtin_amdgcn_readfirstlane(*((volatile LAS unsigned*)(LAS unsigned char*)smem + (L_SYNC / 4 + idx)));
    if (v >= want) break;
    if (SLEEP) __builtin_amdgcn_s_sleep(SLEEP);
  }
  asm volatile("" ::: "memory");
}
__device__ __forceinline__ float val(unsigned u) { return __uint_as_float(u & ~1u); }

__global__ void __launch_bounds__(NTHR, 2)
k_fused(const unsigned short* __restrict__ x, const unsigned char* Kc, const unsigned char* Vc,
        const unsigned char* __restrict__ wq, const unsigned char* wo, const unsigned char* wg,
        const unsigned char* wu, const unsigned char* wd, const unsigned short* __restrict__ gw,
        unsigned char* ws, unsigned* out) {
  __shared__ __attribute__((aligned(16))) unsigned char smem[LDS_BYTES];
  const int tid = threadIdx.x, lane = tid & 63, w = __builtin_amdgcn_readfirstlane(tid >> 6);
  const int bid = blockIdx.x, grp = bid >> 3, sp = bid & 7, b = grp >> 4, kvh = grp & 15;
  const int g = lane >> 4, c = lane & 15;
  const int voff = lane * 16;
  const int ridx = (grp + 4 * sp) & 31;
  const bool is_final = ridx < 9;
  const int fid = sp * 9 + ridx;
  const bool hi8 = (c & 8) != 0;
  const int cm = bid % 36, rb = bid / 36, mat1 = cm >= 18 ? 1 : 0, cblk = cm - 18 * mat1;
  const int u0 = 36 * (bid & 127);
#define MAKE_MSK() v4u msk; _Pragma("unroll") for (int d = 0; d < 4; ++d) msk[d] = (((c & 7) >> 1) == d) ? ((c & 1) ? 0xffff0000u : 0x0000ffffu) : 0u;

  if (w >= 4) {
    const int sw = w - 4, stid = tid - 256;
    const rsrc_t kr = mk_rsrc(Kc + (size_t)grp * (SK * 256), SK * 256);
    const rsrc_t vr = mk_rsrc(Vc + (size_t)grp * (SK * 256), SK * 256);
    const rsrc_t wsr = mk_rsrc(ws, WS_END);
    const unsigned ubase = (unsigned)(sp * 128 + sw);
    v4u Ba[8], Bb[8], Be0[8], Be1[8];
    issue(Be0, kr, voff, ubase);
    asm volatile("" ::: "memory");
    __builtin_amdgcn_s_barrier();
    asm volatile("" ::: "memory");
    FENCE();
    if (sw == 0) {
      const unsigned char* tp = Vc + (size_t)bid * (1 << 20);
      if (lane == 1) tp = (mat1 ? wu : wg) + (size_t)(rb < 7 ? (2 * rb) * 72 / 7 * 4 : 0) * (F * 2);
      if (lane == 2) tp = (mat1 ? wu : wg) + (size_t)(rb < 7 ? (2 * rb + 2) * 72 / 7 * 4 - 1 : 0) * (F * 2);
      if (lane == 3) tp = wd + (size_t)u0 * 1152;
      if (lane == 4) tp = wo + (size_t)(kvh * 256 + 32 * sp) * 1152;
      if (lane == 5) tp = (const unsigned char*)gw;
      unsigned tch = 0;
      if (lane < 6) tch = __builtin_nontemporal_load((const unsigned*)tp);
      asm volatile("" :: "v"(tch));
    }
    MAKE_MSK();
    DRAIN(); issue(Be1, kr, voff, ubase + 4);
    DRAIN(); issue(Ba, kr, voff, ubase + 8);
    DRAIN(); issue(Bb, kr, voff, ubase + 12);
    lds_wait<1>(smem, S_Q, 1u);
    const v4u q0 = *(const LAS v4u*)(smem + L_QS + c * 16), q1 = *(const LAS v4u*)(smem + L_QS + 256 + c * 16);
    const int sc_off = ((c >> 3) * 4 + g) * 16;
#define KSC(B) const v2u s0_ = scores(&B[0], q0, q1, hi8), s1_ = scores(&B[4], q0, q1, hi8);                       \
      v4u sc_; sc_[0] = s0_[0]; sc_[1] = s0_[1]; sc_[2] = s1_[0]; sc_[3] = s1_[1];
#define KSTEP(B, i)                                                                                                \
    {                                                                                                              \
      KSC(B)                                                                                                       \
      if ((c & 7) == 0) *(LAS v4u*)(smem + L_SC + (4 * (i) + sw) * 128 + sc_off) = sc_;                            \
      asm volatile("" ::: "memory");                                                                               \
      FENCE();                                                                                                     \
    }
    int nsl = 0;
    unsigned did = 0;
#define SFLUSH()                                                                                                   \
    {                                                                                                              \
      const v4u v_ = *(const LAS v4u*)(smem + L_SB + sw * 1024 + voff);                                            \
      __builtin_amdgcn_raw_buffer_store_b128(v_, wsr, (lane >> 3) < nsl ? WS_SG + (int)((grp * ND + did) * 128u) + (lane & 7) * 16 : OOB, 0, AUX_SC1); \
      nsl = 0;                                                                                                     \
    }
#define KSTEPG(B, dd)                                                                                              \
    {                                                                                                              \
      KSC(B)                                                                                                       \
      if ((c & 7) == 0) *(LAS v4u*)(smem + L_SB + sw * 1024 + nsl * 128 + sc_off) = sc_;                           \
      did = (lane >> 3) == nsl ? (dd) : did;                                                                       \
      ++nsl;                                                                                                       \
      if (nsl == 8) SFLUSH()                                                                                       \
      asm volatile("" ::: "memory");                                                                               \
      FENCE();                                                                                                     \
    }
#define PACE() do { int lag_; while ((lag_ = (int)((unsigned)__builtin_amdgcn_s_memrealtime() * 16u - due)) < 0) __builtin_amdgcn_s_sleep(1); \
                    if (lag_ > PACE_CREDIT) due += (unsigned)(lag_ - PACE_CREDIT); due += PACE_T; } while (0)
#define MAPD(d) (((d) & 7u) * 128u + NSU + ((d) >> 3))
    const int cnt0 = lane == 0 ? WS_CNT + grp * 256 : OOB;
    unsigned a = 0u;
    KSTEP(Ba, 2);
    issue(Ba, kr, voff, ubase + 16);
    KSTEP(Be0, 0);
    KSTEP(Be1, 1);
    KSTEP(Bb, 3);
    unsigned due = (unsigned)__builtin_amdgcn_s_memrealtime() * 16u;
#pragma unroll 1
    for (int i = 4; i < NST - 1; i += 2) {
      PACE();
      DRAIN();
      issue(Bb, kr, voff, ubase + 4 * (i + 1));
      KSTEP(Ba, i);
      PACE();
      DRAIN();
      a = atomic_inc(wsr, cnt0, i + 3 >= NST, 0);
      issue(Ba, kr, voff, ubase + 4 * (i + 2));
      KSTEP(Bb, i + 1);
    }
    {
      unsigned da, db;
      DRAIN();
      db = __builtin_amdgcn_readfirstlane(a);
      a = atomic_inc(wsr, cnt0, db < ND, 0);
      issue(Bb, db < ND ? kr : vr, voff, db < ND ? MAPD(db) : ubase);
      KSTEP(Ba, NST - 1);
      bool in_b = true;
      while (db < ND) {
        DRAIN();
        da = __builtin_amdgcn_readfirstlane(a);
        a = atomic_inc(wsr, cnt0, da < ND, 0);
        issue(Ba, da < ND ? kr : vr, voff, da < ND ? MAPD(da) : ubase);
        KSTEPG(Bb, db);
        if (da >= ND) { in_b = false; break; }
        DRAIN();
        db = __builtin_amdgcn_readfirstlane(a);
        a = atomic_inc(wsr, cnt0, db < ND, 0);
        issue(Bb, db < ND ? kr : vr, voff, db < ND ? MAPD(db) : ubase);
        KSTEPG(Ba, da);
      }
      SFLUSH()
      DRAIN();
      if (in_b) {
#pragma unroll
        for (int t = 0; t < 8; ++t) Ba[t] = Bb[t];
      }
    }
    lds_inc(smem, S_KDONE, lane);
    St st; st.m = -INFINITY; st.l = 0.0; st.acc = f64x4{0.0, 0.0, 0.0, 0.0};
#define LSC(i) (*(const LAS v4u*)(smem + L_SC + (4 * (i) + sw) * 128 + sc_off))
    due = (unsigned)__builtin_amdgcn_s_memrealtime() * 16u;
#pragma unroll 1
    for (int i = 0; i < NST - 1; i += 2) {
      PACE();
      DRAIN();
      issue(Bb, vr, voff, ubase + 4 * (i + 1));
      pv_unit(Ba, LSC(i), msk, st); pin(st);
      FENCE();
      PACE();
      DRAIN();
      a = atomic_inc(wsr, cnt0, i + 3 >= NST, 4);
      issue(Ba, vr, voff, ubase + 4 * (i + 2));
      pv_unit(Bb, LSC(i + 1), msk, st); pin(st);
      FENCE();
    }
    lds_wait<2>(smem, S_SHARED, 1u);
    lds_inc(smem, S_NEAR, lane);
    v4u sa = LSC(NST - 1), sb;
    const int sg_ld = WS_SG + grp * (int)ND * 128 + sc_off;
    while (true) {
      DRAIN();
      unsigned d = __builtin_amdgcn_readfirstlane(a);
      a = atomic_inc(wsr, cnt0, d < ND, 4);
      sb = __builtin_amdgcn_raw_buffer_load_b128(wsr, sg_ld, d < ND ? (int)(d * 128u) : OOB, AUX_SC1);
      issue(Bb, vr, voff, d < ND ? MAPD(d) : NONE);
      pv_unit(Ba, sa, msk, st); pin(st);
      FENCE();
      if (d >= ND) break;
      DRAIN();
      d = __builtin_amdgcn_readfirstlane(a);
      a = atomic_inc(wsr, cnt0, d < ND, 4);
      sa = __builtin_amdgcn_raw_buffer_load_b128(wsr, sg_ld, d < ND ? (int)(d * 128u) : OOB, AUX_SC1);
      issue(Ba, vr, voff, d < ND ? MAPD(d) : NONE);
      pv_unit(Bb, sb, msk, st); pin(st);
      FENCE();
      if (d >= ND) break;
    }
    {
      const double l = xrow_sumd(st.l);
      const int h = c >> 3;
#pragma unroll
      for (int r = 0; r < 4; ++r) {
        const int dim = 8 * (4 * g + r) + (c & 7);
        *(LAS double*)(smem + L_MRG + (sw * 256 + h * 128 + (dim & 96) + (dim & 3) * 8 + ((dim >> 2) & 7)) * 8) = st.acc[r];
      }
      if (g == 0 && (c & 7) == 0) { *(LAS float*)(smem + L_ML + (h * 4 + sw) * 4) = st.m; *(LAS double*)(smem + L_ML + 32 + (h * 4 + sw) * 8) = l; }
    }
    lds_inc(smem, S_MRG, lane);
    lds_wait<0>(smem, S_MRG, 4u);
    {
      const int hh = sw >> 1;
      const f32x4 m4 = *(const LAS f32x4*)(smem + L_ML + hh * 16);
      const f64x4 l4 = *(const LAS f64x4*)(smem + L_ML + 32 + hh * 32);
      double a4[4];
#pragma unroll
      for (int ww = 0; ww < 4; ++ww) a4[ww] = *(const LAS double*)(smem + L_MRG + (ww * 256 + stid) * 8);
      const float M = fmaxf(fmaxf(m4[0], m4[1]), fmaxf(m4[2], m4[3]));
      double L = 0.0, o = 0.0;
#pragma unroll
      for (int ww = 0; ww < 4; ++ww) {
        const double wt = (double)__builtin_amdgcn_exp2f(m4[ww] - M);
        L = __builtin_fma(wt, l4[ww], L); o = __builtin_fma(wt, a4[ww], o);
      }
      v2u ov = __builtin_bit_cast(v2u, o); ov[0] |= 1u;
      __builtin_amdgcn_raw_buffer_store_b64(ov, wsr, WS_H1 + bid * H1_STRIDE + stid * 8, 0, AUX_SC1);
      if ((stid & 127) < 2) {
        v2u ml = __builtin_bit_cast(v2u, L); ml[0] |= 1u;
        if (stid & 1) { ml[0] = 1u; ml[1] = __float_as_uint(M); }
        __builtin_amdgcn_raw_buffer_store_b64(ml, wsr, WS_H1 + bid * H1_STRIDE + 2048 + hh * 16 + (stid & 1) * 8, 0, AUX_SC1);
      }
    }
    for (int i = 0; i < sw; ++i) __builtin_amdgcn_s_sleep(4);
    {
      const int o_acc = WS_H1 + (grp * 8 + 4 * (c >> 3)) * H1_STRIDE + (32 * sp + 8 * g + (c & 7)) * 8;
      const int o_ml = WS_H1 + (grp * 8 + (c & 7)) * H1_STRIDE + 2048 + (sp >> 2) * 16;
      v2u pv[4];
      v4u pm = {0u, 0u, 0u, 0u};
#pragma unroll
      for (int i = 0; i < 4; ++i) pv[i] = v2u{0u, 0u};
      bool mine = true;
      for (unsigned spins = 0;; ++spins) {
        if (!(pv[0][0] & pv[1][0] & pv[2][0] & pv[3][0] & 1u)) {
#pragma unroll
          for (int i = 0; i < 4; ++i) pv[i] = __builtin_amdgcn_raw_buffer_load_b64(wsr, o_acc, i * H1_STRIDE, AUX_SC1);
        }
        if (!(pm[0] & pm[2] & 1u)) pm = __builtin_amdgcn_raw_buffer_load_b128(wsr, o_ml, 0, AUX_SC1);
        if (__all((pv[0][0] & pv[1][0] & pv[2][0] & pv[3][0] & pm[0] & pm[2] & 1u) != 0u) || spins > SPIN_CAP) break;
        if (__builtin_amdgcn_readfirstlane(*((volatile LAS unsigned*)(smem + L_SYNC) + S_O)) != 0u) { mine = false; break; }
        __builtin_amdgcn_s_sleep(2);
      }
      if (mine) {
        const float Ms = __uint_as_float(pm[3]);
        float Mx = fmaxf(Ms, dpp<XOR1>(Ms)); Mx = fmaxf(Mx, dpp<XOR2>(Mx)); Mx = fmaxf(Mx, dpp<HMIR>(Mx));
        const float wt = __builtin_amdgcn_exp2f(Ms - Mx);
        v2u lv; lv[0] = pm[0]; lv[1] = pm[1];
        double den = (double)wt * __builtin_bit_cast(double, lv);
        den += dppd<XOR1>(den); den += dppd<XOR2>(den); den += dppd<HMIR>(den);
        double num = 0.0;
#define ACC1(i) { const float wl_ = dpp<BCAST + (i)>(wt), wh_ = dpp<BCAST + 4 + (i)>(wt); num = __builtin_fma((double)(hi8 ? wh_ : wl_), __builtin_bit_cast(double, pv[i]), num); }
        ACC1(0) ACC1(1) ACC1(2) ACC1(3)
        num += dppd<ROR8>(num);
        const float ov = (float)num / (float)den;
        *(LAS unsigned*)(smem + L_OS + lane * 4) = pk_bf16(ov, ov);
        lds_inc(smem, S_O, lane);
      }
    }
    if (is_final) {
      const int src = WS_H2 + fid * 8192 + sw * 2048 + voff;
      lds_wait<8>(smem, S_ALL, 1u);
      v4u p0, p1;
      for (unsigned spins = 0;; ++spins) {
        p0 = __builtin_amdgcn_raw_buffer_load_b128(wsr, src, 0, AUX_SC1);
        p1 = __builtin_amdgcn_raw_buffer_load_b128(wsr, src, 1024, AUX_SC1);
        const unsigned m = (p0[0] & p0[1] & p0[2] & p0[3]) & (p1[0] & p1[1] & p1[2] & p1[3]) & 1u;
        if (__all(m != 0u) || spins > SPIN_CAP) break;
        __builtin_amdgcn_s_sleep(2);
      }
      f32x4 t4;
#pragma unroll
      for (int j = 0; j < 4; ++j) {
        float t = val(p0[j]) + val(p1[j]);
        t += dpp<ROR4>(t); t += dpp<ROR8>(t);
        t4[j] = xrow_sum(t);
      }
      if (lane < 4) *(LAS f32x4*)(smem + L_FB + (sw * 4 + lane) * 16) = t4;
      const unsigned arrived = __builtin_amdgcn_readfirstlane(lds_inc(smem, S_FIN, lane));
      asm volatile("" ::: "memory");
      if (arrived == 3u && lane < 8) {
        f32x2 t = *(const LAS f32x2*)(smem + L_FB + lane * 8);
#pragma unroll
        for (int ww = 1; ww < 4; ++ww) t += *(const LAS f32x2*)(smem + L_FB + ww * 64 + lane * 8);
        out[fid * 8 + lane] = pk_bf16(t[0], t[1]);
      }
    }
  } else {
    const int hw = w;
    const v4u cpf = __builtin_amdgcn_raw_buffer_load_b128(mk_rsrc((const void*)(__builtin_amdgcn_s_getpc() & ~255ull), CODE_BYTES),
                                                          hw == 0 ? grp * 1024 + voff : OOB, 0, 0);
    unsigned short xq[9];
    v4u wqv[9];
    {
      const int r = lane >> 3, row0 = 288 * (sp & 1) + 72 * hw;
      const rsrc_t qr = mk_rsrc(wq, D * 8192);
      const int base = (row0 + r) * 8192 + (kvh * 256 + (sp >> 1) * 64) * 2 + (lane & 7) * 16;
#pragma unroll
      for (int i = 0; i < 9; ++i) wqv[i] = __builtin_amdgcn_raw_buffer_load_b128(qr, base, i * 8 * 8192, AUX_NT);
#pragma unroll
      for (int i = 0; i < 9; ++i) xq[i] = x[b * D + row0 + 8 * i + r];
    }
    const int xi = tid < 144 ? tid : 143;
    const v2u xa = ((const v2u*)x)[xi], xb = ((const v2u*)x)[144 + xi];
    if (tid < 3) { const v4u z = {0u, 0u, 0u, 0u}; *(LAS v4u*)(smem + L_SYNC + tid * 16) = z; }
    FENCE();
    LDSW();
    __builtin_amdgcn_s_barrier();
    asm volatile("" ::: "memory");
    const rsrc_t wsr = mk_rsrc(ws, WS_END);
    v2u evv = {0u, 0u};
    if (hw == 3) {
      evv = __builtin_amdgcn_raw_buffer_load_b64(wsr, WS_E, 0, AUX_SC1);
      const v4u z = {0u, 0u, 0u, 0u};
      __builtin_amdgcn_raw_buffer_store_b128(z, wsr, WS_H1 + bid * H1_STRIDE + voff, 0, AUX_SC1);
      __builtin_amdgcn_raw_buffer_store_b128(z, wsr, WS_H1 + bid * H1_STRIDE + 1024 + voff, 0, AUX_SC1);
      const int h2o = WS_H2 + (b * 36 * 128 + (bid & 127)) * 64 + (lane >> 2) * 8192 + (lane & 3) * 16;
      __builtin_amdgcn_raw_buffer_store_b128(z, wsr, h2o, 0, AUX_SC1);
      __builtin_amdgcn_raw_buffer_store_b128(z, wsr, h2o, 16 * 8192, AUX_SC1);
      int off = h2o + 32 * 8192;
      if (lane >= 16) off = WS_H1 + bid * H1_STRIDE + 2048 + (lane & 1) * 16;
      if (lane >= 32) off = sp == 0 ? WS_CNT + grp * 256 : OOB;
      __builtin_amdgcn_raw_buffer_store_b128(z, wsr, off, 0, AUX_SC1);
    }
    FENCE();
    {
      float acc[8];
#pragma unroll
      for (int j = 0; j < 8; ++j) acc[j] = 0.f;
#pragma unroll
      for (int i = 0; i < 9; ++i) {
        const float xv = bflo(xq[i]);
#pragma unroll
        for (int k = 0; k < 4; ++k) { acc[2 * k] = fmaf(xv, bflo(wqv[i][k]), acc[2 * k]); acc[2 * k + 1] = fmaf(xv, bfhi(wqv[i][k]), acc[2 * k + 1]); }
      }
      float a4[4];
#pragma unroll
      for (int j = 0; j < 4; ++j) a4[j] = swap32_sum(acc[j], acc[j + 4]);
      f32x2 r2; r2[0] = swap16_sum(a4[0], a4[2]); r2[1] = swap16_sum(a4[1], a4[3]);
      r2[0] += dpp<ROR8>(r2[0]); r2[1] += dpp<ROR8>(r2[1]);
      if ((lane & 8) == 0) *(LAS f32x2*)(smem + L_QP + (hw * 64 + (lane & 7) * 8 + 4 * (lane >> 5) + 2 * ((lane >> 4) & 1)) * 4) = r2;
      if (tid < 144) {
        v4u xs4;
        xs4[0] = (xa[0] & 0xffffu) | (xb[0] << 16); xs4[1] = (xa[0] >> 16) | (xb[0] & 0xffff0000u);
        xs4[2] = (xa[1] & 0xffffu) | (xb[1] << 16); xs4[3] = (xa[1] >> 16) | (xb[1] & 0xffff0000u);
        *(LAS v4u*)(smem + L_XS + tid * 16) = xs4;
      }
      lds_inc(smem, S_QP, lane);
      lds_wait<0>(smem, S_QP, 4u);
    }
    MAKE_MSK();
    unsigned tag_lo = 0, tag_hi = 0;
    if (hw == 3) {
      const unsigned long long ev = ((unsigned long long)(unsigned)__builtin_amdgcn_readfirstlane(evv[1]) << 32) | (unsigned)__builtin_amdgcn_readfirstlane(evv[0]);
      const unsigned long long tag64 = ev * 6364136223846793005ull + 1442695040888963407ull;
      tag_lo = (unsigned)tag64; tag_hi = (unsigned)(tag64 >> 32);
      const unsigned tag8 = tag_lo >> 24;
      {
        float s = 0.f;
#pragma unroll
        for (int ww = 0; ww < 4; ++ww) s += *(const LAS float*)(smem + L_QP + (ww * 64 + lane) * 4);
        DRAIN();
        v2u gr; gr[0] = ((__float_as_uint(s) + 0x80u) & 0xffffff00u) | tag8; gr[1] = tag_hi;
        __builtin_amdgcn_raw_buffer_store_b64(gr, wsr, WS_QG + ((grp * 2 + (sp & 1)) * 256 + (sp >> 1) * 64 + lane) * 8, 0, AUX_SC1);
        v2u fl; fl[0] = tag_lo; fl[1] = tag_hi;
        __builtin_amdgcn_raw_buffer_store_b64(fl, wsr, lane == 0 ? WS_ST + bid * 8 : OOB, 0, AUX_SC1);
      }
      for (unsigned spins = 0;; ++spins) {
        v4u p[4];
#pragma unroll
        for (int i = 0; i < 4; ++i) p[i] = __builtin_amdgcn_raw_buffer_load_b128(wsr, WS_QG + grp * 4096 + lane * 32 + (i & 1) * 16 + (i >> 1) * 2048, 0, AUX_SC1);
        unsigned bad = 0;
#pragma unroll
        for (int i = 0; i < 4; ++i) bad |= (p[i][1] ^ tag_hi) | (p[i][3] ^ tag_hi) | ((p[i][0] ^ tag8) & 0xffu) | ((p[i][2] ^ tag8) & 0xffu);
        if (__all(bad == 0u) || spins > SPIN_CAP) {
          float qv[4];
#pragma unroll
          for (int i = 0; i < 4; ++i)
            qv[i] = __uint_as_float(p[i >> 1][(i & 1) * 2] & 0xffffff00u) + __uint_as_float(p[2 + (i >> 1)][(i & 1) * 2] & 0xffffff00u);
          v2u qq; qq[0] = pk_bf16(qv[0], qv[1]); qq[1] = pk_bf16(qv[2], qv[3]);
          *(LAS v2u*)(smem + L_QS + lane * 8) = qq;
          break;
        }
        __builtin_amdgcn_s_sleep(2);
      }
      lds_inc(smem, S_Q, lane);
    }
    v4u G1[11], WD[18], WO[16];
    const int hb = 2 * rb + (hw >> 1);
    const int rg0 = hb * 72 / 7, nrg = (hb + 1) * 72 / 7 - rg0;
    const bool has_s1 = bid < 252;
    unsigned short gwv[9];
    {
      const rsrc_t gr = mk_rsrc(mat1 ? wu : wg, has_s1 ? D * F * 2 : 0);
      const int go = (4 * rg0 + g) * (F * 2) + cblk * 512 + (hw & 1) * 256 + c * 16;
#pragma unroll
      for (int i = 0; i < 11; ++i) G1[i] = __builtin_amdgcn_raw_buffer_load_b128(gr, go, (i < 10 || nrg == 11) ? i * 4 * (F * 2) : OOB, AUX_NT);
      const rsrc_t dr = mk_rsrc(wd, F * D * 2);
      const int wd_lo = (u0 + g) * 1152 + hw * 256 + c * 16;
      const int wd_hi = (hw == 0 && c < 8) ? wd_lo + 1024 : OOB;
#pragma unroll
      for (int i = 0; i < 9; ++i) WD[i] = __builtin_amdgcn_raw_buffer_load_b128(dr, wd_lo, i * 4608, AUX_NT);
#pragma unroll
      for (int i = 9; i < 18; ++i) WD[i] = __builtin_amdgcn_raw_buffer_load_b128(dr, wd_hi, (i - 9) * 4608, AUX_NT);
      const rsrc_t wor = mk_rsrc(wo, 4096 * D * 2);
      const int wo_lo = (kvh * 256 + 32 * sp + g) * 1152 + hw * 256 + c * 16;
      const int wo_hi = (hw == 0 && c < 8) ? wo_lo + 1024 : OOB;
#pragma unroll
      for (int i = 0; i < 8; ++i) WO[i] = __builtin_amdgcn_raw_buffer_load_b128(wor, wo_lo, i * 4608, AUX_NT);
#pragma unroll
      for (int i = 8; i < 16; ++i) WO[i] = __builtin_amdgcn_raw_buffer_load_b128(wor, wo_hi, (i - 8) * 4608, AUX_NT);
#pragma unroll
      for (int i = 0; i < 9; ++i) gwv[i] = gw[i * 64 + lane];
    }
    FENCE();
    {
      f32x4 ga = {0.f, 0.f, 0.f, 0.f};
#pragma unroll
      for (int i = 0; i < 11; ++i) {
        const int row = 4 * (rg0 + i) + g;
        const unsigned xv = *(const LAS unsigned*)(smem + L_XS + (row < D ? row : 0) * 4);
        ga = mfma(G1[i], diag(hi8 ? ((xv & 0xffff0000u) | (xv >> 16)) : ((xv << 16) | (xv & 0xffffu)), msk), ga);
      }
      if (hw < 2) {
        *(LAS f32x4*)(smem + L_GU + hw * 1024 + voff) = ga;
        lds_inc(smem, S_GU, lane);
      } else {
        lds_wait<2>(smem, S_GU, 2u);
        ga = *(const LAS f32x4*)(smem + L_GU + (hw & 1) * 1024 + voff) + ga;
        __builtin_amdgcn_raw_buffer_store_b128(__builtin_bit_cast(v4u, ga), wsr, has_s1 ? WS_G1 + (((cblk * 2 + (hw & 1)) * 2 + mat1) * 7 + rb) * 1024 + voff : OOB, 0, AUX_SC1);
        DRAIN();
        lds_inc(smem, S_G1, lane);
      }
    }
    float gt;
    {
      float z = 0.f;
#pragma unroll
      for (int i = 0; i < 9; ++i) {
        const unsigned xv = *(const LAS unsigned*)(smem + L_XS + (i * 64 + lane) * 4);
        z += rbf((b ? bfhi(xv) : bflo(xv)) * bflo(gwv[i]));
      }
      z += dpp<XOR1>(z); z += dpp<XOR2>(z); z += dpp<ROR4>(z); z += dpp<ROR8>(z);
      z = rbf(xrow_sum(z));
      gt = rbf(1.f / (1.f + __expf(-z)));
    }
    if (hw == 3) {
      lds_wait<4>(smem, S_G1, 2u);
      v2u fl; fl[0] = tag_lo; fl[1] = tag_hi;
      __builtin_amdgcn_raw_buffer_store_b64(fl, wsr, (lane == 0 && has_s1) ? WS_F1 + cblk * 128 + (mat1 * 7 + rb) * 8 : OOB, 0, AUX_SC1);
      __builtin_amdgcn_s_sleep(100);
      {
        const int fblk = lane < 32 ? (u0 >> 8) : ((u0 + 35) >> 8);
        const int fo = WS_F1 + fblk * 128 + ((lane & 31) < 14 ? (lane & 31) : 0) * 8;
        for (unsigned spins = 0;; ++spins) {
          const v2u p = __builtin_amdgcn_raw_buffer_load_b64(wsr, fo, 0, AUX_SC1);
          if (__all(((p[1] ^ tag_hi) | (p[0] ^ tag_lo)) == 0u) || spins > SPIN_CAP) break;
          __builtin_amdgcn_s_sleep(64);
        }
      }
      __builtin_amdgcn_fence(__ATOMIC_ACQUIRE, "wavefront");
      const int u = u0 + 4 * c + g;
      const int so = c < 9 ? WS_G1 + (u >> 7) * 14 * 1024 + (((u >> 5) & 3) * 16 + b * 8 + (u & 7)) * 16 + ((u >> 3) & 3) * 4 : OOB;
      float pv[14];
#pragma unroll
      for (int i = 0; i < 14; ++i) pv[i] = __uint_as_float(__builtin_amdgcn_raw_buffer_load_b32(wsr, so, i * 1024, AUX_SC1));
      const float gv = rbf(((pv[0] + pv[1]) + (pv[2] + pv[3])) + ((pv[4] + pv[5]) + pv[6]));
      const float uv = rbf(((pv[7] + pv[8]) + (pv[9] + pv[10])) + ((pv[11] + pv[12]) + pv[13]));
      const float inner = 0.7978845608028654f * (gv + 0.044715f * gv * gv * gv);
      const float hv = rbf(0.5f * gv * (1.f + tanhf(inner))) * uv;
      *(LAS unsigned*)(smem + L_HS + lane * 4) = pk_bf16(hv, hv);
      lds_inc(smem, S_H, lane);
    }
    f32x4 yf = {0.f, 0.f, 0.f, 0.f}, zf = {0.f, 0.f, 0.f, 0.f};
    {
      lds_wait<16>(smem, S_H, 1u);
      const unsigned hp = *(const LAS unsigned*)(smem + L_HS + lane * 4);
#define PRD(i) { const v4u bb = diag(dppu<BCAST + (i)>(hp), msk); yf = mfma(WD[i], bb, yf); zf = mfma(WD[9 + (i)], bb, zf); }
      PRD(0) PRD(1) PRD(2) PRD(3) PRD(4) PRD(5) PRD(6) PRD(7) PRD(8)
      const float og = rbf(1.f - gt);
      yf *= og; zf *= og;
      asm volatile("" : "+v"(yf), "+v"(zf));
    }
    FENCE();
    if (hw == 2) {
      const unsigned char* tp = ((lane & 8) ? Vc : Kc) + (size_t)grp * (SK * 256) + (size_t)(lane & 7) * (1 << 20) + NSU * 8192;
      if (lane == 16) tp = (const unsigned char*)out;
      unsigned tch = 0;
      if (lane < 17) tch = __builtin_nontemporal_load((const unsigned*)tp);
      asm volatile("" :: "v"(tch));
    }
    asm volatile("" :: "v"(cpf));
    if (hw == 3) {
      lds_wait<32>(smem, S_KDONE, 4u);
      v2u fl; fl[0] = tag_lo; fl[1] = tag_hi;
      __builtin_amdgcn_raw_buffer_store_b64(fl, wsr, lane == 0 ? WS_SF + bid * 64 : OOB, 0, AUX_SC1);
      for (unsigned spins = 0;; ++spins) {
        const v2u p = __builtin_amdgcn_raw_buffer_load_b64(wsr, WS_SF + (grp * 8 + (lane & 7)) * 64, 0, AUX_SC1);
        if (__all(((p[1] ^ tag_hi) | (p[0] ^ tag_lo)) == 0u) || spins > SPIN_CAP) break;
        __builtin_amdgcn_s_sleep(32);
      }
      lds_inc(smem, S_SHARED, lane);
      if (is_final) {
        for (unsigned spins = 0;; ++spins) {
          unsigned bad = 0;
#pragma unroll
          for (int i = 0; i < 2; ++i) {
            const v4u p = __builtin_amdgcn_raw_buffer_load_b128(wsr, WS_ST + i * 1024 + voff, 0, AUX_SC1);
            bad |= (p[1] ^ tag_hi) | (p[0] ^ tag_lo) | (p[3] ^ tag_hi) | (p[2] ^ tag_lo);
          }
          if (__all(bad == 0u) || spins > SPIN_CAP) break;
          __builtin_amdgcn_s_sleep(64);
        }
        __builtin_amdgcn_raw_buffer_store_b64(fl, wsr, (fid == 0 && lane == 0) ? WS_E : OOB, 0, AUX_SC1);
        lds_inc(smem, S_ALL, lane);
      }
    }
    FENCE();
    DRAIN();
    lds_wait<32>(smem, S_NEAR, 1u);
    lds_wait<0>(smem, S_O, 1u);
    const unsigned pp = *(const LAS unsigned*)(smem + L_OS + lane * 4);
    {
      f32x4 y0 = {0.f, 0.f, 0.f, 0.f}, y1 = {0.f, 0.f, 0.f, 0.f}, z0 = {0.f, 0.f, 0.f, 0.f};
#define PRJ(i, y) { const v4u bb = diag(dppu<BCAST + (i)>(pp), msk); y = mfma(WO[i], bb, y); z0 = mfma(WO[8 + (i)], bb, z0); }
      PRJ(0, y0) PRJ(1, y1) PRJ(2, y0) PRJ(3, y1) PRJ(4, y0) PRJ(5, y1) PRJ(6, y0) PRJ(7, y1)
      y0 = (y0 + y1) * gt + yf;
      z0 = z0 * gt + zf;
      const int dst = WS_H2 + ((b * 36 + hw * 8 + 2 * g) * 128 + (bid & 127)) * 64 + c * 4;
      __builtin_amdgcn_raw_buffer_store_b32(__float_as_uint(hi8 ? y0[1] : y0[0]) | 1u, wsr, dst, 0, AUX_SC1);
      __builtin_amdgcn_raw_buffer_store_b32(__float_as_uint(hi8 ? y0[3] : y0[2]) | 1u, wsr, dst, 8192, AUX_SC1);
      if (hw == 0) {
        const int dz = g < 2 ? dst + 32 * 8192 : OOB;
        __builtin_amdgcn_raw_buffer_store_b32(__float_as_uint(hi8 ? z0[1] : z0[0]) | 1u, wsr, dz, 0, AUX_SC1);
        __builtin_amdgcn_raw_buffer_store_b32(__float_as_uint(hi8 ? z0[3] : z0[2]) | 1u, wsr, dz, 8192, AUX_SC1);
      }
    }
  }
}

extern "C" void kernel_launch(void* const* d_in, const int* in_sizes, int n_in, void* d_out, int out_size, void* d_ws, size_t ws_size,
                              hipStream_t stream) {
  k_fused<<<256, NTHR, 0, stream>>>((const unsigned short*)d_in[0], (const unsigned char*)d_in[1], (const unsigned char*)d_in[2],
                                    (const unsigned char*)d_in[3], (const unsigned char*)d_in[4], (const unsigned char*)d_in[6],
                                    (const unsigned char*)d_in[7], (const unsigned char*)d_in[8], (const unsigned short*)d_in[9],
                                    (unsigned char*)d_ws, (unsigned*)d_out);
}
```

```cpp
#include <hip/hip_runtime.h>
#include <hip/hip_bf16.h>
#include <stdint.h>

typedef unsigned v4u __attribute__((ext_vector_type(4)));
typedef unsigned v2u __attribute__((ext_vector_type(2)));
typedef float f32x4 __attribute__((ext_vector_type(4)));
typedef float f32x2 __attribute__((ext_vector_type(2)));
typedef double f64x4 __attribute__((ext_vector_type(4)));
typedef short bf16x8 __attribute__((ext_vector_type(8)));
typedef __bf16 bf16x2 __attribute__((ext_vector_type(2)));
typedef __amdgpu_buffer_rsrc_t rsrc_t;
#define LAS __attribute__((address_space(3)))

constexpr int D = 576, SK = 32768, F = 4608;
constexpr int NTHR = 512;
constexpr unsigned NSU = 124;
constexpr unsigned ND = 8 * (128 - NSU);
constexpr unsigned NONE = 1024;
constexpr int NST = NSU / 4;
static_assert(NSU % 4 == 0 && NST % 2 == 1, "own steps per wave: odd");
constexpr int AUX_NT = 2, AUX_SC1 = 16;
constexpr int OOB = 0x40000000;
constexpr unsigned SPIN_CAP = 200000u;
constexpr int PACE_T = 1756;
constexpr int PACE_CREDIT = 3500;
constexpr int CODE_BYTES = 25600;

constexpr int WS_E = 0;
constexpr int WS_CNT = 4096;
constexpr int WS_QG = 16384;
constexpr int WS_H1 = WS_QG + 131072;
constexpr int H1_STRIDE = 2176;
constexpr int WS_H2 = WS_H1 + 256 * H1_STRIDE;
constexpr int WS_ST = WS_H2 + 72 * 8192;
constexpr int WS_SF = WS_ST + 4096;
constexpr int WS_SG = WS_SF + 256 * 64;
constexpr int WS_F1 = WS_SG + 32 * (int)ND * 128;
constexpr int WS_G1 = WS_F1 + 4096;
constexpr int WS_END = WS_G1 + 36 * 14 * 1024;

constexpr int L_XS = 0;
constexpr int L_QP = 2304;
constexpr int L_QS = L_QP + 1024;
constexpr int L_MRG = L_QS + 512;
constexpr int L_ML = L_MRG + 8192;
constexpr int L_OS = L_ML + 96;
constexpr int L_GU = L_OS + 256;
constexpr int L_HS = L_GU + 2048;
constexpr int L_FB = L_HS + 256;
constexpr int L_SYNC = L_FB + 256;
constexpr int L_SC = L_SYNC + 48;
constexpr int L_SB = L_SC + 16384;
constexpr int L_END = L_SB + 4096;
constexpr int LDS_BYTES = 96 * 1024;
static_assert(L_END <= LDS_BYTES, "lds");
enum { S_Q = 0, S_G1 = 1, S_NEAR = 2, S_MRG = 3, S_O = 4, S_QP = 5, S_ALL = 6, S_KDONE = 7, S_FIN = 8, S_SHARED = 9, S_GU = 10, S_H = 11 };

__device__ __forceinline__ float bflo(unsigned u) { return __builtin_bit_cast(float, u << 16); }
__device__ __forceinline__ float bfhi(unsigned u) { return __builtin_bit_cast(float, u & 0xffff0000u); }
__device__ __forceinline__ unsigned pk_bf16(float lo, float hi) {
  bf16x2 v; v[0] = (__bf16)lo; v[1] = (__bf16)hi; return __builtin_bit_cast(unsigned, v);
}
__device__ __forceinline__ float rbf(float f) { return bflo(pk_bf16(f, f)); }
__device__ __forceinline__ float dot2(unsigned a, unsigned b, float c) {
  return __builtin_amdgcn_fdot2_f32_bf16(__builtin_bit_cast(bf16x2, a), __builtin_bit_cast(bf16x2, b), c, false);
}
constexpr int XOR1 = 0xB1, XOR2 = 0x4E, HMIR = 0x141, ROR4 = 0x124, ROR8 = 0x128, BCAST = 0x150;
template <int CTRL> __device__ __forceinline__ unsigned dppu(unsigned x) { return (unsigned)__builtin_amdgcn_mov_dpp((int)x, CTRL, 0xf, 0xf, true); }
template <int CTRL> __device__ __forceinline__ float dpp(float x) { return __uint_as_float(dppu<CTRL>(__float_as_uint(x))); }
template <int CTRL> __device__ __forceinline__ double dppd(double x) {
  const v2u u = __builtin_bit_cast(v2u, x);
  v2u r; r[0] = dppu<CTRL>(u[0]); r[1] = dppu<CTRL>(u[1]);
  return __builtin_bit_cast(double, r);
}
__device__ __forceinline__ f32x4 mfma(v4u a, v4u b, f32x4 c) {
  return __builtin_amdgcn_mfma_f32_16x16x32_bf16(__builtin_bit_cast(bf16x8, a), __builtin_bit_cast(bf16x8, b), c, 0, 0, 0);
}
__device__ __forceinline__ float swap16_sum(float a, float b) {
  auto s = __builtin_amdgcn_permlane16_swap(__float_as_uint(a), __float_as_uint(b), false, false);
  return __uint_as_float(s[0]) + __uint_as_float(s[1]);
}
__device__ __forceinline__ float swap32_sum(float a, float b) {
  auto s = __builtin_amdgcn_permlane32_swap(__float_as_uint(a), __float_as_uint(b), false, false);
  return __uint_as_float(s[0]) + __uint_as_float(s[1]);
}
__device__ __forceinline__ float xrow_max(float x) {
  auto s = __builtin_amdgcn_permlane16_swap(__float_as_uint(x), __float_as_uint(x), false, false);
  x = fmaxf(__uint_as_float(s[0]), __uint_as_float(s[1]));
  auto t = __builtin_amdgcn_permlane32_swap(__float_as_uint(x), __float_as_uint(x), false, false);
  return fmaxf(__uint_as_float(t[0]), __uint_as_float(t[1]));
}
__device__ __forceinline__ float xrow_sum(float x) { x = swap16_sum(x, x); return swap32_sum(x, x); }
__device__ __forceinline__ double xrow_sumd(double x) {
  v2u u = __builtin_bit_cast(v2u, x), a, b2;
  { auto s0 = __builtin_amdgcn_permlane16_swap(u[0], u[0], false, false); auto s1 = __builtin_amdgcn_permlane16_swap(u[1], u[1], false, false);
    a[0] = s0[0]; a[1] = s1[0]; b2[0] = s0[1]; b2[1] = s1[1]; }
  x = __builtin_bit_cast(double, a) + __builtin_bit_cast(double, b2);
  u = __builtin_bit_cast(v2u, x);
  { auto s0 = __builtin_amdgcn_permlane32_swap(u[0], u[0], false, false); auto s1 = __builtin_amdgcn_permlane32_swap(u[1], u[1], false, false);
    a[0] = s0[0]; a[1] = s1[0]; b2[0] = s0[1]; b2[1] = s1[1]; }
  return __builtin_bit_cast(double, a) + __builtin_bit_cast(double, b2);
}
__device__ __forceinline__ rsrc_t mk_rsrc(const void* p, int bytes) { return __builtin_amdgcn_make_buffer_rsrc((void*)p, 0, bytes, 0x00020000); }
#define DRAIN() do { __builtin_amdgcn_s_waitcnt(0x0F70); asm volatile("s_waitcnt vmcnt(0)" ::: "memory"); } while (0)
#define LDSW() asm volatile("s_waitcnt lgkmcnt(0)" ::: "memory")
#define FENCE() __builtin_amdgcn_sched_barrier(0)

constexpr float SCALE = 0.08838834764831845f, L2E = 1.4426950408889634f;

struct St { float m; double l; f64x4 acc; };
__device__ __forceinline__ void pin(St& st) { asm volatile("" : "+v"(st.m), "+v"(st.l), "+v"(st.acc) :: "memory"); }
__device__ __forceinline__ v4u diag(unsigned pp, const v4u& msk) {
  v4u bb; bb[0] = pp & msk[0]; bb[1] = pp & msk[1]; bb[2] = pp & msk[2]; bb[3] = pp & msk[3];
  return bb;
}

__device__ __forceinline__ v2u scores(const v4u* KB, const v4u& q0, const v4u& q1, bool hi8) {
  float s[4];
#pragma unroll
  for (int t = 0; t < 4; ++t) {
    float a0 = dot2(KB[t][0], q0[0], 0.f), a1 = dot2(KB[t][0], q1[0], 0.f);
#pragma unroll
    for (int k = 1; k < 4; ++k) { a0 = dot2(KB[t][k], q0[k], a0); a1 = dot2(KB[t][k], q1[k], a1); }
    const float keep = hi8 ? a1 : a0, send = hi8 ? a0 : a1;
    float v = keep + dpp<ROR8>(send);
    v += dpp<HMIR>(v); v += dpp<XOR2>(v); v += dpp<XOR1>(v);
    s[t] = v;
  }
  const unsigned u0 = pk_bf16(s[0], s[1]), u1 = pk_bf16(s[2], s[3]);
  v2u r; r[0] = pk_bf16(bflo(u0) * SCALE, bfhi(u0) * SCALE); r[1] = pk_bf16(bflo(u1) * SCALE, bfhi(u1) * SCALE);
  return r;
}
__device__ __forceinline__ void pv_unit(const v4u (&VB)[8], const v4u sc, const v4u& msk, St& st) {
  float s[8];
#pragma unroll
  for (int t = 0; t < 4; ++t) { s[2 * t] = bflo(sc[t]); s[2 * t + 1] = bfhi(sc[t]); }
  float tm = fmaxf(fmaxf(fmaxf(s[0], s[1]), fmaxf(s[2], s[3])), fmaxf(fmaxf(s[4], s[5]), fmaxf(s[6], s[7])));
  tm = xrow_max(tm);
  const float tl = tm * L2E, mt = __builtin_ceilf(tl), fr = tl - mt;
  const float mn = fmaxf(st.m, mt);
  const double alpha = (double)__builtin_amdgcn_exp2f(st.m - mn);
  const float beta = __builtin_amdgcn_exp2f(mt - mn);
  st.m = mn;
  float p[8];
#pragma unroll
  for (int t = 0; t < 8; ++t) p[t] = __builtin_amdgcn_exp2f(fmaf(s[t] - tm, L2E, fr));
  f32x4 ta = {0.f, 0.f, 0.f, 0.f};
#pragma unroll
  for (int t = 0; t < 8; ++t) ta = mfma(VB[t], diag(pk_bf16(p[t], p[t]), msk), ta);
  st.l = __builtin_fma(st.l, alpha, (double)((((p[0] + p[1]) + (p[2] + p[3])) + ((p[4] + p[5]) + (p[6] + p[7]))) * beta));
#pragma unroll
  for (int r = 0; r < 4; ++r) st.acc[r] = __builtin_fma(st.acc[r], alpha, (double)(ta[r] * beta));
}
__device__ __forceinline__ void issue(v4u (&B)[8], rsrc_t r, int voff, unsigned unit) {
  const int so = (int)(unit * 8192u);
#pragma unroll
  for (int t = 0; t < 4; ++t) B[t] = __builtin_amdgcn_raw_buffer_load_b128(r, voff + t * 1024, so, AUX_NT);
#pragma unroll
  for (int t = 0; t < 4; ++t) B[4 + t] = __builtin_amdgcn_raw_buffer_load_b128(r, voff + t * 1024, so + 4096, AUX_NT);
  FENCE();
}
__device__ __forceinline__ unsigned atomic_inc(rsrc_t r, int voff0, bool on, int so) {
  return (unsigned)__builtin_amdgcn_raw_ptr_buffer_atomic_add_i32(1, r, voff0, on ? so : OOB, 0);
}
__device__ __forceinline__ unsigned lds_inc(unsigned char* smem, int idx, int lane) {
  LDSW();
  unsigned old = 0;
  if (lane == 0) old = __hip_atomic_fetch_add((LAS unsigned*)(LAS unsigned char*)smem + (L_SYNC / 4 + idx), 1u, __ATOMIC_RELAXED, __HIP_MEMORY_SCOPE_WORKGROUP);
  return old;
}
template <int SLEEP> __device__ __forceinline__ void lds_wait(unsigned char* smem, int idx, unsigned want) {
#pragma unroll 1
  for (unsigned spins = 0; spins < 8000000u / (1 + SLEEP); ++spins) {
    const unsigned v = __builtin_amdgcn_readfirstlane(*((volatile LAS unsigned*)(LAS unsigned char*)smem + (L_SYNC / 4 + idx)));
    if (v >= want) break;
    if (SLEEP) __builtin_amdgcn_s_sleep(SLEEP);
  }
  asm volatile("" ::: "memory");
}
__device__ __forceinline__ float val(unsigned u) { return __uint_as_float(u & ~1u); }

__global__ void __launch_bounds__(NTHR, 2)
k_fused(const unsigned short* __restrict__ x, const unsigned char* Kc, const unsigned char* Vc,
        const unsigned char* __restrict__ wq, const unsigned char* wo, const unsigned char* wg,
        const unsigned char* wu, const unsigned char* wd, const unsigned short* __restrict__ gw,
        unsigned char* ws, unsigned* out) {
  __shared__ __attribute__((aligned(16))) unsigned char smem[LDS_BYTES];
  const int tid = threadIdx.x, lane = tid & 63, w = __builtin_amdgcn_readfirstlane(tid >> 6);
  const int bid = blockIdx.x, grp = bid >> 3, sp = bid & 7, b = grp >> 4, kvh = grp & 15;
  const int g = lane >> 4, c = lane & 15;
  const int voff = lane * 16;
  const int ridx = (grp + 4 * sp) & 31;
  const bool is_final = ridx < 9;
  const int fid = sp * 9 + ridx;
  const bool hi8 = (c & 8) != 0;
  const int cm = bid % 36, rb = bid / 36, mat1 = cm >= 18 ? 1 : 0, cblk = cm - 18 * mat1;
  const int u0 = 36 * (bid & 127);
#define MAKE_MSK() v4u msk; _Pragma("unroll") for (int d = 0; d < 4; ++d) msk[d] = (((c & 7) >> 1) == d) ? ((c & 1) ? 0xffff0000u : 0x0000ffffu) : 0u;

  if (w >= 4) {
    const int sw = w - 4, stid = tid - 256;
    const rsrc_t kr = mk_rsrc(Kc + (size_t)grp * (SK * 256), SK * 256);
    const rsrc_t vr = mk_rsrc(Vc + (size_t)grp * (SK * 256), SK * 256);
    const rsrc_t wsr = mk_rsrc(ws, WS_END);
    const unsigned ubase = (unsigned)(sp * 128 + sw);
    v4u Ba[8], Bb[8], Be0[8], Be1[8];
    issue(Be0, kr, voff, ubase);
    asm volatile("" ::: "memory");
    __builtin_amdgcn_s_barrier();
    asm volatile("" ::: "memory");
    FENCE();
    if (sw == 0) {
      const unsigned char* tp = Vc + (size_t)bid * (1 << 20);
      if (lane == 1) tp = (mat1 ? wu : wg) + (size_t)(rb < 7 ? (2 * rb) * 72 / 7 * 4 : 0) * (F * 2);
      if (lane == 2) tp = (mat1 ? wu : wg) + (size_t)(rb < 7 ? (2 * rb + 2) * 72 / 7 * 4 - 1 : 0) * (F * 2);
      if (lane == 3) tp = wd + (size_t)u0 * 1152;
      if (lane == 4) tp = wo + (size_t)(kvh * 256 + 32 * sp) * 1152;
      if (lane == 5) tp = (const unsigned char*)gw;
      unsigned tch = 0;
      if (lane < 6) tch = __builtin_nontemporal_load((const unsigned*)tp);
      asm volatile("" :: "v"(tch));
    }
    MAKE_MSK();
    DRAIN(); issue(Be1, kr, voff, ubase + 4);
    DRAIN(); issue(Ba, kr, voff, ubase + 8);
    DRAIN(); issue(Bb, kr, voff, ubase + 12);
    lds_wait<1>(smem, S_Q, 1u);
    const v4u q0 = *(const LAS v4u*)(smem + L_QS + c * 16), q1 = *(const LAS v4u*)(smem + L_QS + 256 + c * 16);
    const int sc_off = ((c >> 3) * 4 + g) * 16;
#define KSC(B) const v2u s0_ = scores(&B[0], q0, q1, hi8), s1_ = scores(&B[4], q0, q1, hi8);                       \
      v4u sc_; sc_[0] = s0_[0]; sc_[1] = s0_[1]; sc_[2] = s1_[0]; sc_[3] = s1_[1];
#define KSTEP(B, i)                                                                                                \
    {                                                                                                              \
      KSC(B)                                                                                                       \
      if ((c & 7) == 0) *(LAS v4u*)(smem + L_SC + (4 * (i) + sw) * 128 + sc_off) = sc_;                            \
      asm volatile("" ::: "memory");                                                                               \
      FENCE();                                                                                                     \
    }
    int nsl = 0;
    unsigned did = 0;
#define SFLUSH()                                                                                                   \
    {                                                                                                              \
      const v4u v_ = *(const LAS v4u*)(smem + L_SB + sw * 1024 + voff);                                            \
      __builtin_amdgcn_raw_buffer_store_b128(v_, wsr, (lane >> 3) < nsl ? WS_SG + (int)((grp * ND + did) * 128u) + (lane & 7) * 16 : OOB, 0, AUX_SC1); \
      nsl = 0;                                                                                                     \
    }
#define KSTEPG(B, dd)                                                                                              \
    {                                                                                                              \
      KSC(B)                                                                                                       \
      if ((c & 7) == 0) *(LAS v4u*)(smem + L_SB + sw * 1024 + nsl * 128 + sc_off) = sc_;                           \
      did = (lane >> 3) == nsl ? (dd) : did;                                                                       \
      ++nsl;                                                                                                       \
      if (nsl == 8) SFLUSH()                                                                                       \
      asm volatile("" ::: "memory");                                                                               \
      FENCE();                                                                                                     \
    }
#define PACE() do { int lag_; while ((lag_ = (int)((unsigned)__builtin_amdgcn_s_memrealtime() * 16u - due)) < 0) __builtin_amdgcn_s_sleep(1); \
                    if (lag_ > PACE_CREDIT) due += (unsigned)(lag_ - PACE_CREDIT); due += PACE_T; } while (0)
#define MAPD(d) (((d) & 7u) * 128u + NSU + ((d) >> 3))
    const int cnt0 = lane == 0 ? WS_CNT + grp * 256 : OOB;
    unsigned a = 0u;
    KSTEP(Ba, 2);
    issue(Ba, kr, voff, ubase + 16);
    KSTEP(Be0, 0);
    KSTEP(Be1, 1);
    KSTEP(Bb, 3);
    unsigned due = (unsigned)__builtin_amdgcn_s_memrealtime() * 16u;
#pragma unroll 1
    for (int i = 4; i < NST - 1; i += 2) {
      PACE();
      DRAIN();
      issue(Bb, kr, voff, ubase + 4 * (i + 1));
      KSTEP(Ba, i);
      PACE();
      DRAIN();
      a = atomic_inc(wsr, cnt0, i + 3 >= NST, 0);
      issue(Ba, kr, voff, ubase + 4 * (i + 2));
      KSTEP(Bb, i + 1);
    }
    {
      unsigned da, db;
      DRAIN();
      db = __builtin_amdgcn_readfirstlane(a);
      a = atomic_inc(wsr, cnt0, db < ND, 0);
      issue(Bb, db < ND ? kr : vr, voff, db < ND ? MAPD(db) : ubase);
      KSTEP(Ba, NST - 1);
      bool in_b = true;
      while (db < ND) {
        DRAIN();
        da = __builtin_amdgcn_readfirstlane(a);
        a = atomic_inc(wsr, cnt0, da < ND, 0);
        issue(Ba, da < ND ? kr : vr, voff, da < ND ? MAPD(da) : ubase);
        KSTEPG(Bb, db);
        if (da >= ND) { in_b = false; break; }
        DRAIN();
        db = __builtin_amdgcn_readfirstlane(a);
        a = atomic_inc(wsr, cnt0, db < ND, 0);
        issue(Bb, db < ND ? kr : vr, voff, db < ND ? MAPD(db) : ubase);
        KSTEPG(Ba, da);
      }
      SFLUSH()
      DRAIN();
      if (in_b) {
#pragma unroll
        for (int t = 0; t < 8; ++t) Ba[t] = Bb[t];
      }
    }
    lds_inc(smem, S_KDONE, lane);
    St st; st.m = -INFINITY; st.l = 0.0; st.acc = f64x4{0.0, 0.0, 0.0, 0.0};
#define LSC(i) (*(const LAS v4u*)(smem + L_SC + (4 * (i) + sw) * 128 + sc_off))
    due = (unsigned)__builtin_amdgcn_s_memrealtime() * 16u;
#pragma unroll 1
    for (int i = 0; i < NST - 1; i += 2) {
      PACE();
      DRAIN();
      issue(Bb, vr, voff, ubase + 4 * (i + 1));
      pv_unit(Ba, LSC(i), msk, st); pin(st);
      FENCE();
      PACE();
      DRAIN();
      a = atomic_inc(wsr, cnt0, i + 3 >= NST, 4);
      issue(Ba, vr, voff, ubase + 4 * (i + 2));
      pv_unit(Bb, LSC(i + 1), msk, st); pin(st);
      FENCE();
    }
    lds_wait<2>(smem, S_SHARED, 1u);
    lds_inc(smem, S_NEAR, lane);
    v4u sa = LSC(NST - 1), sb;
    const int sg_ld = WS_SG + grp * (int)ND * 128 + sc_off;
    while (true) {
      DRAIN();
      unsigned d = __builtin_amdgcn_readfirstlane(a);
      a = atomic_inc(wsr, cnt0, d < ND, 4);
      sb = __builtin_amdgcn_raw_buffer_load_b128(wsr, sg_ld, d < ND ? (int)(d * 128u) : OOB, AUX_SC1);
      issue(Bb, vr, voff, d < ND ? MAPD(d) : NONE);
      pv_unit(Ba, sa, msk, st); pin(st);
      FENCE();
      if (d >= ND) break;
      DRAIN();
      d = __builtin_amdgcn_readfirstlane(a);
      a = atomic_inc(wsr, cnt0, d < ND, 4);
      sa = __builtin_amdgcn_raw_buffer_load_b128(wsr, sg_ld, d < ND ? (int)(d * 128u) : OOB, AUX_SC1);
      issue(Ba, vr, voff, d < ND ? MAPD(d) : NONE);
      pv_unit(Bb, sb, msk, st); pin(st);
      FENCE();
      if (d >= ND) break;
    }
    {
      const double l = xrow_sumd(st.l);
      const int h = c >> 3;
#pragma unroll
      for (int r = 0; r < 4; ++r) {
        const int dim = 8 * (4 * g + r) + (c & 7);
        *(LAS double*)(smem + L_MRG + (sw * 256 + h * 128 + (dim & 96) + (dim & 3) * 8 + ((dim >> 2) & 7)) * 8) = st.acc[r];
      }
      if (g == 0 && (c & 7) == 0) { *(LAS float*)(smem + L_ML + (h * 4 + sw) * 4) = st.m; *(LAS double*)(smem + L_ML + 32 + (h * 4 + sw) * 8) = l; }
    }
    lds_inc(smem, S_MRG, lane);
    lds_wait<0>(smem, S_MRG, 4u);
    {
      const int hh = sw >> 1;
      const f32x4 m4 = *(const LAS f32x4*)(smem + L_ML + hh * 16);
      const f64x4 l4 = *(const LAS f64x4*)(smem + L_ML + 32 + hh * 32);
      double a4[4];
#pragma unroll
      for (int ww = 0; ww < 4; ++ww) a4[ww] = *(const LAS double*)(smem + L_MRG + (ww * 256 + stid) * 8);
      const float M = fmaxf(fmaxf(m4[0], m4[1]), fmaxf(m4[2], m4[3]));
      double L = 0.0, o = 0.0;
#pragma unroll
      for (int ww = 0; ww < 4; ++ww) {
        const double wt = (double)__builtin_amdgcn_exp2f(m4[ww] - M);
        L = __builtin_fma(wt, l4[ww], L); o = __builtin_fma(wt, a4[ww], o);
      }
      v2u ov = __builtin_bit_cast(v2u, o); ov[0] |= 1u;
      __builtin_amdgcn_raw_buffer_store_b64(ov, wsr, WS_H1 + bid * H1_STRIDE + stid * 8, 0, AUX_SC1);
      if ((stid & 127) < 2) {
        v2u ml = __builtin_bit_cast(v2u, L); ml[0] |= 1u;
        if (stid & 1) { ml[0] = 1u; ml[1] = __float_as_uint(M); }
        __builtin_amdgcn_raw_buffer_store_b64(ml, wsr, WS_H1 + bid * H1_STRIDE + 2048 + hh * 16 + (stid & 1) * 8, 0, AUX_SC1);
      }
    }
    for (int i = 0; i < sw; ++i) __builtin_amdgcn_s_sleep(4);
    {
      const int o_acc = WS_H1 + (grp * 8 + 4 * (c >> 3)) * H1_STRIDE + (32 * sp + 8 * g + (c & 7)) * 8;
      const int o_ml = WS_H1 + (grp * 8 + (c & 7)) * H1_STRIDE + 2048 + (sp >> 2) * 16;
      v2u pv[4];
      v4u pm = {0u, 0u, 0u, 0u};
#pragma unroll
      for (int i = 0; i < 4; ++i) pv[i] = v2u{0u, 0u};
      bool mine = true;
      for (unsigned spins = 0;; ++spins) {
        if (!(pv[0][0] & pv[1][0] & pv[2][0] & pv[3][0] & 1u)) {
#pragma unroll
          for (int i = 0; i < 4; ++i) pv[i] = __builtin_amdgcn_raw_buffer_load_b64(wsr, o_acc, i * H1_STRIDE, AUX_SC1);
        }
        if (!(pm[0] & pm[2] & 1u)) pm = __builtin_amdgcn_raw_buffer_load_b128(wsr, o_ml, 0, AUX_SC1);
        if (__all((pv[0][0] & pv[1][0] & pv[2][0] & pv[3][0] & pm[0] & pm[2] & 1u) != 0u) || spins > SPIN_CAP) break;
        if (__builtin_amdgcn_readfirstlane(*((volatile LAS unsigned*)(smem + L_SYNC) + S_O)) != 0u) { mine = false; break; }
        __builtin_amdgcn_s_sleep(2);
      }
      if (mine) {
        const float Ms = __uint_as_float(pm[3]);
        float Mx = fmaxf(Ms, dpp<XOR1>(Ms)); Mx = fmaxf(Mx, dpp<XOR2>(Mx)); Mx = fmaxf(Mx, dpp<HMIR>(Mx));
        const float wt = __builtin_amdgcn_exp2f(Ms - Mx);
        v2u lv; lv[0] = pm[0]; lv[1] = pm[1];
        double den = (double)wt * __builtin_bit_cast(double, lv);
        den += dppd<XOR1>(den); den += dppd<XOR2>(den); den += dppd<HMIR>(den);
        double num = 0.0;
#define ACC1(i) { const float wl_ = dpp<BCAST + (i)>(wt), wh_ = dpp<BCAST + 4 + (i)>(wt); num = __builtin_fma((double)(hi8 ? wh_ : wl_), __builtin_bit_cast(double, pv[i]), num); }
        ACC1(0) ACC1(1) ACC1(2) ACC1(3)
        num += dppd<ROR8>(num);
        const float ov = (float)num / (float)den;
        *(LAS unsigned*)(smem + L_OS + lane * 4) = pk_bf16(ov, ov);
        lds_inc(smem, S_O, lane);
      }
    }
    if (is_final) {
      const int src = WS_H2 + fid * 8192 + sw * 2048 + voff;
      lds_wait<8>(smem, S_ALL, 1u);
      v4u p0, p1;
      for (unsigned spins = 0;; ++spins) {
        p0 = __builtin_amdgcn_raw_buffer_load_b128(wsr, src, 0, AUX_SC1);
        p1 = __builtin_amdgcn_raw_buffer_load_b128(wsr, src, 1024, AUX_SC1);
        const unsigned m = (p0[0] & p0[1] & p0[2] & p0[3]) & (p1[0] & p1[1] & p1[2] & p1[3]) & 1u;
        if (__all(m != 0u) || spins > SPIN_CAP) break;
        __builtin_amdgcn_s_sleep(2);
      }
      f32x4 t4;
#pragma unroll
      for (int j = 0; j < 4; ++j) {
        float t = val(p0[j]) + val(p1[j]);
        t += dpp<ROR4>(t); t += dpp<ROR8>(t);
        t4[j] = xrow_sum(t);
      }
      if (lane < 4) *(LAS f32x4*)(smem + L_FB + (sw * 4 + lane) * 16) = t4;
      const unsigned arrived = __builtin_amdgcn_readfirstlane(lds_inc(smem, S_FIN, lane));
      asm volatile("" ::: "memory");
      if (arrived == 3u && lane < 8) {
        f32x2 t = *(const LAS f32x2*)(smem + L_FB + lane * 8);
#pragma unroll
        for (int ww = 1; ww < 4; ++ww) t += *(const LAS f32x2*)(smem + L_FB + ww * 64 + lane * 8);
        out[fid * 8 + lane] = pk_bf16(t[0], t[1]);
      }
    }
  } else {
    const int hw = w;
    const v4u cpf = __builtin_amdgcn_raw_buffer_load_b128(mk_rsrc((const void*)(__builtin_amdgcn_s_getpc() & ~255ull), CODE_BYTES),
                                                          hw == 0 ? grp * 1024 + voff : OOB, 0, 0);
    unsigned short xq[9];
    v4u wqv[9];
    {
      const int r = lane >> 3, row0 = 288 * (sp & 1) + 72 * hw;
      const rsrc_t qr = mk_rsrc(wq, D * 8192);
      const int base = (row0 + r) * 8192 + (kvh * 256 + (sp >> 1) * 64) * 2 + (lane & 7) * 16;
#pragma unroll
      for (int i = 0; i < 9; ++i) wqv[i] = __builtin_amdgcn_raw_buffer_load_b128(qr, base, i * 8 * 8192, 0);
#pragma unroll
      for (int i = 0; i < 9; ++i) xq[i] = x[b * D + row0 + 8 * i + r];
    }
    const int xi = tid < 144 ? tid : 143;
    const v2u xa = ((const v2u*)x)[xi], xb = ((const v2u*)x)[144 + xi];
    if (tid < 3) { const v4u z = {0u, 0u, 0u, 0u}; *(LAS v4u*)(smem + L_SYNC + tid * 16) = z; }
    FENCE();
    LDSW();
    __builtin_amdgcn_s_barrier();
    asm volatile("" ::: "memory");
    const rsrc_t wsr = mk_rsrc(ws, WS_END);
    v2u evv = {0u, 0u};
    if (hw == 3) {
      evv = __builtin_amdgcn_raw_buffer_load_b64(wsr, WS_E, 0, AUX_SC1);
      const v4u z = {0u, 0u, 0u, 0u};
      __builtin_amdgcn_raw_buffer_store_b128(z, wsr, WS_H1 + bid * H1_STRIDE + voff, 0, AUX_SC1);
      __builtin_amdgcn_raw_buffer_store_b128(z, wsr, WS_H1 + bid * H1_STRIDE + 1024 + voff, 0, AUX_SC1);
      const int h2o = WS_H2 + (b * 36 * 128 + (bid & 127)) * 64 + (lane >> 2) * 8192 + (lane & 3) * 16;
      __builtin_amdgcn_raw_buffer_store_b128(z, wsr, h2o, 0, AUX_SC1);
      __builtin_amdgcn_raw_buffer_store_b128(z, wsr, h2o, 16 * 8192, AUX_SC1);
      int off = h2o + 32 * 8192;
      if (lane >= 16) off = WS_H1 + bid * H1_STRIDE + 2048 + (lane & 1) * 16;
      if (lane >= 32) off = sp == 0 ? WS_CNT + grp * 256 : OOB;
      __builtin_amdgcn_raw_buffer_store_b128(z, wsr, off, 0, AUX_SC1);
    }
    FENCE();
    {
      float acc[8];
#pragma unroll
      for (int j = 0; j < 8; ++j) acc[j] = 0.f;
#pragma unroll
      for (int i = 0; i < 9; ++i) {
        const float xv = bflo(xq[i]);
#pragma unroll
        for (int k = 0; k < 4; ++k) { acc[2 * k] = fmaf(xv, bflo(wqv[i][k]), acc[2 * k]); acc[2 * k + 1] = fmaf(xv, bfhi(wqv[i][k]), acc[2 * k + 1]); }
      }
      float a4[4];
#pragma unroll
      for (int j = 0; j < 4; ++j) a4[j] = swap32_sum(acc[j], acc[j + 4]);
      f32x2 r2; r2[0] = swap16_sum(a4[0], a4[2]); r2[1] = swap16_sum(a4[1], a4[3]);
      r2[0] += dpp<ROR8>(r2[0]); r2[1] += dpp<ROR8>(r2[1]);
      if ((lane & 8) == 0) *(LAS f32x2*)(smem + L_QP + (hw * 64 + (lane & 7) * 8 + 4 * (lane >> 5) + 2 * ((lane >> 4) & 1)) * 4) = r2;
      if (tid < 144) {
        v4u xs4;
        xs4[0] = (xa[0] & 0xffffu) | (xb[0] << 16); xs4[1] = (xa[0] >> 16) | (xb[0] & 0xffff0000u);
        xs4[2] = (xa[1] & 0xffffu) | (xb[1] << 16); xs4[3] = (xa[1] >> 16) | (xb[1] & 0xffff0000u);
        *(LAS v4u*)(smem + L_XS + tid * 16) = xs4;
      }
      lds_inc(smem, S_QP, lane);
      lds_wait<0>(smem, S_QP, 4u);
    }
    MAKE_MSK();
    unsigned tag_lo = 0, tag_hi = 0;
    if (hw == 3) {
      const unsigned long long ev = ((unsigned long long)(unsigned)__builtin_amdgcn_readfirstlane(evv[1]) << 32) | (unsigned)__builtin_amdgcn_readfirstlane(evv[0]);
      const unsigned long long tag64 = ev * 6364136223846793005ull + 1442695040888963407ull;
      tag_lo = (unsigned)tag64; tag_hi = (unsigned)(tag64 >> 32);
      const unsigned tag8 = tag_lo >> 24;
      {
        float s = 0.f;
#pragma unroll
        for (int ww = 0; ww < 4; ++ww) s += *(const LAS float*)(smem + L_QP + (ww * 64 + lane) * 4);
        DRAIN();
        v2u gr; gr[0] = ((__float_as_uint(s) + 0x80u) & 0xffffff00u) | tag8; gr[1] = tag_hi;
        __builtin_amdgcn_raw_buffer_store_b64(gr, wsr, WS_QG + ((grp * 2 + (sp & 1)) * 256 + (sp >> 1) * 64 + lane) * 8, 0, AUX_SC1);
        v2u fl; fl[0] = tag_lo; fl[1] = tag_hi;
        __builtin_amdgcn_raw_buffer_store_b64(fl, wsr, lane == 0 ? WS_ST + bid * 8 : OOB, 0, AUX_SC1);
      }
      for (unsigned spins = 0;; ++spins) {
        v4u p[4];
#pragma unroll
        for (int i = 0; i < 4; ++i) p[i] = __builtin_amdgcn_raw_buffer_load_b128(wsr, WS_QG + grp * 4096 + lane * 32 + (i & 1) * 16 + (i >> 1) * 2048, 0, AUX_SC1);
        unsigned bad = 0;
#pragma unroll
        for (int i = 0; i < 4; ++i) bad |= (p[i][1] ^ tag_hi) | (p[i][3] ^ tag_hi) | ((p[i][0] ^ tag8) & 0xffu) | ((p[i][2] ^ tag8) & 0xffu);
        if (__all(bad == 0u) || spins > SPIN_CAP) {
          float qv[4];
#pragma unroll
          for (int i = 0; i < 4; ++i)
            qv[i] = __uint_as_float(p[i >> 1][(i & 1) * 2] & 0xffffff00u) + __uint_as_float(p[2 + (i >> 1)][(i & 1) * 2] & 0xffffff00u);
          v2u qq; qq[0] = pk_bf16(qv[0], qv[1]); qq[1] = pk_bf16(qv[2], qv[3]);
          *(LAS v2u*)(smem + L_QS + lane * 8) = qq;
          break;
        }
        __builtin_amdgcn_s_sleep(2);
      }
      lds_inc(smem, S_Q, lane);
    }
    v4u G1[11], WD[18], WO[16];
    const int hb = 2 * rb + (hw >> 1);
    const int rg0 = hb * 72 / 7, nrg = (hb + 1) * 72 / 7 - rg0;
    const bool has_s1 = bid < 252;
    unsigned short gwv[9];
    {
      const rsrc_t gr = mk_rsrc(mat1 ? wu : wg, has_s1 ? D * F * 2 : 0);
      const int go = (4 * rg0 + g) * (F * 2) + cblk * 512 + (hw & 1) * 256 + c * 16;
#pragma unroll
      for (int i = 0; i < 11; ++i) G1[i] = __builtin_amdgcn_raw_buffer_load_b128(gr, go, (i < 10 || nrg == 11) ? i * 4 * (F * 2) : OOB, 0);
      const rsrc_t dr = mk_rsrc(wd, F * D * 2);
      const int wd_lo = (u0 + g) * 1152 + hw * 256 + c * 16;
      const int wd_hi = (hw == 0 && c < 8) ? wd_lo + 1024 : OOB;
#pragma unroll
      for (int i = 0; i < 9; ++i) WD[i] = __builtin_amdgcn_raw_buffer_load_b128(dr, wd_lo, i * 4608, 0);
#pragma unroll
      for (int i = 9; i < 18; ++i) WD[i] = __builtin_amdgcn_raw_buffer_load_b128(dr, wd_hi, (i - 9) * 4608, 0);
      const rsrc_t wor = mk_rsrc(wo, 4096 * D * 2);
      const int wo_lo = (kvh * 256 + 32 * sp + g) * 1152 + hw * 256 + c * 16;
      const int wo_hi = (hw == 0 && c < 8) ? wo_lo + 1024 : OOB;
#pragma unroll
      for (int i = 0; i < 8; ++i) WO[i] = __builtin_amdgcn_raw_buffer_load_b128(wor, wo_lo, i * 4608, 0);
#pragma unroll
      for (int i = 8; i < 16; ++i) WO[i] = __builtin_amdgcn_raw_buffer_load_b128(wor, wo_hi, (i - 8) * 4608, 0);
#pragma unroll
      for (int i = 0; i < 9; ++i) gwv[i] = gw[i * 64 + lane];
    }
    FENCE();
    {
      f32x4 ga = {0.f, 0.f, 0.f, 0.f};
#pragma unroll
      for (int i = 0; i < 11; ++i) {
        const int row = 4 * (rg0 + i) + g;
        const unsigned xv = *(const LAS unsigned*)(smem + L_XS + (row < D ? row : 0) * 4);
        ga = mfma(G1[i], diag(hi8 ? ((xv & 0xffff0000u) | (xv >> 16)) : ((xv << 16) | (xv & 0xffffu)), msk), ga);
      }
      if (hw < 2) {
        *(LAS f32x4*)(smem + L_GU + hw * 1024 + voff) = ga;
        lds_inc(smem, S_GU, lane);
      } else {
        lds_wait<2>(smem, S_GU, 2u);
        ga = *(const LAS f32x4*)(smem + L_GU + (hw & 1) * 1024 + voff) + ga;
        __builtin_amdgcn_raw_buffer_store_b128(__builtin_bit_cast(v4u, ga), wsr, has_s1 ? WS_G1 + (((cblk * 2 + (hw & 1)) * 2 + mat1) * 7 + rb) * 1024 + voff : OOB, 0, AUX_SC1);
        DRAIN();
        lds_inc(smem, S_G1, lane);
      }
    }
    float gt;
    {
      float z = 0.f;
#pragma unroll
      for (int i = 0; i < 9; ++i) {
        const unsigned xv = *(const LAS unsigned*)(smem + L_XS + (i * 64 + lane) * 4);
        z += rbf((b ? bfhi(xv) : bflo(xv)) * bflo(gwv[i]));
      }
      z += dpp<XOR1>(z); z += dpp<XOR2>(z); z += dpp<ROR4>(z); z += dpp<ROR8>(z);
      z = rbf(xrow_sum(z));
      gt = rbf(1.f / (1.f + __expf(-z)));
    }
    if (hw == 3) {
      lds_wait<4>(smem, S_G1, 2u);
      v2u fl; fl[0] = tag_lo; fl[1] = tag_hi;
      __builtin_amdgcn_raw_buffer_store_b64(fl, wsr, (lane == 0 && has_s1) ? WS_F1 + cblk * 128 + (mat1 * 7 + rb) * 8 : OOB, 0, AUX_SC1);
      __builtin_amdgcn_s_sleep(100);
      {
        const int fblk = lane < 32 ? (u0 >> 8) : ((u0 + 35) >> 8);
        const int fo = WS_F1 + fblk * 128 + ((lane & 31) < 14 ? (lane & 31) : 0) * 8;
        for (unsigned spins = 0;; ++spins) {
          const v2u p = __builtin_amdgcn_raw_buffer_load_b64(wsr, fo, 0, AUX_SC1);
          if (__all(((p[1] ^ tag_hi) | (p[0] ^ tag_lo)) == 0u) || spins > SPIN_CAP) break;
          __builtin_amdgcn_s_sleep(64);
        }
      }
      __builtin_amdgcn_fence(__ATOMIC_ACQUIRE, "wavefront");
      const int u = u0 + 4 * c + g;
      const int so = c < 9 ? WS_G1 + (u >> 7) * 14 * 1024 + (((u >> 5) & 3) * 16 + b * 8 + (u & 7)) * 16 + ((u >> 3) & 3) * 4 : OOB;
      float pv[14];
#pragma unroll
      for (int i = 0; i < 14; ++i) pv[i] = __uint_as_float(__builtin_amdgcn_raw_buffer_load_b32(wsr, so, i * 1024, AUX_SC1));
      const float gv = rbf(((pv[0] + pv[1]) + (pv[2] + pv[3])) + ((pv[4] + pv[5]) + pv[6]));
      const float uv = rbf(((pv[7] + pv[8]) + (pv[9] + pv[10])) + ((pv[11] + pv[12]) + pv[13]));
      const float inner = 0.7978845608028654f * (gv + 0.044715f * gv * gv * gv);
      const float hv = rbf(0.5f * gv * (1.f + tanhf(inner))) * uv;
      *(LAS unsigned*)(smem + L_HS + lane * 4) = pk_bf16(hv, hv);
      lds_inc(smem, S_H, lane);
    }
    f32x4 yf = {0.f, 0.f, 0.f, 0.f}, zf = {0.f, 0.f, 0.f, 0.f};
    {
      lds_wait<16>(smem, S_H, 1u);
      const unsigned hp = *(const LAS unsigned*)(smem + L_HS + lane * 4);
#define PRD(i) { const v4u bb = diag(dppu<BCAST + (i)>(hp), msk); yf = mfma(WD[i], bb, yf); zf = mfma(WD[9 + (i)], bb, zf); }
      PRD(0) PRD(1) PRD(2) PRD(3) PRD(4) PRD(5) PRD(6) PRD(7) PRD(8)
      const float og = rbf(1.f - gt);
      yf *= og; zf *= og;
      asm volatile("" : "+v"(yf), "+v"(zf));
    }
    FENCE();
    if (hw == 2) {
      const unsigned char* tp = ((lane & 8) ? Vc : Kc) + (size_t)grp * (SK * 256) + (size_t)(lane & 7) * (1 << 20) + NSU * 8192;
      if (lane == 16) tp = (const unsigned char*)out;
      unsigned tch = 0;
      if (lane < 17) tch = __builtin_nontemporal_load((const unsigned*)tp);
      asm volatile("" :: "v"(tch));
    }
    asm volatile("" :: "v"(cpf));
    if (hw == 3) {
      lds_wait<32>(smem, S_KDONE, 4u);
      v2u fl; fl[0] = tag_lo; fl[1] = tag_hi;
      __builtin_amdgcn_raw_buffer_store_b64(fl, wsr, lane == 0 ? WS_SF + bid * 64 : OOB, 0, AUX_SC1);
      for (unsigned spins = 0;; ++spins) {
        const v2u p = __builtin_amdgcn_raw_buffer_load_b64(wsr, WS_SF + (grp * 8 + (lane & 7)) * 64, 0, AUX_SC1);
        if (__all(((p[1] ^ tag_hi) | (p[0] ^ tag_lo)) == 0u) || spins > SPIN_CAP) break;
        __builtin_amdgcn_s_sleep(32);
      }
      lds_inc(smem, S_SHARED, lane);
      if (is_final) {
        for (unsigned spins = 0;; ++spins) {
          unsigned bad = 0;
#pragma unroll
          for (int i = 0; i < 2; ++i) {
            const v4u p = __builtin_amdgcn_raw_buffer_load_b128(wsr, WS_ST + i * 1024 + voff, 0, AUX_SC1);
            bad |= (p[1] ^ tag_hi) | (p[0] ^ tag_lo) | (p[3] ^ tag_hi) | (p[2] ^ tag_lo);
          }
          if (__all(bad == 0u) || spins > SPIN_CAP) break;
          __builtin_amdgcn_s_sleep(64);
        }
        __builtin_amdgcn_raw_buffer_store_b64(fl, wsr, (fid == 0 && lane == 0) ? WS_E : OOB, 0, AUX_SC1);
        lds_inc(smem, S_ALL, lane);
      }
    }
    FENCE();
    DRAIN();
    lds_wait<32>(smem, S_NEAR, 1u);
    lds_wait<0>(smem, S_O, 1u);
    const unsigned pp = *(const LAS unsigned*)(smem + L_OS + lane * 4);
    {
      f32x4 y0 = {0.f, 0.f, 0.f, 0.f}, y1 = {0.f, 0.f, 0.f, 0.f}, z0 = {0.f, 0.f, 0.f, 0.f};
#define PRJ(i, y) { const v4u bb = diag(dppu<BCAST + (i)>(pp), msk); y = mfma(WO[i], bb, y); z0 = mfma(WO[8 + (i)], bb, z0); }
      PRJ(0, y0) PRJ(1, y1) PRJ(2, y0) PRJ(3, y1) PRJ(4, y0) PRJ(5, y1) PRJ(6, y0) PRJ(7, y1)
      y0 = (y0 + y1) * gt + yf;
      z0 = z0 * gt + zf;
      const int dst = WS_H2 + ((b * 36 + hw * 8 + 2 * g) * 128 + (bid & 127)) * 64 + c * 4;
      __builtin_amdgcn_raw_buffer_store_b32(__float_as_uint(hi8 ? y0[1] : y0[0]) | 1u, wsr, dst, 0, AUX_SC1);
      __builtin_amdgcn_raw_buffer_store_b32(__float_as_uint(hi8 ? y0[3] : y0[2]) | 1u, wsr, dst, 8192, AUX_SC1);
      if (hw == 0) {
        const int dz = g < 2 ? dst + 32 * 8192 : OOB;
        __builtin_amdgcn_raw_buffer_store_b32(__float_as_uint(hi8 ? z0[1] : z0[0]) | 1u, wsr, dz, 0, AUX_SC1);
        __builtin_amdgcn_raw_buffer_store_b32(__float_as_uint(hi8 ? z0[3] : z0[2]) | 1u, wsr, dz, 8192, AUX_SC1);
      }
    }
  }
}

extern "C" void kernel_launch(void* const* d_in, const int* in_sizes, int n_in, void* d_out, int out_size, void* d_ws, size_t ws_size,
                              hipStream_t stream) {
  k_fused<<<256, NTHR, 0, stream>>>((const unsigned short*)d_in[0], (const unsigned char*)d_in[1], (const unsigned char*)d_in[2],
                                    (const unsigned char*)d_in[3], (const unsigned char*)d_in[4], (const unsigned char*)d_in[6],
                                    (const unsigned char*)d_in[7], (const unsigned char*)d_in[8], (const unsigned short*)d_in[9],
                                    (unsigned char*)d_ws, (unsigned*)d_out);
}
```

```cpp
#define VLO 0
#define VHI 4
#define KLO 32
#define KHI 32
#include <hip/hip_runtime.h>
#include <hip/hip_bf16.h>
#include <stdint.h>

typedef unsigned v4u __attribute__((ext_vector_type(4)));
typedef unsigned v2u __attribute__((ext_vector_type(2)));
typedef float f32x4 __attribute__((ext_vector_type(4)));
typedef float f32x2 __attribute__((ext_vector_type(2)));
typedef double f64x4 __attribute__((ext_vector_type(4)));
typedef short bf16x8 __attribute__((ext_vector_type(8)));
typedef __bf16 bf16x2 __attribute__((ext_vector_type(2)));
typedef __amdgpu_buffer_rsrc_t rsrc_t;
#define LAS __attribute__((address_space(3)))

constexpr int D = 576, SK = 32768, F = 4608;
constexpr int NTHR = 512;
constexpr unsigned NSU = 124;
constexpr unsigned ND = 8 * (128 - NSU);
constexpr unsigned NONE = 1024;
constexpr int NST = NSU / 4;
static_assert(NSU % 4 == 0 && NST % 2 == 1, "own steps per wave: odd");
constexpr int AUX_NT = 2, AUX_SC1 = 16;
constexpr int AUX_W = 0;
constexpr int OOB = 0x40000000;
constexpr unsigned SPIN_CAP = 200000u;
constexpr int PACE_T = 1756;
constexpr int PACE_CREDIT = 3500;
constexpr int CODE_BYTES = 25600;

constexpr int WS_E = 0;
constexpr int WS_CNT = 4096;
constexpr int WS_QG = 16384;
constexpr int WS_H1 = WS_QG + 131072;
constexpr int H1_STRIDE = 2176;
constexpr int WS_H2 = WS_H1 + 256 * H1_STRIDE;
constexpr int WS_ST = WS_H2 + 72 * 8192;
constexpr int WS_SF = WS_ST + 4096;
constexpr int WS_SG = WS_SF + 256 * 64;
constexpr int WS_F1 = WS_SG + 32 * (int)ND * 128;
constexpr int WS_G1 = WS_F1 + 4096;
constexpr int WS_END = WS_G1 + 36 * 14 * 1024;

constexpr int L_XS = 0;
constexpr int L_QP = 2304;
constexpr int L_QS = L_QP + 1024;
constexpr int L_MRG = L_QS + 512;
constexpr int L_ML = L_MRG + 8192;
constexpr int L_OS = L_ML + 96;
constexpr int L_GU = L_OS + 256;
constexpr int L_HS = L_GU + 2048;
constexpr int L_FB = L_HS + 256;
constexpr int L_SYNC = L_FB + 256;
constexpr int L_SC = L_SYNC + 48;
constexpr int L_SB = L_SC + 16384;
constexpr int L_END = L_SB + 4096;
constexpr int LDS_BYTES = 96 * 1024;
static_assert(L_END <= LDS_BYTES, "lds");
enum { S_Q = 0, S_G1 = 1, S_NEAR = 2, S_MRG = 3, S_O = 4, S_QP = 5, S_ALL = 6, S_KDONE = 7, S_FIN = 8, S_SHARED = 9, S_GU = 10, S_H = 11 };

__device__ __forceinline__ float bflo(unsigned u) { return __builtin_bit_cast(float, u << 16); }
__device__ __forceinline__ float bfhi(unsigned u) { return __builtin_bit_cast(float, u & 0xffff0000u); }
__device__ __forceinline__ unsigned pk_bf16(float lo, float hi) {
  bf16x2 v; v[0] = (__bf16)lo; v[1] = (__bf16)hi; return __builtin_bit_cast(unsigned, v);
}
__device__ __forceinline__ float rbf(float f) { return bflo(pk_bf16(f, f)); }
__device__ __forceinline__ float dot2(unsigned a, unsigned b, float c) {
  return __builtin_amdgcn_fdot2_f32_bf16(__builtin_bit_cast(bf16x2, a), __builtin_bit_cast(bf16x2, b), c, false);
}
constexpr int XOR1 = 0xB1, XOR2 = 0x4E, HMIR = 0x141, ROR4 = 0x124, ROR8 = 0x128, BCAST = 0x150;
template <int CTRL> __device__ __forceinline__ unsigned dppu(unsigned x) { return (unsigned)__builtin_amdgcn_mov_dpp((int)x, CTRL, 0xf, 0xf, true); }
template <int CTRL> __device__ __forceinline__ float dpp(float x) { return __uint_as_float(dppu<CTRL>(__float_as_uint(x))); }
template <int CTRL> __device__ __forceinline__ double dppd(double x) {
  const v2u u = __builtin_bit_cast(v2u, x);
  v2u r; r[0] = dppu<CTRL>(u[0]); r[1] = dppu<CTRL>(u[1]);
  return __builtin_bit_cast(double, r);
}
__device__ __forceinline__ f32x4 mfma(v4u a, v4u b, f32x4 c) {
  return __builtin_amdgcn_mfma_f32_16x16x32_bf16(__builtin_bit_cast(bf16x8, a), __builtin_bit_cast(bf16x8, b), c, 0, 0, 0);
}
__device__ __forceinline__ float swap16_sum(float a, float b) {
  auto s = __builtin_amdgcn_permlane16_swap(__float_as_uint(a), __float_as_uint(b), false, false);
  return __uint_as_float(s[0]) + __uint_as_float(s[1]);
}
__device__ __forceinline__ float swap32_sum(float a, float b) {
  auto s = __builtin_amdgcn_permlane32_swap(__float_as_uint(a), __float_as_uint(b), false, false);
  return __uint_as_float(s[0]) + __uint_as_float(s[1]);
}
__device__ __forceinline__ float xrow_max(float x) {
  auto s = __builtin_amdgcn_permlane16_swap(__float_as_uint(x), __float_as_uint(x), false, false);
  x = fmaxf(__uint_as_float(s[0]), __uint_as_float(s[1]));
  auto t = __builtin_amdgcn_permlane32_swap(__float_as_uint(x), __float_as_uint(x), false, false);
  return fmaxf(__uint_as_float(t[0]), __uint_as_float(t[1]));
}
__device__ __forceinline__ float xrow_sum(float x) { x = swap16_sum(x, x); return swap32_sum(x, x); }
__device__ __forceinline__ double xrow_sumd(double x) {
  v2u u = __builtin_bit_cast(v2u, x), a, b2;
  { auto s0 = __builtin_amdgcn_permlane16_swap(u[0], u[0], false, false); auto s1 = __builtin_amdgcn_permlane16_swap(u[1], u[1], false, false);
    a[0] = s0[0]; a[1] = s1[0]; b2[0] = s0[1]; b2[1] = s1[1]; }
  x = __builtin_bit_cast(double, a) + __builtin_bit_cast(double, b2);
  u = __builtin_bit_cast(v2u, x);
  { auto s0 = __builtin_amdgcn_permlane32_swap(u[0], u[0], false, false); auto s1 = __builtin_amdgcn_permlane32_swap(u[1], u[1], false, false);
    a[0] = s0[0]; a[1] = s1[0]; b2[0] = s0[1]; b2[1] = s1[1]; }
  return __builtin_bit_cast(double, a) + __builtin_bit_cast(double, b2);
}
__device__ __forceinline__ rsrc_t mk_rsrc(const void* p, int bytes) { return __builtin_amdgcn_make_buffer_rsrc((void*)p, 0, bytes, 0x00020000); }
#define DRAIN() do { __builtin_amdgcn_s_waitcnt(0x0F70); asm volatile("s_waitcnt vmcnt(0)" ::: "memory"); } while (0)
#define LDSW() asm volatile("s_waitcnt lgkmcnt(0)" ::: "memory")
#define FENCE() __builtin_amdgcn_sched_barrier(0)

constexpr float SCALE = 0.08838834764831845f, L2E = 1.4426950408889634f;

struct St { float m; double l; f64x4 acc; };
__device__ __forceinline__ void pin(St& st) { asm volatile("" : "+v"(st.m), "+v"(st.l), "+v"(st.acc) :: "memory"); }
__device__ __forceinline__ v4u diag(unsigned pp, const v4u& msk) {
  v4u bb; bb[0] = pp & msk[0]; bb[1] = pp & msk[1]; bb[2] = pp & msk[2]; bb[3] = pp & msk[3];
  return bb;
}

__device__ __forceinline__ v2u scores(const v4u* KB, const v4u& q0, const v4u& q1, bool hi8) {
  float s[4];
#pragma unroll
  for (int t = 0; t < 4; ++t) {
    float a0 = dot2(KB[t][0], q0[0], 0.f), a1 = dot2(KB[t][0], q1[0], 0.f);
#pragma unroll
    for (int k = 1; k < 4; ++k) { a0 = dot2(KB[t][k], q0[k], a0); a1 = dot2(KB[t][k], q1[k], a1); }
    const float keep = hi8 ? a1 : a0, send = hi8 ? a0 : a1;
    float v = keep + dpp<ROR8>(send);
    v += dpp<HMIR>(v); v += dpp<XOR2>(v); v += dpp<XOR1>(v);
    s[t] = v;
  }
  const unsigned u0 = pk_bf16(s[0], s[1]), u1 = pk_bf16(s[2], s[3]);
  v2u r; r[0] = pk_bf16(bflo(u0) * SCALE, bfhi(u0) * SCALE); r[1] = pk_bf16(bflo(u1) * SCALE, bfhi(u1) * SCALE);
  return r;
}
__device__ __forceinline__ void pv_unit(const v4u (&VB)[8], const v4u sc, const v4u& msk, St& st) {
  float s[8];
#pragma unroll
  for (int t = 0; t < 4; ++t) { s[2 * t] = bflo(sc[t]); s[2 * t + 1] = bfhi(sc[t]); }
  float tm = fmaxf(fmaxf(fmaxf(s[0], s[1]), fmaxf(s[2], s[3])), fmaxf(fmaxf(s[4], s[5]), fmaxf(s[6], s[7])));
  tm = xrow_max(tm);
  const float tl = tm * L2E, mt = __builtin_ceilf(tl), fr = tl - mt;
  const float mn = fmaxf(st.m, mt);
  const double alpha = (double)__builtin_amdgcn_exp2f(st.m - mn);
  const float beta = __builtin_amdgcn_exp2f(mt - mn);
  st.m = mn;
  float p[8];
#pragma unroll
  for (int t = 0; t < 8; ++t) p[t] = __builtin_amdgcn_exp2f(fmaf(s[t] - tm, L2E, fr));
  f32x4 ta = {0.f, 0.f, 0.f, 0.f};
#pragma unroll
  for (int t = 0; t < 8; ++t) ta = mfma(VB[t], diag(pk_bf16(p[t], p[t]), msk), ta);
  st.l = __builtin_fma(st.l, alpha, (double)((((p[0] + p[1]) + (p[2] + p[3])) + ((p[4] + p[5]) + (p[6] + p[7]))) * beta));
#pragma unroll
  for (int r = 0; r < 4; ++r) st.acc[r] = __builtin_fma(st.acc[r], alpha, (double)(ta[r] * beta));
}
__device__ __forceinline__ void issue(v4u (&B)[8], rsrc_t r, int voff, unsigned unit) {
  const int so = (int)(unit * 8192u);
#pragma unroll
  for (int t = 0; t < 4; ++t) B[t] = __builtin_amdgcn_raw_buffer_load_b128(r, voff + t * 1024, so, AUX_NT);
#pragma unroll
  for (int t = 0; t < 4; ++t) B[4 + t] = __builtin_amdgcn_raw_buffer_load_b128(r, voff + t * 1024, so + 4096, AUX_NT);
  FENCE();
}
__device__ __forceinline__ void issue2(v4u (&B)[8], rsrc_t r, int voff, unsigned unit, bool def) {
  const int so = (int)(unit * 8192u);
  if (def) {
#pragma unroll
    for (int t = 0; t < 4; ++t) B[t] = __builtin_amdgcn_raw_buffer_load_b128(r, voff + t * 1024, so, 0);
#pragma unroll
    for (int t = 0; t < 4; ++t) B[4 + t] = __builtin_amdgcn_raw_buffer_load_b128(r, voff + t * 1024, so + 4096, 0);
  } else {
#pragma unroll
    for (int t = 0; t < 4; ++t) B[t] = __builtin_amdgcn_raw_buffer_load_b128(r, voff + t * 1024, so, AUX_NT);
#pragma unroll
    for (int t = 0; t < 4; ++t) B[4 + t] = __builtin_amdgcn_raw_buffer_load_b128(r, voff + t * 1024, so + 4096, AUX_NT);
  }
  FENCE();
}
__device__ __forceinline__ unsigned atomic_inc(rsrc_t r, int voff0, bool on, int so) {
  return (unsigned)__builtin_amdgcn_raw_ptr_buffer_atomic_add_i32(1, r, voff0, on ? so : OOB, 0);
}
__device__ __forceinline__ unsigned lds_inc(unsigned char* smem, int idx, int lane) {
  LDSW();
  unsigned old = 0;
  if (lane == 0) old = __hip_atomic_fetch_add((LAS unsigned*)(LAS unsigned char*)smem + (L_SYNC / 4 + idx), 1u, __ATOMIC_RELAXED, __HIP_MEMORY_SCOPE_WORKGROUP);
  return old;
}
template <int SLEEP> __device__ __forceinline__ void lds_wait(unsigned char* smem, int idx, unsigned want) {
#pragma unroll 1
  for (unsigned spins = 0; spins < 8000000u / (1 + SLEEP); ++spins) {
    const unsigned v = __builtin_amdgcn_readfirstlane(*((volatile LAS unsigned*)(LAS unsigned char*)smem + (L_SYNC / 4 + idx)));
    if (v >= want) break;
    if (SLEEP) __builtin_amdgcn_s_sleep(SLEEP);
  }
  asm volatile("" ::: "memory");
}
__device__ __forceinline__ float val(unsigned u) { return __uint_as_float(u & ~1u); }

__global__ void __launch_bounds__(NTHR, 2)
k_fused(const unsigned short* __restrict__ x, const unsigned char* Kc, const unsigned char* Vc,
        const unsigned char* __restrict__ wq, const unsigned char* wo, const unsigned char* wg,
        const unsigned char* wu, const unsigned char* wd, const unsigned short* __restrict__ gw,
        unsigned char* ws, unsigned* out) {
  __shared__ __attribute__((aligned(16))) unsigned char smem[LDS_BYTES];
  const int tid = threadIdx.x, lane = tid & 63, w = __builtin_amdgcn_readfirstlane(tid >> 6);
  const int bid = blockIdx.x, grp = bid >> 3, sp = bid & 7, b = grp >> 4, kvh = grp & 15;
  const int g = lane >> 4, c = lane & 15;
  const int voff = lane * 16;
  const int ridx = (grp + 4 * sp) & 31;
  const bool is_final = ridx < 9;
  const int fid = sp * 9 + ridx;
  const bool hi8 = (c & 8) != 0;
  const int cm = bid % 36, rb = bid / 36, mat1 = cm >= 18 ? 1 : 0, cblk = cm - 18 * mat1;
  const int u0 = 36 * (bid & 127);
#define MAKE_MSK() v4u msk; _Pragma("unroll") for (int d = 0; d < 4; ++d) msk[d] = (((c & 7) >> 1) == d) ? ((c & 1) ? 0xffff0000u : 0x0000ffffu) : 0u;

  if (w >= 4) {
    const int sw = w - 4, stid = tid - 256;
    const rsrc_t kr = mk_rsrc(Kc + (size_t)grp * (SK * 256), SK * 256);
    const rsrc_t vr = mk_rsrc(Vc + (size_t)grp * (SK * 256), SK * 256);
    const rsrc_t wsr = mk_rsrc(ws, WS_END);
    const unsigned ubase = (unsigned)(sp * 128 + sw);
    v4u Ba[8], Bb[8], Be0[8], Be1[8];
    issue(Be0, kr, voff, ubase);
    asm volatile("" ::: "memory");
    __builtin_amdgcn_s_barrier();
    asm volatile("" ::: "memory");
    FENCE();
    if (sw == 0) {
      const unsigned char* tp = Vc + (size_t)bid * (1 << 20);
      if (lane == 1) tp = (mat1 ? wu : wg) + (size_t)(rb < 7 ? (2 * rb) * 72 / 7 * 4 : 0) * (F * 2);
      if (lane == 2) tp = (mat1 ? wu : wg) + (size_t)(rb < 7 ? (2 * rb + 2) * 72 / 7 * 4 - 1 : 0) * (F * 2);
      if (lane == 3) tp = wd + (size_t)u0 * 1152;
      if (lane == 4) tp = wo + (size_t)(kvh * 256 + 32 * sp) * 1152;
      if (lane == 5) tp = (const unsigned char*)gw;
      unsigned tch = 0;
      if (lane < 6) tch = __builtin_nontemporal_load((const unsigned*)tp);
      asm volatile("" :: "v"(tch));
    }
    MAKE_MSK();
    DRAIN(); issue(Be1, kr, voff, ubase + 4);
    DRAIN(); issue(Ba, kr, voff, ubase + 8);
    DRAIN(); issue(Bb, kr, voff, ubase + 12);
    lds_wait<1>(smem, S_Q, 1u);
    const v4u q0 = *(const LAS v4u*)(smem + L_QS + c * 16), q1 = *(const LAS v4u*)(smem + L_QS + 256 + c * 16);
    const int sc_off = ((c >> 3) * 4 + g) * 16;
#define KSC(B) const v2u s0_ = scores(&B[0], q0, q1, hi8), s1_ = scores(&B[4], q0, q1, hi8);                       \
      v4u sc_; sc_[0] = s0_[0]; sc_[1] = s0_[1]; sc_[2] = s1_[0]; sc_[3] = s1_[1];
#define KSTEP(B, i)                                                                                                \
    {                                                                                                              \
      KSC(B)                                                                                                       \
      if ((c & 7) == 0) *(LAS v4u*)(smem + L_SC + (4 * (i) + sw) * 128 + sc_off) = sc_;                            \
      asm volatile("" ::: "memory");                                                                               \
      FENCE();                                                                                                     \
    }
    int nsl = 0;
    unsigned did = 0;
#define SFLUSH()                                                                                                   \
    {                                                                                                              \
      const v4u v_ = *(const LAS v4u*)(smem + L_SB + sw * 1024 + voff);                                            \
      __builtin_amdgcn_raw_buffer_store_b128(v_, wsr, (lane >> 3) < nsl ? WS_SG + (int)((grp * ND + did) * 128u) + (lane & 7) * 16 : OOB, 0, AUX_SC1); \
      nsl = 0;                                                                                                     \
    }
#define KSTEPG(B, dd)                                                                                              \
    {                                                                                                              \
      KSC(B)                                                                                                       \
      if ((c & 7) == 0) *(LAS v4u*)(smem + L_SB + sw * 1024 + nsl * 128 + sc_off) = sc_;                           \
      did = (lane >> 3) == nsl ? (dd) : did;                                                                       \
      ++nsl;                                                                                                       \
      if (nsl == 8) SFLUSH()                                                                                       \
      asm volatile("" ::: "memory");                                                                               \
      FENCE();                                                                                                     \
    }
#define PACE() do { int lag_; while ((lag_ = (int)((unsigned)__builtin_amdgcn_s_memrealtime() * 16u - due)) < 0) __builtin_amdgcn_s_sleep(1); \
                    if (lag_ > PACE_CREDIT) due += (unsigned)(lag_ - PACE_CREDIT); due += PACE_T; } while (0)
#define MAPD(d) (((d) & 7u) * 128u + NSU + ((d) >> 3))
    const int cnt0 = lane == 0 ? WS_CNT + grp * 256 : OOB;
    unsigned a = 0u;
    KSTEP(Ba, 2);
    issue(Ba, kr, voff, ubase + 16);
    KSTEP(Be0, 0);
    KSTEP(Be1, 1);
    KSTEP(Bb, 3);
    unsigned due = (unsigned)__builtin_amdgcn_s_memrealtime() * 16u;
#pragma unroll 1
    for (int i = 4; i < NST - 1; i += 2) {
      PACE();
      DRAIN();
      issue2(Bb, kr, voff, ubase + 4 * (i + 1), grp >= KLO && grp < KHI);
      KSTEP(Ba, i);
      PACE();
      DRAIN();
      a = atomic_inc(wsr, cnt0, i + 3 >= NST, 0);
      issue2(Ba, kr, voff, ubase + 4 * (i + 2), grp >= KLO && grp < KHI);
      KSTEP(Bb, i + 1);
    }
    {
      unsigned da, db;
      DRAIN();
      db = __builtin_amdgcn_readfirstlane(a);
      a = atomic_inc(wsr, cnt0, db < ND, 0);
      issue(Bb, db < ND ? kr : vr, voff, db < ND ? MAPD(db) : ubase);
      KSTEP(Ba, NST - 1);
      bool in_b = true;
      while (db < ND) {
        DRAIN();
        da = __builtin_amdgcn_readfirstlane(a);
        a = atomic_inc(wsr, cnt0, da < ND, 0);
        issue(Ba, da < ND ? kr : vr, voff, da < ND ? MAPD(da) : ubase);
        KSTEPG(Bb, db);
        if (da >= ND) { in_b = false; break; }
        DRAIN();
        db = __builtin_amdgcn_readfirstlane(a);
        a = atomic_inc(wsr, cnt0, db < ND, 0);
        issue(Bb, db < ND ? kr : vr, voff, db < ND ? MAPD(db) : ubase);
        KSTEPG(Ba, da);
      }
      SFLUSH()
      DRAIN();
      if (in_b) {
#pragma unroll
        for (int t = 0; t < 8; ++t) Ba[t] = Bb[t];
      }
    }
    lds_inc(smem, S_KDONE, lane);
    St st; st.m = -INFINITY; st.l = 0.0; st.acc = f64x4{0.0, 0.0, 0.0, 0.0};
#define LSC(i) (*(const LAS v4u*)(smem + L_SC + (4 * (i) + sw) * 128 + sc_off))
    due = (unsigned)__builtin_amdgcn_s_memrealtime() * 16u;
#pragma unroll 1
    for (int i = 0; i < NST - 1; i += 2) {
      PACE();
      DRAIN();
      issue2(Bb, vr, voff, ubase + 4 * (i + 1), grp >= VLO && grp < VHI);
      pv_unit(Ba, LSC(i), msk, st); pin(st);
      FENCE();
      PACE();
      DRAIN();
      a = atomic_inc(wsr, cnt0, i + 3 >= NST, 4);
      issue2(Ba, vr, voff, ubase + 4 * (i + 2), grp >= VLO && grp < VHI);
      pv_unit(Bb, LSC(i + 1), msk, st); pin(st);
      FENCE();
    }
    lds_wait<2>(smem, S_SHARED, 1u);
    lds_inc(smem, S_NEAR, lane);
    v4u sa = LSC(NST - 1), sb;
    const int sg_ld = WS_SG + grp * (int)ND * 128 + sc_off;
    while (true) {
      DRAIN();
      unsigned d = __builtin_amdgcn_readfirstlane(a);
      a = atomic_inc(wsr, cnt0, d < ND, 4);
      sb = __builtin_amdgcn_raw_buffer_load_b128(wsr, sg_ld, d < ND ? (int)(d * 128u) : OOB, AUX_SC1);
      issue(Bb, vr, voff, d < ND ? MAPD(d) : NONE);
      pv_unit(Ba, sa, msk, st); pin(st);
      FENCE();
      if (d >= ND) break;
      DRAIN();
      d = __builtin_amdgcn_readfirstlane(a);
      a = atomic_inc(wsr, cnt0, d < ND, 4);
      sa = __builtin_amdgcn_raw_buffer_load_b128(wsr, sg_ld, d < ND ? (int)(d * 128u) : OOB, AUX_SC1);
      issue(Ba, vr, voff, d < ND ? MAPD(d) : NONE);
      pv_unit(Bb, sb, msk, st); pin(st);
      FENCE();
      if (d >= ND) break;
    }
    {
      const double l = xrow_sumd(st.l);
      const int h = c >> 3;
#pragma unroll
      for (int r = 0; r < 4; ++r) {
        const int dim = 8 * (4 * g + r) + (c & 7);
        *(LAS double*)(smem + L_MRG + (sw * 256 + h * 128 + (dim & 96) + (dim & 3) * 8 + ((dim >> 2) & 7)) * 8) = st.acc[r];
      }
      if (g == 0 && (c & 7) == 0) { *(LAS float*)(smem + L_ML + (h * 4 + sw) * 4) = st.m; *(LAS double*)(smem + L_ML + 32 + (h * 4 + sw) * 8) = l; }
    }
    lds_inc(smem, S_MRG, lane);
    lds_wait<0>(smem, S_MRG, 4u);
    {
      const int hh = sw >> 1;
      const f32x4 m4 = *(const LAS f32x4*)(smem + L_ML + hh * 16);
      const f64x4 l4 = *(const LAS f64x4*)(smem + L_ML + 32 + hh * 32);
      double a4[4];
#pragma unroll
      for (int ww = 0; ww < 4; ++ww) a4[ww] = *(const LAS double*)(smem + L_MRG + (ww * 256 + stid) * 8);
      const float M = fmaxf(fmaxf(m4[0], m4[1]), fmaxf(m4[2], m4[3]));
      double L = 0.0, o = 0.0;
#pragma unroll
      for (int ww = 0; ww < 4; ++ww) {
        const double wt = (double)__builtin_amdgcn_exp2f(m4[ww] - M);
        L = __builtin_fma(wt, l4[ww], L); o = __builtin_fma(wt, a4[ww], o);
      }
      v2u ov = __builtin_bit_cast(v2u, o); ov[0] |= 1u;
      __builtin_amdgcn_raw_buffer_store_b64(ov, wsr, WS_H1 + bid * H1_STRIDE + stid * 8, 0, AUX_SC1);
      if ((stid & 127) < 2) {
        v2u ml = __builtin_bit_cast(v2u, L); ml[0] |= 1u;
        if (stid & 1) { ml[0] = 1u; ml[1] = __float_as_uint(M); }
        __builtin_amdgcn_raw_buffer_store_b64(ml, wsr, WS_H1 + bid * H1_STRIDE + 2048 + hh * 16 + (stid & 1) * 8, 0, AUX_SC1);
      }
    }
    for (int i = 0; i < sw; ++i) __builtin_amdgcn_s_sleep(4);
    {
      const int o_acc = WS_H1 + (grp * 8 + 4 * (c >> 3)) * H1_STRIDE + (32 * sp + 8 * g + (c & 7)) * 8;
      const int o_ml = WS_H1 + (grp * 8 + (c & 7)) * H1_STRIDE + 2048 + (sp >> 2) * 16;
      v2u pv[4];
      v4u pm = {0u, 0u, 0u, 0u};
#pragma unroll
      for (int i = 0; i < 4; ++i) pv[i] = v2u{0u, 0u};
      bool mine = true;
      for (unsigned spins = 0;; ++spins) {
        if (!(pv[0][0] & pv[1][0] & pv[2][0] & pv[3][0] & 1u)) {
#pragma unroll
          for (int i = 0; i < 4; ++i) pv[i] = __builtin_amdgcn_raw_buffer_load_b64(wsr, o_acc, i * H1_STRIDE, AUX_SC1);
        }
        if (!(pm[0] & pm[2] & 1u)) pm = __builtin_amdgcn_raw_buffer_load_b128(wsr, o_ml, 0, AUX_SC1);
        if (__all((pv[0][0] & pv[1][0] & pv[2][0] & pv[3][0] & pm[0] & pm[2] & 1u) != 0u) || spins > SPIN_CAP) break;
        if (__builtin_amdgcn_readfirstlane(*((volatile LAS unsigned*)(smem + L_SYNC) + S_O)) != 0u) { mine = false; break; }
        __builtin_amdgcn_s_sleep(2);
      }
      if (mine) {
        const float Ms = __uint_as_float(pm[3]);
        float Mx = fmaxf(Ms, dpp<XOR1>(Ms)); Mx = fmaxf(Mx, dpp<XOR2>(Mx)); Mx = fmaxf(Mx, dpp<HMIR>(Mx));
        const float wt = __builtin_amdgcn_exp2f(Ms - Mx);
        v2u lv; lv[0] = pm[0]; lv[1] = pm[1];
        double den = (double)wt * __builtin_bit_cast(double, lv);
        den += dppd<XOR1>(den); den += dppd<XOR2>(den); den += dppd<HMIR>(den);
        double num = 0.0;
#define ACC1(i) { const float wl_ = dpp<BCAST + (i)>(wt), wh_ = dpp<BCAST + 4 + (i)>(wt); num = __builtin_fma((double)(hi8 ? wh_ : wl_), __builtin_bit_cast(double, pv[i]), num); }
        ACC1(0) ACC1(1) ACC1(2) ACC1(3)
        num += dppd<ROR8>(num);
        const float ov = (float)num / (float)den;
        *(LAS unsigned*)(smem + L_OS + lane * 4) = pk_bf16(ov, ov);
        lds_inc(smem, S_O, lane);
      }
    }
    if (is_final) {
      const int src = WS_H2 + fid * 8192 + sw * 2048 + voff;
      lds_wait<8>(smem, S_ALL, 1u);
      v4u p0, p1;
      for (unsigned spins = 0;; ++spins) {
        p0 = __builtin_amdgcn_raw_buffer_load_b128(wsr, src, 0, AUX_SC1);
        p1 = __builtin_amdgcn_raw_buffer_load_b128(wsr, src, 1024, AUX_SC1);
        const unsigned m = (p0[0] & p0[1] & p0[2] & p0[3]) & (p1[0] & p1[1] & p1[2] & p1[3]) & 1u;
        if (__all(m != 0u) || spins > SPIN_CAP) break;
        __builtin_amdgcn_s_sleep(2);
      }
      f32x4 t4;
#pragma unroll
      for (int j = 0; j < 4; ++j) {
        float t = val(p0[j]) + val(p1[j]);
        t += dpp<ROR4>(t); t += dpp<ROR8>(t);
        t4[j] = xrow_sum(t);
      }
      if (lane < 4) *(LAS f32x4*)(smem + L_FB + (sw * 4 + lane) * 16) = t4;
      const unsigned arrived = __builtin_amdgcn_readfirstlane(lds_inc(smem, S_FIN, lane));
      asm volatile("" ::: "memory");
      if (arrived == 3u && lane < 8) {
        f32x2 t = *(const LAS f32x2*)(smem + L_FB + lane * 8);
#pragma unroll
        for (int ww = 1; ww < 4; ++ww) t += *(const LAS f32x2*)(smem + L_FB + ww * 64 + lane * 8);
        out[fid * 8 + lane] = pk_bf16(t[0], t[1]);
      }
    }
  } else {
    const int hw = w;
    const v4u cpf = __builtin_amdgcn_raw_buffer_load_b128(mk_rsrc((const void*)(__builtin_amdgcn_s_getpc() & ~255ull), CODE_BYTES),
                                                          hw == 0 ? grp * 1024 + voff : OOB, 0, 0);
    unsigned short xq[9];
    v4u wqv[9];
    {
      const int r = lane >> 3, row0 = 288 * (sp & 1) + 72 * hw;
      const rsrc_t qr = mk_rsrc(wq, D * 8192);
      const int base = (row0 + r) * 8192 + (kvh * 256 + (sp >> 1) * 64) * 2 + (lane & 7) * 16;
#pragma unroll
      for (int i = 0; i < 9; ++i) wqv[i] = __builtin_amdgcn_raw_buffer_load_b128(qr, base, i * 8 * 8192, AUX_W);
#pragma unroll
      for (int i = 0; i < 9; ++i) xq[i] = x[b * D + row0 + 8 * i + r];
    }
    const int xi = tid < 144 ? tid : 143;
    const v2u xa = ((const v2u*)x)[xi], xb = ((const v2u*)x)[144 + xi];
    if (tid < 3) { const v4u z = {0u, 0u, 0u, 0u}; *(LAS v4u*)(smem + L_SYNC + tid * 16) = z; }
    FENCE();
    LDSW();
    __builtin_amdgcn_s_barrier();
    asm volatile("" ::: "memory");
    const rsrc_t wsr = mk_rsrc(ws, WS_END);
    v2u evv = {0u, 0u};
    if (hw == 3) {
      evv = __builtin_amdgcn_raw_buffer_load_b64(wsr, WS_E, 0, AUX_SC1);
      const v4u z = {0u, 0u, 0u, 0u};
      __builtin_amdgcn_raw_buffer_store_b128(z, wsr, WS_H1 + bid * H1_STRIDE + voff, 0, AUX_SC1);
      __builtin_amdgcn_raw_buffer_store_b128(z, wsr, WS_H1 + bid * H1_STRIDE + 1024 + voff, 0, AUX_SC1);
      const int h2o = WS_H2 + (b * 36 * 128 + (bid & 127)) * 64 + (lane >> 2) * 8192 + (lane & 3) * 16;
      __builtin_amdgcn_raw_buffer_store_b128(z, wsr, h2o, 0, AUX_SC1);
      __builtin_amdgcn_raw_buffer_store_b128(z, wsr, h2o, 16 * 8192, AUX_SC1);
      int off = h2o + 32 * 8192;
      if (lane >= 16) off = WS_H1 + bid * H1_STRIDE + 2048 + (lane & 1) * 16;
      if (lane >= 32) off = sp == 0 ? WS_CNT + grp * 256 : OOB;
      __builtin_amdgcn_raw_buffer_store_b128(z, wsr, off, 0, AUX_SC1);
    }
    FENCE();
    {
      float acc[8];
#pragma unroll
      for (int j = 0; j < 8; ++j) acc[j] = 0.f;
#pragma unroll
      for (int i = 0; i < 9; ++i) {
        const float xv = bflo(xq[i]);
#pragma unroll
        for (int k = 0; k < 4; ++k) { acc[2 * k] = fmaf(xv, bflo(wqv[i][k]), acc[2 * k]); acc[2 * k + 1] = fmaf(xv, bfhi(wqv[i][k]), acc[2 * k + 1]); }
      }
      float a4[4];
#pragma unroll
      for (int j = 0; j < 4; ++j) a4[j] = swap32_sum(acc[j], acc[j + 4]);
      f32x2 r2; r2[0] = swap16_sum(a4[0], a4[2]); r2[1] = swap16_sum(a4[1], a4[3]);
      r2[0] += dpp<ROR8>(r2[0]); r2[1] += dpp<ROR8>(r2[1]);
      if ((lane & 8) == 0) *(LAS f32x2*)(smem + L_QP + (hw * 64 + (lane & 7) * 8 + 4 * (lane >> 5) + 2 * ((lane >> 4) & 1)) * 4) = r2;
      if (tid < 144) {
        v4u xs4;
        xs4[0] = (xa[0] & 0xffffu) | (xb[0] << 16); xs4[1] = (xa[0] >> 16) | (xb[0] & 0xffff0000u);
        xs4[2] = (xa[1] & 0xffffu) | (xb[1] << 16); xs4[3] = (xa[1] >> 16) | (xb[1] & 0xffff0000u);
        *(LAS v4u*)(smem + L_XS + tid * 16) = xs4;
      }
      lds_inc(smem, S_QP, lane);
      lds_wait<0>(smem, S_QP, 4u);
    }
    MAKE_MSK();
    unsigned tag_lo = 0, tag_hi = 0;
    if (hw == 3) {
      const unsigned long long ev = ((unsigned long long)(unsigned)__builtin_amdgcn_readfirstlane(evv[1]) << 32) | (unsigned)__builtin_amdgcn_readfirstlane(evv[0]);
      const unsigned long long tag64 = ev * 6364136223846793005ull + 1442695040888963407ull;
      tag_lo = (unsigned)tag64; tag_hi = (unsigned)(tag64 >> 32);
      const unsigned tag8 = tag_lo >> 24;
      {
        float s = 0.f;
#pragma unroll
        for (int ww = 0; ww < 4; ++ww) s += *(const LAS float*)(smem + L_QP + (ww * 64 + lane) * 4);
        DRAIN();
        v2u gr; gr[0] = ((__float_as_uint(s) + 0x80u) & 0xffffff00u) | tag8; gr[1] = tag_hi;
        __builtin_amdgcn_raw_buffer_store_b64(gr, wsr, WS_QG + ((grp * 2 + (sp & 1)) * 256 + (sp >> 1) * 64 + lane) * 8, 0, AUX_SC1);
        v2u fl; fl[0] = tag_lo; fl[1] = tag_hi;
        __builtin_amdgcn_raw_buffer_store_b64(fl, wsr, lane == 0 ? WS_ST + bid * 8 : OOB, 0, AUX_SC1);
      }
      for (unsigned spins = 0;; ++spins) {
        v4u p[4];
#pragma unroll
        for (int i = 0; i < 4; ++i) p[i] = __builtin_amdgcn_raw_buffer_load_b128(wsr, WS_QG + grp * 4096 + lane * 32 + (i & 1) * 16 + (i >> 1) * 2048, 0, AUX_SC1);
        unsigned bad = 0;
#pragma unroll
        for (int i = 0; i < 4; ++i) bad |= (p[i][1] ^ tag_hi) | (p[i][3] ^ tag_hi) | ((p[i][0] ^ tag8) & 0xffu) | ((p[i][2] ^ tag8) & 0xffu);
        if (__all(bad == 0u) || spins > SPIN_CAP) {
          float qv[4];
#pragma unroll
          for (int i = 0; i < 4; ++i)
            qv[i] = __uint_as_float(p[i >> 1][(i & 1) * 2] & 0xffffff00u) + __uint_as_float(p[2 + (i >> 1)][(i & 1) * 2] & 0xffffff00u);
          v2u qq; qq[0] = pk_bf16(qv[0], qv[1]); qq[1] = pk_bf16(qv[2], qv[3]);
          *(LAS v2u*)(smem + L_QS + lane * 8) = qq;
          break;
        }
        __builtin_amdgcn_s_sleep(2);
      }
      lds_inc(smem, S_Q, lane);
    }
    v4u G1[11], WD[18], WO[16];
    const int hb = 2 * rb + (hw >> 1);
    const int rg0 = hb * 72 / 7, nrg = (hb + 1) * 72 / 7 - rg0;
    const bool has_s1 = bid < 252;
    unsigned short gwv[9];
    {
      const rsrc_t gr = mk_rsrc(mat1 ? wu : wg, has_s1 ? D * F * 2 : 0);
      const int go = (4 * rg0 + g) * (F * 2) + cblk * 512 + (hw & 1) * 256 + c * 16;
#pragma unroll
      for (int i = 0; i < 11; ++i) G1[i] = __builtin_amdgcn_raw_buffer_load_b128(gr, go, (i < 10 || nrg == 11) ? i * 4 * (F * 2) : OOB, AUX_W);
      const rsrc_t dr = mk_rsrc(wd, F * D * 2);
      const int wd_lo = (u0 + g) * 1152 + hw * 256 + c * 16;
      const int wd_hi = (hw == 0 && c < 8) ? wd_lo + 1024 : OOB;
#pragma unroll
      for (int i = 0; i < 9; ++i) WD[i] = __builtin_amdgcn_raw_buffer_load_b128(dr, wd_lo, i * 4608, AUX_W);
#pragma unroll
      for (int i = 9; i < 18; ++i) WD[i] = __builtin_amdgcn_raw_buffer_load_b128(dr, wd_hi, (i - 9) * 4608, AUX_W);
      const rsrc_t wor = mk_rsrc(wo, 4096 * D * 2);
      const int wo_lo = (kvh * 256 + 32 * sp + g) * 1152 + hw * 256 + c * 16;
      const int wo_hi = (hw == 0 && c < 8) ? wo_lo + 1024 : OOB;
#pragma unroll
      for (int i = 0; i < 8; ++i) WO[i] = __builtin_amdgcn_raw_buffer_load_b128(wor, wo_lo, i * 4608, AUX_W);
#pragma unroll
      for (int i = 8; i < 16; ++i) WO[i] = __builtin_amdgcn_raw_buffer_load_b128(wor, wo_hi, (i - 8) * 4608, AUX_W);
#pragma unroll
      for (int i = 0; i < 9; ++i) gwv[i] = gw[i * 64 + lane];
    }
    FENCE();
    {
      f32x4 ga = {0.f, 0.f, 0.f, 0.f};
#pragma unroll
      for (int i = 0; i < 11; ++i) {
        const int row = 4 * (rg0 + i) + g;
        const unsigned xv = *(const LAS unsigned*)(smem + L_XS + (row < D ? row : 0) * 4);
        ga = mfma(G1[i], diag(hi8 ? ((xv & 0xffff0000u) | (xv >> 16)) : ((xv << 16) | (xv & 0xffffu)), msk), ga);
      }
      if (hw < 2) {
        *(LAS f32x4*)(smem + L_GU + hw * 1024 + voff) = ga;
        lds_inc(smem, S_GU, lane);
      } else {
        lds_wait<2>(smem, S_GU, 2u);
        ga = *(const LAS f32x4*)(smem + L_GU + (hw & 1) * 1024 + voff) + ga;
        __builtin_amdgcn_raw_buffer_store_b128(__builtin_bit_cast(v4u, ga), wsr, has_s1 ? WS_G1 + (((cblk * 2 + (hw & 1)) * 2 + mat1) * 7 + rb) * 1024 + voff : OOB, 0, AUX_SC1);
        DRAIN();
        lds_inc(smem, S_G1, lane);
      }
    }
    float gt;
    {
      float z = 0.f;
#pragma unroll
      for (int i = 0; i < 9; ++i) {
        const unsigned xv = *(const LAS unsigned*)(smem + L_XS + (i * 64 + lane) * 4);
        z += rbf((b ? bfhi(xv) : bflo(xv)) * bflo(gwv[i]));
      }
      z += dpp<XOR1>(z); z += dpp<XOR2>(z); z += dpp<ROR4>(z); z += dpp<ROR8>(z);
      z = rbf(xrow_sum(z));
      gt = rbf(1.f / (1.f + __expf(-z)));
    }
    if (hw == 3) {
      lds_wait<4>(smem, S_G1, 2u);
      v2u fl; fl[0] = tag_lo; fl[1] = tag_hi;
      __builtin_amdgcn_raw_buffer_store_b64(fl, wsr, (lane == 0 && has_s1) ? WS_F1 + cblk * 128 + (mat1 * 7 + rb) * 8 : OOB, 0, AUX_SC1);
      __builtin_amdgcn_s_sleep(100);
      {
        const int fblk = lane < 32 ? (u0 >> 8) : ((u0 + 35) >> 8);
        const int fo = WS_F1 + fblk * 128 + ((lane & 31) < 14 ? (lane & 31) : 0) * 8;
        for (unsigned spins = 0;; ++spins) {
          const v2u p = __builtin_amdgcn_raw_buffer_load_b64(wsr, fo, 0, AUX_SC1);
          if (__all(((p[1] ^ tag_hi) | (p[0] ^ tag_lo)) == 0u) || spins > SPIN_CAP) break;
          __builtin_amdgcn_s_sleep(64);
        }
      }
      __builtin_amdgcn_fence(__ATOMIC_ACQUIRE, "wavefront");
      const int u = u0 + 4 * c + g;
      const int so = c < 9 ? WS_G1 + (u >> 7) * 14 * 1024 + (((u >> 5) & 3) * 16 + b * 8 + (u & 7)) * 16 + ((u >> 3) & 3) * 4 : OOB;
      float pv[14];
#pragma unroll
      for (int i = 0; i < 14; ++i) pv[i] = __uint_as_float(__builtin_amdgcn_raw_buffer_load_b32(wsr, so, i * 1024, AUX_SC1));
      const float gv = rbf(((pv[0] + pv[1]) + (pv[2] + pv[3])) + ((pv[4] + pv[5]) + pv[6]));
      const float uv = rbf(((pv[7] + pv[8]) + (pv[9] + pv[10])) + ((pv[11] + pv[12]) + pv[13]));
      const float inner = 0.7978845608028654f * (gv + 0.044715f * gv * gv * gv);
      const float hv = rbf(0.5f * gv * (1.f + tanhf(inner))) * uv;
      *(LAS unsigned*)(smem + L_HS + lane * 4) = pk_bf16(hv, hv);
      lds_inc(smem, S_H, lane);
    }
    f32x4 yf = {0.f, 0.f, 0.f, 0.f}, zf = {0.f, 0.f, 0.f, 0.f};
    {
      lds_wait<16>(smem, S_H, 1u);
      const unsigned hp = *(const LAS unsigned*)(smem + L_HS + lane * 4);
#define PRD(i) { const v4u bb = diag(dppu<BCAST + (i)>(hp), msk); yf = mfma(WD[i], bb, yf); zf = mfma(WD[9 + (i)], bb, zf); }
      PRD(0) PRD(1) PRD(2) PRD(3) PRD(4) PRD(5) PRD(6) PRD(7) PRD(8)
      const float og = rbf(1.f - gt);
      yf *= og; zf *= og;
      asm volatile("" : "+v"(yf), "+v"(zf));
    }
    FENCE();
    if (hw == 2) {
      const unsigned char* tp = ((lane & 8) ? Vc : Kc) + (size_t)grp * (SK * 256) + (size_t)(lane & 7) * (1 << 20) + NSU * 8192;
      if (lane == 16) tp = (const unsigned char*)out;
      unsigned tch = 0;
      if (lane < 17) tch = __builtin_nontemporal_load((const unsigned*)tp);
      asm volatile("" :: "v"(tch));
    }
    asm volatile("" :: "v"(cpf));
    if (hw == 3) {
      lds_wait<32>(smem, S_KDONE, 4u);
      v2u fl; fl[0] = tag_lo; fl[1] = tag_hi;
      __builtin_amdgcn_raw_buffer_store_b64(fl, wsr, lane == 0 ? WS_SF + bid * 64 : OOB, 0, AUX_SC1);
      for (unsigned spins = 0;; ++spins) {
        const v2u p = __builtin_amdgcn_raw_buffer_load_b64(wsr, WS_SF + (grp * 8 + (lane & 7)) * 64, 0, AUX_SC1);
        if (__all(((p[1] ^ tag_hi) | (p[0] ^ tag_lo)) == 0u) || spins > SPIN_CAP) break;
        __builtin_amdgcn_s_sleep(32);
      }
      lds_inc(smem, S_SHARED, lane);
      if (is_final) {
        for (unsigned spins = 0;; ++spins) {
          unsigned bad = 0;
#pragma unroll
          for (int i = 0; i < 2; ++i) {
            const v4u p = __builtin_amdgcn_raw_buffer_load_b128(wsr, WS_ST + i * 1024 + voff, 0, AUX_SC1);
            bad |= (p[1] ^ tag_hi) | (p[0] ^ tag_lo) | (p[3] ^ tag_hi) | (p[2] ^ tag_lo);
          }
          if (__all(bad == 0u) || spins > SPIN_CAP) break;
          __builtin_amdgcn_s_sleep(64);
        }
        __builtin_amdgcn_raw_buffer_store_b64(fl, wsr, (fid == 0 && lane == 0) ? WS_E : OOB, 0, AUX_SC1);
        lds_inc(smem, S_ALL, lane);
      }
    }
    FENCE();
    DRAIN();
    lds_wait<32>(smem, S_NEAR, 1u);
    lds_wait<0>(smem, S_O, 1u);
    const unsigned pp = *(const LAS unsigned*)(smem + L_OS + lane * 4);
    {
      f32x4 y0 = {0.f, 0.f, 0.f, 0.f}, y1 = {0.f, 0.f, 0.f, 0.f}, z0 = {0.f, 0.f, 0.f, 0.f};
#define PRJ(i, y) { const v4u bb = diag(dppu<BCAST + (i)>(pp), msk); y = mfma(WO[i], bb, y); z0 = mfma(WO[8 + (i)], bb, z0); }
      PRJ(0, y0) PRJ(1, y1) PRJ(2, y0) PRJ(3, y1) PRJ(4, y0) PRJ(5, y1) PRJ(6, y0) PRJ(7, y1)
      y0 = (y0 + y1) * gt + yf;
      z0 = z0 * gt + zf;
      const int dst = WS_H2 + ((b * 36 + hw * 8 + 2 * g) * 128 + (bid & 127)) * 64 + c * 4;
      __builtin_amdgcn_raw_buffer_store_b32(__float_as_uint(hi8 ? y0[1] : y0[0]) | 1u, wsr, dst, 0, AUX_SC1);
      __builtin_amdgcn_raw_buffer_store_b32(__float_as_uint(hi8 ? y0[3] : y0[2]) | 1u, wsr, dst, 8192, AUX_SC1);
      if (hw == 0) {
        const int dz = g < 2 ? dst + 32 * 8192 : OOB;
        __builtin_amdgcn_raw_buffer_store_b32(__float_as_uint(hi8 ? z0[1] : z0[0]) | 1u, wsr, dz, 0, AUX_SC1);
        __builtin_amdgcn_raw_buffer_store_b32(__float_as_uint(hi8 ? z0[3] : z0[2]) | 1u, wsr, dz, 8192, AUX_SC1);
      }
    }
  }
}

extern "C" void kernel_launch(void* const* d_in, const int* in_sizes, int n_in, void* d_out, int out_size, void* d_ws, size_t ws_size,
                              hipStream_t stream) {
  k_fused<<<256, NTHR, 0, stream>>>((const unsigned short*)d_in[0], (const unsigned char*)d_in[1], (const unsigned char*)d_in[2],
                                    (const unsigned char*)d_in[3], (const unsigned char*)d_in[4], (const unsigned char*)d_in[6],
                                    (const unsigned char*)d_in[7], (const unsigned char*)d_in[8], (const unsigned short*)d_in[9],
                                    (unsigned char*)d_ws, (unsigned*)d_out);
}
```

```cpp
#include <hip/hip_runtime.h>
#include <hip/hip_bf16.h>
#include <stdint.h>

typedef unsigned v4u __attribute__((ext_vector_type(4)));
typedef unsigned v2u __attribute__((ext_vector_type(2)));
typedef float f32x4 __attribute__((ext_vector_type(4)));
typedef float f32x2 __attribute__((ext_vector_type(2)));
typedef double f64x4 __attribute__((ext_vector_type(4)));
typedef short bf16x8 __attribute__((ext_vector_type(8)));
typedef __bf16 bf16x2 __attribute__((ext_vector_type(2)));
typedef __amdgpu_buffer_rsrc_t rsrc_t;
#define LAS __attribute__((address_space(3)))

constexpr int D = 576, SK = 32768, F = 4608;
constexpr int NTHR = 512;
constexpr unsigned NSU = 124;
constexpr unsigned ND = 8 * (128 - NSU);
constexpr unsigned NONE = 1024;
constexpr int NST = NSU / 4;
static_assert(NSU % 4 == 0 && NST % 2 == 1, "own steps per wave: odd");
constexpr int AUX_NT = 2, AUX_SC1 = 16;
constexpr int AUX_W = 0;
constexpr int OOB = 0x40000000;
constexpr unsigned SPIN_CAP = 200000u;
constexpr int PACE_T = 1756;
constexpr int PACE_CREDIT = 3500;
constexpr int CODE_BYTES = 25600;

constexpr int WS_E = 0;
constexpr int WS_CNT = 4096;
constexpr int WS_QG = 16384;
constexpr int WS_H1 = WS_QG + 131072;
constexpr int H1_STRIDE = 2176;
constexpr int WS_H2 = WS_H1 + 256 * H1_STRIDE;
constexpr int WS_ST = WS_H2 + 72 * 8192;
constexpr int WS_SF = WS_ST + 4096;
constexpr int WS_SG = WS_SF + 256 * 64;
constexpr int WS_F1 = WS_SG + 32 * (int)ND * 128;
constexpr int WS_G1 = WS_F1 + 4096;
constexpr int WS_END = WS_G1 + 36 * 14 * 1024;

constexpr int L_XS = 0;
constexpr int L_QP = 2304;
constexpr int L_QS = L_QP + 1024;
constexpr int L_MRG = L_QS + 512;
constexpr int L_ML = L_MRG + 8192;
constexpr int L_OS = L_ML + 96;
constexpr int L_GU = L_OS + 256;
constexpr int L_HS = L_GU + 2048;
constexpr int L_FB = L_HS + 256;
constexpr int L_SYNC = L_FB + 256;
constexpr int L_SC = L_SYNC + 48;
constexpr int L_SB = L_SC + 16384;
constexpr int L_END = L_SB + 4096;
constexpr int LDS_BYTES = 96 * 1024;
static_assert(L_END <= LDS_BYTES, "lds");
enum { S_Q = 0, S_G1 = 1, S_NEAR = 2, S_MRG = 3, S_O = 4, S_QP = 5, S_ALL = 6, S_KDONE = 7, S_FIN = 8, S_SHARED = 9, S_GU = 10, S_H = 11 };

__device__ __forceinline__ float bflo(unsigned u) { return __builtin_bit_cast(float, u << 16); }
__device__ __forceinline__ float bfhi(unsigned u) { return __builtin_bit_cast(float, u & 0xffff0000u); }
__device__ __forceinline__ unsigned pk_bf16(float lo, float hi) {
  bf16x2 v; v[0] = (__bf16)lo; v[1] = (__bf16)hi; return __builtin_bit_cast(unsigned, v);
}
__device__ __forceinline__ float rbf(float f) { return bflo(pk_bf16(f, f)); }
__device__ __forceinline__ float dot2(unsigned a, unsigned b, float c) {
  return __builtin_amdgcn_fdot2_f32_bf16(__builtin_bit_cast(bf16x2, a), __builtin_bit_cast(bf16x2, b), c, false);
}
constexpr int XOR1 = 0xB1, XOR2 = 0x4E, HMIR = 0x141, ROR4 = 0x124, ROR8 = 0x128, BCAST = 0x150;
template <int CTRL> __device__ __forceinline__ unsigned dppu(unsigned x) { return (unsigned)__builtin_amdgcn_mov_dpp((int)x, CTRL, 0xf, 0xf, true); }
template <int CTRL> __device__ __forceinline__ float dpp(float x) { return __uint_as_float(dppu<CTRL>(__float_as_uint(x))); }
template <int CTRL> __device__ __forceinline__ double dppd(double x) {
  const v2u u = __builtin_bit_cast(v2u, x);
  v2u r; r[0] = dppu<CTRL>(u[0]); r[1] = dppu<CTRL>(u[1]);
  return __builtin_bit_cast(double, r);
}
__device__ __forceinline__ f32x4 mfma(v4u a, v4u b, f32x4 c) {
  return __builtin_amdgcn_mfma_f32_16x16x32_bf16(__builtin_bit_cast(bf16x8, a), __builtin_bit_cast(bf16x8, b), c, 0, 0, 0);
}
__device__ __forceinline__ float swap16_sum(float a, float b) {
  auto s = __builtin_amdgcn_permlane16_swap(__float_as_uint(a), __float_as_uint(b), false, false);
  return __uint_as_float(s[0]) + __uint_as_float(s[1]);
}
__device__ __forceinline__ float swap32_sum(float a, float b) {
  auto s = __builtin_amdgcn_permlane32_swap(__float_as_uint(a), __float_as_uint(b), false, false);
  return __uint_as_float(s[0]) + __uint_as_float(s[1]);
}
__device__ __forceinline__ float xrow_max(float x) {
  auto s = __builtin_amdgcn_permlane16_swap(__float_as_uint(x), __float_as_uint(x), false, false);
  x = fmaxf(__uint_as_float(s[0]), __uint_as_float(s[1]));
  auto t = __builtin_amdgcn_permlane32_swap(__float_as_uint(x), __float_as_uint(x), false, false);
  return fmaxf(__uint_as_float(t[0]), __uint_as_float(t[1]));
}
__device__ __forceinline__ float xrow_sum(float x) { x = swap16_sum(x, x); return swap32_sum(x, x); }
__device__ __forceinline__ double xrow_sumd(double x) {
  v2u u = __builtin_bit_cast(v2u, x), a, b2;
  { auto s0 = __builtin_amdgcn_permlane16_swap(u[0], u[0], false, false); auto s1 = __builtin_amdgcn_permlane16_swap(u[1], u[1], false, false);
    a[0] = s0[0]; a[1] = s1[0]; b2[0] = s0[1]; b2[1] = s1[1]; }
  x = __builtin_bit_cast(double, a) + __builtin_bit_cast(double, b2);
  u = __builtin_bit_cast(v2u, x);
  { auto s0 = __builtin_amdgcn_permlane32_swap(u[0], u[0], false, false); auto s1 = __builtin_amdgcn_permlane32_swap(u[1], u[1], false, false);
    a[0] = s0[0]; a[1] = s1[0]; b2[0] = s0[1]; b2[1] = s1[1]; }
  return __builtin_bit_cast(double, a) + __builtin_bit_cast(double, b2);
}
__device__ __forceinline__ rsrc_t mk_rsrc(const void* p, int bytes) { return __builtin_amdgcn_make_buffer_rsrc((void*)p, 0, bytes, 0x00020000); }
#define DRAIN() do { __builtin_amdgcn_s_waitcnt(0x0F70); asm volatile("s_waitcnt vmcnt(0)" ::: "memory"); } while (0)
#define LDSW() asm volatile("s_waitcnt lgkmcnt(0)" ::: "memory")
#define FENCE() __builtin_amdgcn_sched_barrier(0)

constexpr float SCALE = 0.08838834764831845f, L2E = 1.4426950408889634f;

struct St { float m; double l; f64x4 acc; };
__device__ __forceinline__ void pin(St& st) { asm volatile("" : "+v"(st.m), "+v"(st.l), "+v"(st.acc) :: "memory"); }
__device__ __forceinline__ v4u diag(unsigned pp, const v4u& msk) {
  v4u bb; bb[0] = pp & msk[0]; bb[1] = pp & msk[1]; bb[2] = pp & msk[2]; bb[3] = pp & msk[3];
  return bb;
}

__device__ __forceinline__ v2u scores(const v4u* KB, const v4u& q0, const v4u& q1, bool hi8) {
  float s[4];
#pragma unroll
  for (int t = 0; t < 4; ++t) {
    float a0 = dot2(KB[t][0], q0[0], 0.f), a1 = dot2(KB[t][0], q1[0], 0.f);
#pragma unroll
    for (int k = 1; k < 4; ++k) { a0 = dot2(KB[t][k], q0[k], a0); a1 = dot2(KB[t][k], q1[k], a1); }
    const float keep = hi8 ? a1 : a0, send = hi8 ? a0 : a1;
    float v = keep + dpp<ROR8>(send);
    v += dpp<HMIR>(v); v += dpp<XOR2>(v); v += dpp<XOR1>(v);
    s[t] = v;
  }
  const unsigned u0 = pk_bf16(s[0], s[1]), u1 = pk_bf16(s[2], s[3]);
  v2u r; r[0] = pk_bf16(bflo(u0) * SCALE, bfhi(u0) * SCALE); r[1] = pk_bf16(bflo(u1) * SCALE, bfhi(u1) * SCALE);
  return r;
}
__device__ __forceinline__ void pv_unit(const v4u (&VB)[8], const v4u sc, const v4u& msk, St& st) {
  float s[8];
#pragma unroll
  for (int t = 0; t < 4; ++t) { s[2 * t] = bflo(sc[t]); s[2 * t + 1] = bfhi(sc[t]); }
  float tm = fmaxf(fmaxf(fmaxf(s[0], s[1]), fmaxf(s[2], s[3])), fmaxf(fmaxf(s[4], s[5]), fmaxf(s[6], s[7])));
  tm = xrow_max(tm);
  const float tl = tm * L2E, mt = __builtin_ceilf(tl), fr = tl - mt;
  const float mn = fmaxf(st.m, mt);
  const double alpha = (double)__builtin_amdgcn_exp2f(st.m - mn);
  const float beta = __builtin_amdgcn_exp2f(mt - mn);
  st.m = mn;
  float p[8];
#pragma unroll
  for (int t = 0; t < 8; ++t) p[t] = __builtin_amdgcn_exp2f(fmaf(s[t] - tm, L2E, fr));
  f32x4 ta = {0.f, 0.f, 0.f, 0.f};
#pragma unroll
  for (int t = 0; t < 8; ++t) ta = mfma(VB[t], diag(pk_bf16(p[t], p[t]), msk), ta);
  st.l = __builtin_fma(st.l, alpha, (double)((((p[0] + p[1]) + (p[2] + p[3])) + ((p[4] + p[5]) + (p[6] + p[7]))) * beta));
#pragma unroll
  for (int r = 0; r < 4; ++r) st.acc[r] = __builtin_fma(st.acc[r], alpha, (double)(ta[r] * beta));
}
__device__ __forceinline__ void issue(v4u (&B)[8], rsrc_t r, int voff, unsigned unit) {
  const int so = (int)(unit * 8192u);
#pragma unroll
  for (int t = 0; t < 4; ++t) B[t] = __builtin_amdgcn_raw_buffer_load_b128(r, voff + t * 1024, so, AUX_NT);
#pragma unroll
  for (int t = 0; t < 4; ++t) B[4 + t] = __builtin_amdgcn_raw_buffer_load_b128(r, voff + t * 1024, so + 4096, AUX_NT);
  FENCE();
}
__device__ __forceinline__ unsigned atomic_inc(rsrc_t r, int voff0, bool on, int so) {
  return (unsigned)__builtin_amdgcn_raw_ptr_buffer_atomic_add_i32(1, r, voff0, on ? so : OOB, 0);
}
__device__ __forceinline__ unsigned lds_inc(unsigned char* smem, int idx, int lane) {
  LDSW();
  unsigned old = 0;
  if (lane == 0) old = __hip_atomic_fetch_add((LAS unsigned*)(LAS unsigned char*)smem + (L_SYNC / 4 + idx), 1u, __ATOMIC_RELAXED, __HIP_MEMORY_SCOPE_WORKGROUP);
  return old;
}
template <int SLEEP> __device__ __forceinline__ void lds_wait(unsigned char* smem, int idx, unsigned want) {
#pragma unroll 1
  for (unsigned spins = 0; spins < 8000000u / (1 + SLEEP); ++spins) {
    const unsigned v = __builtin_amdgcn_readfirstlane(*((volatile LAS unsigned*)(LAS unsigned char*)smem + (L_SYNC / 4 + idx)));
    if (v >= want) break;
    if (SLEEP) __builtin_amdgcn_s_sleep(SLEEP);
  }
  asm volatile("" ::: "memory");
}
__device__ __forceinline__ float val(unsigned u) { return __uint_as_float(u & ~1u); }

__global__ void __launch_bounds__(NTHR, 2)
k_fused(const unsigned short* __restrict__ x, const unsigned char* Kc, const unsigned char* Vc,
        const unsigned char* __restrict__ wq, const unsigned char* wo, const unsigned char* wg,
        const unsigned char* wu, const unsigned char* wd, const unsigned short* __restrict__ gw,
        unsigned char* ws, unsigned* out) {
  __shared__ __attribute__((aligned(16))) unsigned char smem[LDS_BYTES];
  const int tid = threadIdx.x, lane = tid & 63, w = __builtin_amdgcn_readfirstlane(tid >> 6);
  const int bid = blockIdx.x, grp = bid >> 3, sp = bid & 7, b = grp >> 4, kvh = grp & 15;
  const int g = lane >> 4, c = lane & 15;
  const int voff = lane * 16;
  const int ridx = (grp + 4 * sp) & 31;
  const bool is_final = ridx < 9;
  const int fid = sp * 9 + ridx;
  const bool hi8 = (c & 8) != 0;
  const int cm = bid % 36, rb = bid / 36, mat1 = cm >= 18 ? 1 : 0, cblk = cm - 18 * mat1;
  const int u0 = 36 * (bid & 127);
#define MAKE_MSK() v4u msk; _Pragma("unroll") for (int d = 0; d < 4; ++d) msk[d] = (((c & 7) >> 1) == d) ? ((c & 1) ? 0xffff0000u : 0x0000ffffu) : 0u;

  if (w >= 4) {
    const int sw = w - 4, stid = tid - 256;
    const rsrc_t kr = mk_rsrc(Kc + (size_t)grp * (SK * 256), SK * 256);
    const rsrc_t vr = mk_rsrc(Vc + (size_t)grp * (SK * 256), SK * 256);
    const rsrc_t wsr = mk_rsrc(ws, WS_END);
    const unsigned ubase = (unsigned)(sp * 128 + sw);
    v4u Ba[8], Bb[8], Be0[8], Be1[8];
    issue(Be0, kr, voff, ubase);
    asm volatile("" ::: "memory");
    __builtin_amdgcn_s_barrier();
    asm volatile("" ::: "memory");
    FENCE();
    if (sw == 0) {
      const unsigned char* tp = Vc + (size_t)bid * (1 << 20);
      if (lane == 1) tp = (mat1 ? wu : wg) + (size_t)(rb < 7 ? (2 * rb) * 72 / 7 * 4 : 0) * (F * 2);
      if (lane == 2) tp = (mat1 ? wu : wg) + (size_t)(rb < 7 ? (2 * rb + 2) * 72 / 7 * 4 - 1 : 0) * (F * 2);
      if (lane == 3) tp = wd + (size_t)u0 * 1152;
      if (lane == 4) tp = wo + (size_t)(kvh * 256 + 32 * sp) * 1152;
      if (lane == 5) tp = (const unsigned char*)gw;
      unsigned tch = 0;
      if (lane < 6) tch = __builtin_nontemporal_load((const unsigned*)tp);
      asm volatile("" :: "v"(tch));
    }
    MAKE_MSK();
    DRAIN(); issue(Be1, kr, voff, ubase + 4);
    DRAIN(); issue(Ba, kr, voff, ubase + 8);
    DRAIN(); issue(Bb, kr, voff, ubase + 12);
    lds_wait<1>(smem, S_Q, 1u);
    const v4u q0 = *(const LAS v4u*)(smem + L_QS + c * 16), q1 = *(const LAS v4u*)(smem + L_QS + 256 + c * 16);
    const int sc_off = ((c >> 3) * 4 + g) * 16;
#define KSC(B) const v2u s0_ = scores(&B[0], q0, q1, hi8), s1_ = scores(&B[4], q0, q1, hi8);                       \
      v4u sc_; sc_[0] = s0_[0]; sc_[1] = s0_[1]; sc_[2] = s1_[0]; sc_[3] = s1_[1];
#define KSTEP(B, i)                                                                                                \
    {                                                                                                              \
      KSC(B)                                                                                                       \
      if ((c & 7) == 0) *(LAS v4u*)(smem + L_SC + (4 * (i) + sw) * 128 + sc_off) = sc_;                            \
      asm volatile("" ::: "memory");                                                                               \
      FENCE();                                                                                                     \
    }
    int nsl = 0;
    unsigned did = 0;
#define SFLUSH()                                                                                                   \
    {                                                                                                              \
      const v4u v_ = *(const LAS v4u*)(smem + L_SB + sw * 1024 + voff);                                            \
      __builtin_amdgcn_raw_buffer_store_b128(v_, wsr, (lane >> 3) < nsl ? WS_SG + (int)((grp * ND + did) * 128u) + (lane & 7) * 16 : OOB, 0, AUX_SC1); \
      nsl = 0;                                                                                                     \
    }
#define KSTEPG(B, dd)                                                                                              \
    {                                                                                                              \
      KSC(B)                                                                                                       \
      if ((c & 7) == 0) *(LAS v4u*)(smem + L_SB + sw * 1024 + nsl * 128 + sc_off) = sc_;                           \
      did = (lane >> 3) == nsl ? (dd) : did;                                                                       \
      ++nsl;                                                                                                       \
      if (nsl == 8) SFLUSH()                                                                                       \
      asm volatile("" ::: "memory");                                                                               \
      FENCE();                                                                                                     \
    }
#define PACE() do { int lag_; while ((lag_ = (int)((unsigned)__builtin_amdgcn_s_memrealtime() * 16u - due)) < 0) __builtin_amdgcn_s_sleep(1); \
                    if (lag_ > PACE_CREDIT) due += (unsigned)(lag_ - PACE_CREDIT); due += PACE_T; } while (0)
#define MAPD(d) (((d) & 7u) * 128u + NSU + ((d) >> 3))
    const int cnt0 = lane == 0 ? WS_CNT + grp * 256 : OOB;
    unsigned a = 0u;
    KSTEP(Ba, 2);
    issue(Ba, kr, voff, ubase + 16);
    KSTEP(Be0, 0);
    KSTEP(Be1, 1);
    KSTEP(Bb, 3);
    unsigned due = (unsigned)__builtin_amdgcn_s_memrealtime() * 16u;
#pragma unroll 1
    for (int i = 4; i < NST - 1; i += 2) {
      PACE();
      DRAIN();
      issue(Bb, kr, voff, ubase + 4 * (i + 1));
      KSTEP(Ba, i);
      PACE();
      DRAIN();
      a = atomic_inc(wsr, cnt0, i + 3 >= NST, 0);
      issue(Ba, kr, voff, ubase + 4 * (i + 2));
      KSTEP(Bb, i + 1);
    }
    {
      unsigned da, db;
      DRAIN();
      db = __builtin_amdgcn_readfirstlane(a);
      a = atomic_inc(wsr, cnt0, db < ND, 0);
      issue(Bb, db < ND ? kr : vr, voff, db < ND ? MAPD(db) : ubase);
      KSTEP(Ba, NST - 1);
      bool in_b = true;
      while (db < ND) {
        DRAIN();
        da = __builtin_amdgcn_readfirstlane(a);
        a = atomic_inc(wsr, cnt0, da < ND, 0);
        issue(Ba, da < ND ? kr : vr, voff, da < ND ? MAPD(da) : ubase);
        KSTEPG(Bb, db);
        if (da >= ND) { in_b = false; break; }
        DRAIN();
        db = __builtin_amdgcn_readfirstlane(a);
        a = atomic_inc(wsr, cnt0, db < ND, 0);
        issue(Bb, db < ND ? kr : vr, voff, db < ND ? MAPD(db) : ubase);
        KSTEPG(Ba, da);
      }
      SFLUSH()
      DRAIN();
      if (in_b) {
#pragma unroll
        for (int t = 0; t < 8; ++t) Ba[t] = Bb[t];
      }
    }
    lds_inc(smem, S_KDONE, lane);
    St st; st.m = -INFINITY; st.l = 0.0; st.acc = f64x4{0.0, 0.0, 0.0, 0.0};
#define LSC(i) (*(const LAS v4u*)(smem + L_SC + (4 * (i) + sw) * 128 + sc_off))
    due = (unsigned)__builtin_amdgcn_s_memrealtime() * 16u;
#pragma unroll 1
    for (int i = 0; i < NST - 1; i += 2) {
      PACE();
      DRAIN();
      issue(Bb, vr, voff, ubase + 4 * (i + 1));
      pv_unit(Ba, LSC(i), msk, st); pin(st);
      FENCE();
      PACE();
      DRAIN();
      a = atomic_inc(wsr, cnt0, i + 3 >= NST, 4);
      issue(Ba, vr, voff, ubase + 4 * (i + 2));
      pv_unit(Bb, LSC(i + 1), msk, st); pin(st);
      FENCE();
    }
    lds_wait<2>(smem, S_SHARED, 1u);
    lds_inc(smem, S_NEAR, lane);
    v4u sa = LSC(NST - 1), sb;
    const int sg_ld = WS_SG + grp * (int)ND * 128 + sc_off;
    while (true) {
      DRAIN();
      unsigned d = __builtin_amdgcn_readfirstlane(a);
      a = atomic_inc(wsr, cnt0, d < ND, 4);
      sb = __builtin_amdgcn_raw_buffer_load_b128(wsr, sg_ld, d < ND ? (int)(d * 128u) : OOB, AUX_SC1);
      issue(Bb, vr, voff, d < ND ? MAPD(d) : NONE);
      pv_unit(Ba, sa, msk, st); pin(st);
      FENCE();
      if (d >= ND) break;
      DRAIN();
      d = __builtin_amdgcn_readfirstlane(a);
      a = atomic_inc(wsr, cnt0, d < ND, 4);
      sa = __builtin_amdgcn_raw_buffer_load_b128(wsr, sg_ld, d < ND ? (int)(d * 128u) : OOB, AUX_SC1);
      issue(Ba, vr, voff, d < ND ? MAPD(d) : NONE);
      pv_unit(Bb, sb, msk, st); pin(st);
      FENCE();
      if (d >= ND) break;
    }
    {
      const double l = xrow_sumd(st.l);
      const int h = c >> 3;
#pragma unroll
      for (int r = 0; r < 4; ++r) {
        const int dim = 8 * (4 * g + r) + (c & 7);
        *(LAS double*)(smem + L_MRG + (sw * 256 + h * 128 + (dim & 96) + (dim & 3) * 8 + ((dim >> 2) & 7)) * 8) = st.acc[r];
      }
      if (g == 0 && (c & 7) == 0) { *(LAS float*)(smem + L_ML + (h * 4 + sw) * 4) = st.m; *(LAS double*)(smem + L_ML + 32 + (h * 4 + sw) * 8) = l; }
    }
    lds_inc(smem, S_MRG, lane);
    lds_wait<0>(smem, S_MRG, 4u);
    {
      const int hh = sw >> 1;
      const f32x4 m4 = *(const LAS f32x4*)(smem + L_ML + hh * 16);
      const f64x4 l4 = *(const LAS f64x4*)(smem + L_ML + 32 + hh * 32);
      double a4[4];
#pragma unroll
      for (int ww = 0; ww < 4; ++ww) a4[ww] = *(const LAS double*)(smem + L_MRG + (ww * 256 + stid) * 8);
      const float M = fmaxf(fmaxf(m4[0], m4[1]), fmaxf(m4[2], m4[3]));
      double L = 0.0, o = 0.0;
#pragma unroll
      for (int ww = 0; ww < 4; ++ww) {
        const double wt = (double)__builtin_amdgcn_exp2f(m4[ww] - M);
        L = __builtin_fma(wt, l4[ww], L); o = __builtin_fma(wt, a4[ww], o);
      }
      v2u ov = __builtin_bit_cast(v2u, o); ov[0] |= 1u;
      __builtin_amdgcn_raw_buffer_store_b64(ov, wsr, WS_H1 + bid * H1_STRIDE + stid * 8, 0, AUX_SC1);
      if ((stid & 127) < 2) {
        v2u ml = __builtin_bit_cast(v2u, L); ml[0] |= 1u;
        if (stid & 1) { ml[0] = 1u; ml[1] = __float_as_uint(M); }
        __builtin_amdgcn_raw_buffer_store_b64(ml, wsr, WS_H1 + bid * H1_STRIDE + 2048 + hh * 16 + (stid & 1) * 8, 0, AUX_SC1);
      }
    }
    for (int i = 0; i < sw; ++i) __builtin_amdgcn_s_sleep(4);
    {
      const int o_acc = WS_H1 + (grp * 8 + 4 * (c >> 3)) * H1_STRIDE + (32 * sp + 8 * g + (c & 7)) * 8;
      const int o_ml = WS_H1 + (grp * 8 + (c & 7)) * H1_STRIDE + 2048 + (sp >> 2) * 16;
      v2u pv[4];
      v4u pm = {0u, 0u, 0u, 0u};
#pragma unroll
      for (int i = 0; i < 4; ++i) pv[i] = v2u{0u, 0u};
      bool mine = true;
      for (unsigned spins = 0;; ++spins) {
        if (!(pv[0][0] & pv[1][0] & pv[2][0] & pv[3][0] & 1u)) {
#pragma unroll
          for (int i = 0; i < 4; ++i) pv[i] = __builtin_amdgcn_raw_buffer_load_b64(wsr, o_acc, i * H1_STRIDE, AUX_SC1);
        }
        if (!(pm[0] & pm[2] & 1u)) pm = __builtin_amdgcn_raw_buffer_load_b128(wsr, o_ml, 0, AUX_SC1);
        if (__all((pv[0][0] & pv[1][0] & pv[2][0] & pv[3][0] & pm[0] & pm[2] & 1u) != 0u) || spins > SPIN_CAP) break;
        if (__builtin_amdgcn_readfirstlane(*((volatile LAS unsigned*)(smem + L_SYNC) + S_O)) != 0u) { mine = false; break; }
        __builtin_amdgcn_s_sleep(2);
      }
      if (mine) {
        const float Ms = __uint_as_float(pm[3]);
        float Mx = fmaxf(Ms, dpp<XOR1>(Ms)); Mx = fmaxf(Mx, dpp<XOR2>(Mx)); Mx = fmaxf(Mx, dpp<HMIR>(Mx));
        const float wt = __builtin_amdgcn_exp2f(Ms - Mx);
        v2u lv; lv[0] = pm[0]; lv[1] = pm[1];
        double den = (double)wt * __builtin_bit_cast(double, lv);
        den += dppd<XOR1>(den); den += dppd<XOR2>(den); den += dppd<HMIR>(den);
        double num = 0.0;
#define ACC1(i) { const float wl_ = dpp<BCAST + (i)>(wt), wh_ = dpp<BCAST + 4 + (i)>(wt); num = __builtin_fma((double)(hi8 ? wh_ : wl_), __builtin_bit_cast(double, pv[i]), num); }
        ACC1(0) ACC1(1) ACC1(2) ACC1(3)
        num += dppd<ROR8>(num);
        const float ov = (float)num / (float)den;
        *(LAS unsigned*)(smem + L_OS + lane * 4) = pk_bf16(ov, ov);
        lds_inc(smem, S_O, lane);
      }
    }
    if (is_final) {
      const int src = WS_H2 + fid * 8192 + sw * 2048 + voff;
      lds_wait<8>(smem, S_ALL, 1u);
      v4u p0, p1;
      for (unsigned spins = 0;; ++spins) {
        p0 = __builtin_amdgcn_raw_buffer_load_b128(wsr, src, 0, AUX_SC1);
        p1 = __builtin_amdgcn_raw_buffer_load_b128(wsr, src, 1024, AUX_SC1);
        const unsigned m = (p0[0] & p0[1] & p0[2] & p0[3]) & (p1[0] & p1[1] & p1[2] & p1[3]) & 1u;
        if (__all(m != 0u) || spins > SPIN_CAP) break;
        __builtin_amdgcn_s_sleep(2);
      }
      f32x4 t4;
#pragma unroll
      for (int j = 0; j < 4; ++j) {
        float t = val(p0[j]) + val(p1[j]);
        t += dpp<ROR4>(t); t += dpp<ROR8>(t);
        t4[j] = xrow_sum(t);
      }
      if (lane < 4) *(LAS f32x4*)(smem + L_FB + (sw * 4 + lane) * 16) = t4;
      const unsigned arrived = __builtin_amdgcn_readfirstlane(lds_inc(smem, S_FIN, lane));
      asm volatile("" ::: "memory");
      if (arrived == 3u && lane < 8) {
        f32x2 t = *(const LAS f32x2*)(smem + L_FB + lane * 8);
#pragma unroll
        for (int ww = 1; ww < 4; ++ww) t += *(const LAS f32x2*)(smem + L_FB + ww * 64 + lane * 8);
        out[fid * 8 + lane] = pk_bf16(t[0], t[1]);
      }
    }
  } else {
    const int hw = w;
    const v4u cpf = __builtin_amdgcn_raw_buffer_load_b128(mk_rsrc((const void*)(__builtin_amdgcn_s_getpc() & ~255ull), CODE_BYTES),
                                                          hw == 0 ? grp * 1024 + voff : OOB, 0, 0);
    unsigned short xq[9];
    v4u wqv[9];
    {
      const int r = lane >> 3, row0 = 288 * (sp & 1) + 72 * hw;
      const rsrc_t qr = mk_rsrc(wq, D * 8192);
      const int base = (row0 + r) * 8192 + (kvh * 256 + (sp >> 1) * 64) * 2 + (lane & 7) * 16;
#pragma unroll
      for (int i = 0; i < 9; ++i) wqv[i] = __builtin_amdgcn_raw_buffer_load_b128(qr, base, i * 8 * 8192, AUX_W);
#pragma unroll
      for (int i = 0; i < 9; ++i) xq[i] = x[b * D + row0 + 8 * i + r];
    }
    const int xi = tid < 144 ? tid : 143;
    const v2u xa = ((const v2u*)x)[xi], xb = ((const v2u*)x)[144 + xi];
    if (tid < 3) { const v4u z = {0u, 0u, 0u, 0u}; *(LAS v4u*)(smem + L_SYNC + tid * 16) = z; }
    FENCE();
    LDSW();
    __builtin_amdgcn_s_barrier();
    asm volatile("" ::: "memory");
    const rsrc_t wsr = mk_rsrc(ws, WS_END);
    v2u evv = {0u, 0u};
    if (hw == 3) {
      evv = __builtin_amdgcn_raw_buffer_load_b64(wsr, WS_E, 0, AUX_SC1);
      const v4u z = {0u, 0u, 0u, 0u};
      __builtin_amdgcn_raw_buffer_store_b128(z, wsr, WS_H1 + bid * H1_STRIDE + voff, 0, AUX_SC1);
      __builtin_amdgcn_raw_buffer_store_b128(z, wsr, WS_H1 + bid * H1_STRIDE + 1024 + voff, 0, AUX_SC1);
      const int h2o = WS_H2 + (b * 36 * 128 + (bid & 127)) * 64 + (lane >> 2) * 8192 + (lane & 3) * 16;
      __builtin_amdgcn_raw_buffer_store_b128(z, wsr, h2o, 0, AUX_SC1);
      __builtin_amdgcn_raw_buffer_store_b128(z, wsr, h2o, 16 * 8192, AUX_SC1);
      int off = h2o + 32 * 8192;
      if (lane >= 16) off = WS_H1 + bid * H1_STRIDE + 2048 + (lane & 1) * 16;
      if (lane >= 32) off = sp == 0 ? WS_CNT + grp * 256 : OOB;
      __builtin_amdgcn_raw_buffer_store_b128(z, wsr, off, 0, AUX_SC1);
    }
    FENCE();
    {
      float acc[8];
#pragma unroll
      for (int j = 0; j < 8; ++j) acc[j] = 0.f;
#pragma unroll
      for (int i = 0; i < 9; ++i) {
        const float xv = bflo(xq[i]);
#pragma unroll
        for (int k = 0; k < 4; ++k) { acc[2 * k] = fmaf(xv, bflo(wqv[i][k]), acc[2 * k]); acc[2 * k + 1] = fmaf(xv, bfhi(wqv[i][k]), acc[2 * k + 1]); }
      }
      float a4[4];
#pragma unroll
      for (int j = 0; j < 4; ++j) a4[j] = swap32_sum(acc[j], acc[j + 4]);
      f32x2 r2; r2[0] = swap16_sum(a4[0], a4[2]); r2[1] = swap16_sum(a4[1], a4[3]);
      r2[0] += dpp<ROR8>(r2[0]); r2[1] += dpp<ROR8>(r2[1]);
      if ((lane & 8) == 0) *(LAS f32x2*)(smem + L_QP + (hw * 64 + (lane & 7) * 8 + 4 * (lane >> 5) + 2 * ((lane >> 4) & 1)) * 4) = r2;
      if (tid < 144) {
        v4u xs4;
        xs4[0] = (xa[0] & 0xffffu) | (xb[0] << 16); xs4[1] = (xa[0] >> 16) | (xb[0] & 0xffff0000u);
        xs4[2] = (xa[1] & 0xffffu) | (xb[1] << 16); xs4[3] = (xa[1] >> 16) | (xb[1] & 0xffff0000u);
        *(LAS v4u*)(smem + L_XS + tid * 16) = xs4;
      }
      lds_inc(smem, S_QP, lane);
      lds_wait<0>(smem, S_QP, 4u);
    }
    MAKE_MSK();
    unsigned tag_lo = 0, tag_hi = 0;
    if (hw == 3) {
      const unsigned long long ev = ((unsigned long long)(unsigned)__builtin_amdgcn_readfirstlane(evv[1]) << 32) | (unsigned)__builtin_amdgcn_readfirstlane(evv[0]);
      const unsigned long long tag64 = ev * 6364136223846793005ull + 1442695040888963407ull;
      tag_lo = (unsigned)tag64; tag_hi = (unsigned)(tag64 >> 32);
      const unsigned tag8 = tag_lo >> 24;
      {
        float s = 0.f;
#pragma unroll
        for (int ww = 0; ww < 4; ++ww) s += *(const LAS float*)(smem + L_QP + (ww * 64 + lane) * 4);
        DRAIN();
        v2u gr; gr[0] = ((__float_as_uint(s) + 0x80u) & 0xffffff00u) | tag8; gr[1] = tag_hi;
        __builtin_amdgcn_raw_buffer_store_b64(gr, wsr, WS_QG + ((grp * 2 + (sp & 1)) * 256 + (sp >> 1) * 64 + lane) * 8, 0, AUX_SC1);
        v2u fl; fl[0] = tag_lo; fl[1] = tag_hi;
        __builtin_amdgcn_raw_buffer_store_b64(fl, wsr, lane == 0 ? WS_ST + bid * 8 : OOB, 0, AUX_SC1);
      }
      for (unsigned spins = 0;; ++spins) {
        v4u p[4];
#pragma unroll
        for (int i = 0; i < 4; ++i) p[i] = __builtin_amdgcn_raw_buffer_load_b128(wsr, WS_QG + grp * 4096 + lane * 32 + (i & 1) * 16 + (i >> 1) * 2048, 0, AUX_SC1);
        unsigned bad = 0;
#pragma unroll
        for (int i = 0; i < 4; ++i) bad |= (p[i][1] ^ tag_hi) | (p[i][3] ^ tag_hi) | ((p[i][0] ^ tag8) & 0xffu) | ((p[i][2] ^ tag8) & 0xffu);
        if (__all(bad == 0u) || spins > SPIN_CAP) {
          float qv[4];
#pragma unroll
          for (int i = 0; i < 4; ++i)
            qv[i] = __uint_as_float(p[i >> 1][(i & 1) * 2] & 0xffffff00u) + __uint_as_float(p[2 + (i >> 1)][(i & 1) * 2] & 0xffffff00u);
          v2u qq; qq[0] = pk_bf16(qv[0], qv[1]); qq[1] = pk_bf16(qv[2], qv[3]);
          *(LAS v2u*)(smem + L_QS + lane * 8) = qq;
          break;
        }
        __builtin_amdgcn_s_sleep(2);
      }
      lds_inc(smem, S_Q, lane);
    }
    v4u G1[11], WD[18], WO[16];
    const int hb = 2 * rb + (hw >> 1);
    const int rg0 = hb * 72 / 7, nrg = (hb + 1) * 72 / 7 - rg0;
    const bool has_s1 = bid < 252;
    unsigned short gwv[9];
    {
      const rsrc_t gr = mk_rsrc(mat1 ? wu : wg, has_s1 ? D * F * 2 : 0);
      const int go = (4 * rg0 + g) * (F * 2) + cblk * 512 + (hw & 1) * 256 + c * 16;
#pragma unroll
      for (int i = 0; i < 11; ++i) G1[i] = __builtin_amdgcn_raw_buffer_load_b128(gr, go, (i < 10 || nrg == 11) ? i * 4 * (F * 2) : OOB, AUX_W);
      const rsrc_t dr = mk_rsrc(wd, F * D * 2);
      const int wd_lo = (u0 + g) * 1152 + hw * 256 + c * 16;
      const int wd_hi = (hw == 0 && c < 8) ? wd_lo + 1024 : OOB;
#pragma unroll
      for (int i = 0; i < 9; ++i) WD[i] = __builtin_amdgcn_raw_buffer_load_b128(dr, wd_lo, i * 4608, AUX_W);
#pragma unroll
      for (int i = 9; i < 18; ++i) WD[i] = __builtin_amdgcn_raw_buffer_load_b128(dr, wd_hi, (i - 9) * 4608, AUX_W);
      const rsrc_t wor = mk_rsrc(wo, 4096 * D * 2);
      const int wo_lo = (kvh * 256 + 32 * sp + g) * 1152 + hw * 256 + c * 16;
      const int wo_hi = (hw == 0 && c < 8) ? wo_lo + 1024 : OOB;
#pragma unroll
      for (int i = 0; i < 8; ++i) WO[i] = __builtin_amdgcn_raw_buffer_load_b128(wor, wo_lo, i * 4608, AUX_W);
#pragma unroll
      for (int i = 8; i < 16; ++i) WO[i] = __builtin_amdgcn_raw_buffer_load_b128(wor, wo_hi, (i - 8) * 4608, AUX_W);
#pragma unroll
      for (int i = 0; i < 9; ++i) gwv[i] = gw[i * 64 + lane];
    }
    FENCE();
    {
      f32x4 ga = {0.f, 0.f, 0.f, 0.f};
#pragma unroll
      for (int i = 0; i < 11; ++i) {
        const int row = 4 * (rg0 + i) + g;
        const unsigned xv = *(const LAS unsigned*)(smem + L_XS + (row < D ? row : 0) * 4);
        ga = mfma(G1[i], diag(hi8 ? ((xv & 0xffff0000u) | (xv >> 16)) : ((xv << 16) | (xv & 0xffffu)), msk), ga);
      }
      if (hw < 2) {
        *(LAS f32x4*)(smem + L_GU + hw * 1024 + voff) = ga;
        lds_inc(smem, S_GU, lane);
      } else {
        lds_wait<2>(smem, S_GU, 2u);
        ga = *(const LAS f32x4*)(smem + L_GU + (hw & 1) * 1024 + voff) + ga;
        __builtin_amdgcn_raw_buffer_store_b128(__builtin_bit_cast(v4u, ga), wsr, has_s1 ? WS_G1 + (((cblk * 2 + (hw & 1)) * 2 + mat1) * 7 + rb) * 1024 + voff : OOB, 0, AUX_SC1);
        DRAIN();
        lds_inc(smem, S_G1, lane);
      }
    }
    float gt;
    {
      float z = 0.f;
#pragma unroll
      for (int i = 0; i < 9; ++i) {
        const unsigned xv = *(const LAS unsigned*)(smem + L_XS + (i * 64 + lane) * 4);
        z += rbf((b ? bfhi(xv) : bflo(xv)) * bflo(gwv[i]));
      }
      z += dpp<XOR1>(z); z += dpp<XOR2>(z); z += dpp<ROR4>(z); z += dpp<ROR8>(z);
      z = rbf(xrow_sum(z));
      gt = rbf(1.f / (1.f + __expf(-z)));
    }
    if (hw == 3) {
      lds_wait<4>(smem, S_G1, 2u);
      v2u fl; fl[0] = tag_lo; fl[1] = tag_hi;
      __builtin_amdgcn_raw_buffer_store_b64(fl, wsr, (lane == 0 && has_s1) ? WS_F1 + cblk * 128 + (mat1 * 7 + rb) * 8 : OOB, 0, AUX_SC1);
      __builtin_amdgcn_s_sleep(100);
      {
        const int fblk = lane < 32 ? (u0 >> 8) : ((u0 + 35) >> 8);
        const int fo = WS_F1 + fblk * 128 + ((lane & 31) < 14 ? (lane & 31) : 0) * 8;
        for (unsigned spins = 0;; ++spins) {
          const v2u p = __builtin_amdgcn_raw_buffer_load_b64(wsr, fo, 0, AUX_SC1);
          if (__all(((p[1] ^ tag_hi) | (p[0] ^ tag_lo)) == 0u) || spins > SPIN_CAP) break;
          __builtin_amdgcn_s_sleep(64);
        }
      }
      __builtin_amdgcn_fence(__ATOMIC_ACQUIRE, "wavefront");
      const int u = u0 + 4 * c + g;
      const int so = c < 9 ? WS_G1 + (u >> 7) * 14 * 1024 + (((u >> 5) & 3) * 16 + b * 8 + (u & 7)) * 16 + ((u >> 3) & 3) * 4 : OOB;
      float pv[14];
#pragma unroll
      for (int i = 0; i < 14; ++i) pv[i] = __uint_as_float(__builtin_amdgcn_raw_buffer_load_b32(wsr, so, i * 1024, AUX_SC1));
      const float gv = rbf(((pv[0] + pv[1]) + (pv[2] + pv[3])) + ((pv[4] + pv[5]) + pv[6]));
      const float uv = rbf(((pv[7] + pv[8]) + (pv[9] + pv[10])) + ((pv[11] + pv[12]) + pv[13]));
      const float inner = 0.7978845608028654f * (gv + 0.044715f * gv * gv * gv);
      const float hv = rbf(0.5f * gv * (1.f + tanhf(inner))) * uv;
      *(LAS unsigned*)(smem + L_HS + lane * 4) = pk_bf16(hv, hv);
      lds_inc(smem, S_H, lane);
    }
    f32x4 yf = {0.f, 0.f, 0.f, 0.f}, zf = {0.f, 0.f, 0.f, 0.f};
    {
      lds_wait<16>(smem, S_H, 1u);
      const unsigned hp = *(const LAS unsigned*)(smem + L_HS + lane * 4);
#define PRD(i) { const v4u bb = diag(dppu<BCAST + (i)>(hp), msk); yf = mfma(WD[i], bb, yf); zf = mfma(WD[9 + (i)], bb, zf); }
      PRD(0) PRD(1) PRD(2) PRD(3) PRD(4) PRD(5) PRD(6) PRD(7) PRD(8)
      const float og = rbf(1.f - gt);
      yf *= og; zf *= og;
      asm volatile("" : "+v"(yf), "+v"(zf));
    }
    FENCE();
    if (hw == 2) {
      const unsigned char* tp = ((lane & 8) ? Vc : Kc) + (size_t)grp * (SK * 256) + (size_t)(lane & 7) * (1 << 20) + NSU * 8192;
      if (lane == 16) tp = (const unsigned char*)out;
      unsigned tch = 0;
      if (lane < 17) tch = __builtin_nontemporal_load((const unsigned*)tp);
      asm volatile("" :: "v"(tch));
    }
    asm volatile("" :: "v"(cpf));
    if (hw == 3) {
      lds_wait<32>(smem, S_KDONE, 4u);
      v2u fl; fl[0] = tag_lo; fl[1] = tag_hi;
      __builtin_amdgcn_raw_buffer_store_b64(fl, wsr, lane == 0 ? WS_SF + bid * 64 : OOB, 0, AUX_SC1);
      for (unsigned spins = 0;; ++spins) {
        const v2u p = __builtin_amdgcn_raw_buffer_load_b64(wsr, WS_SF + (grp * 8 + (lane & 7)) * 64, 0, AUX_SC1);
        if (__all(((p[1] ^ tag_hi) | (p[0] ^ tag_lo)) == 0u) || spins > SPIN_CAP) break;
        __builtin_amdgcn_s_sleep(32);
      }
      lds_inc(smem, S_SHARED, lane);
      if (is_final) {
        for (unsigned spins = 0;; ++spins) {
          unsigned bad = 0;
#pragma unroll
          for (int i = 0; i < 2; ++i) {
            const v4u p = __builtin_amdgcn_raw_buffer_load_b128(wsr, WS_ST + i * 1024 + voff, 0, AUX_SC1);
            bad |= (p[1] ^ tag_hi) | (p[0] ^ tag_lo) | (p[3] ^ tag_hi) | (p[2] ^ tag_lo);
          }
          if (__all(bad == 0u) || spins > SPIN_CAP) break;
          __builtin_amdgcn_s_sleep(64);
        }
        __builtin_amdgcn_raw_buffer_store_b64(fl, wsr, (fid == 0 && lane == 0) ? WS_E : OOB, 0, AUX_SC1);
        lds_inc(smem, S_ALL, lane);
      }
    }
    FENCE();
    DRAIN();
    lds_wait<32>(smem, S_NEAR, 1u);
    lds_wait<0>(smem, S_O, 1u);
    const unsigned pp = *(const LAS unsigned*)(smem + L_OS + lane * 4);
    {
      f32x4 y0 = {0.f, 0.f, 0.f, 0.f}, y1 = {0.f, 0.f, 0.f, 0.f}, z0 = {0.f, 0.f, 0.f, 0.f};
#define PRJ(i, y) { const v4u bb = diag(dppu<BCAST + (i)>(pp), msk); y = mfma(WO[i], bb, y); z0 = mfma(WO[8 + (i)], bb, z0); }
      PRJ(0, y0) PRJ(1, y1) PRJ(2, y0) PRJ(3, y1) PRJ(4, y0) PRJ(5, y1) PRJ(6, y0) PRJ(7, y1)
      y0 = (y0 + y1) * gt + yf;
      z0 = z0 * gt + zf;
      const int dst = WS_H2 + ((b * 36 + hw * 8 + 2 * g) * 128 + (bid & 127)) * 64 + c * 4;
      __builtin_amdgcn_raw_buffer_store_b32(__float_as_uint(hi8 ? y0[1] : y0[0]) | 1u, wsr, dst, 0, AUX_SC1);
      __builtin_amdgcn_raw_buffer_store_b32(__float_as_uint(hi8 ? y0[3] : y0[2]) | 1u, wsr, dst, 8192, AUX_SC1);
      if (hw == 0) {
        const int dz = g < 2 ? dst + 32 * 8192 : OOB;
        __builtin_amdgcn_raw_buffer_store_b32(__float_as_uint(hi8 ? z0[1] : z0[0]) | 1u, wsr, dz, 0, AUX_SC1);
        __builtin_amdgcn_raw_buffer_store_b32(__float_as_uint(hi8 ? z0[3] : z0[2]) | 1u, wsr, dz, 8192, AUX_SC1);
      }
    }
  }
}

extern "C" void kernel_launch(void* const* d_in, const int* in_sizes, int n_in, void* d_out, int out_size, void* d_ws, size_t ws_size,
                              hipStream_t stream) {
  k_fused<<<256, NTHR, 0, stream>>>((const unsigned short*)d_in[0], (const unsigned char*)d_in[1], (const unsigned char*)d_in[2],
                                    (const unsigned char*)d_in[3], (const unsigned char*)d_in[4], (const unsigned char*)d_in[6],
                                    (const unsigned char*)d_in[7], (const unsigned char*)d_in[8], (const unsigned short*)d_in[9],
                                    (unsigned char*)d_ws, (unsigned*)d_out);
}
```

```cpp
#include <hip/hip_runtime.h>
#include <hip/hip_bf16.h>
#include <stdint.h>

typedef unsigned v4u __attribute__((ext_vector_type(4)));
typedef unsigned v2u __attribute__((ext_vector_type(2)));
typedef float f32x4 __attribute__((ext_vector_type(4)));
typedef float f32x2 __attribute__((ext_vector_type(2)));
typedef double f64x4 __attribute__((ext_vector_type(4)));
typedef short bf16x8 __attribute__((ext_vector_type(8)));
typedef __bf16 bf16x2 __attribute__((ext_vector_type(2)));
typedef __amdgpu_buffer_rsrc_t rsrc_t;
#define LAS __attribute__((address_space(3)))

constexpr int D = 576, SK = 32768, F = 4608;
constexpr int NTHR = 512;
constexpr unsigned NSU = 124;
constexpr unsigned ND = 8 * (128 - NSU);
constexpr unsigned NONE = 1024;
constexpr int NST = NSU / 4;
static_assert(NSU % 4 == 0 && NST % 2 == 1, "own steps per wave: odd");
constexpr int AUX_NT = 2, AUX_SC1 = 16;
constexpr int AUX_W = 0;
constexpr int OOB = 0x40000000;
constexpr unsigned SPIN_CAP = 200000u;
constexpr int PACE_T = 1756;
constexpr int PACE_CREDIT = 3500;
constexpr int CODE_BYTES = 25600;

constexpr int WS_E = 0;
constexpr int WS_CNT = 4096;
constexpr int WS_QG = 16384;
constexpr int WS_H1 = WS_QG + 131072;
constexpr int H1_STRIDE = 2176;
constexpr int WS_H2 = WS_H1 + 256 * H1_STRIDE;
constexpr int WS_ST = WS_H2 + 72 * 8192;
constexpr int WS_SF = WS_ST + 4096;
constexpr int WS_SG = WS_SF + 256 * 64;
constexpr int WS_F1 = WS_SG + 32 * (int)ND * 128;
constexpr int WS_G1 = WS_F1 + 4096;
constexpr int WS_END = WS_G1 + 36 * 14 * 1024;

constexpr int L_XS = 0;
constexpr int L_QP = 2304;
constexpr int L_QS = L_QP + 1024;
constexpr int L_MRG = L_QS + 512;
constexpr int L_ML = L_MRG + 8192;
constexpr int L_OS = L_ML + 96;
constexpr int L_GU = L_OS + 512;
constexpr int L_HS = L_GU + 2048;
constexpr int L_FB = L_HS + 256;
constexpr int L_SYNC = L_FB + 256;
constexpr int L_SC = L_SYNC + 48;
constexpr int L_SB = L_SC + 16384;
constexpr int L_END = L_SB + 4096;
constexpr int LDS_BYTES = 96 * 1024;
static_assert(L_END <= LDS_BYTES, "lds");
enum { S_Q = 0, S_G1 = 1, S_NEAR = 2, S_MRG = 3, S_LAST = 4, S_QP = 5, S_ALL = 6, S_KDONE = 7, S_FIN = 8, S_SHARED = 9, S_GU = 10, S_H = 11 };

__device__ __forceinline__ float bflo(unsigned u) { return __builtin_bit_cast(float, u << 16); }
__device__ __forceinline__ float bfhi(unsigned u) { return __builtin_bit_cast(float, u & 0xffff0000u); }
__device__ __forceinline__ unsigned pk_bf16(float lo, float hi) {
  bf16x2 v; v[0] = (__bf16)lo; v[1] = (__bf16)hi; return __builtin_bit_cast(unsigned, v);
}
__device__ __forceinline__ float rbf(float f) { return bflo(pk_bf16(f, f)); }
__device__ __forceinline__ float dot2(unsigned a, unsigned b, float c) {
  return __builtin_amdgcn_fdot2_f32_bf16(__builtin_bit_cast(bf16x2, a), __builtin_bit_cast(bf16x2, b), c, false);
}
constexpr int XOR1 = 0xB1, XOR2 = 0x4E, HMIR = 0x141, ROR4 = 0x124, ROR8 = 0x128, BCAST = 0x150;
template <int CTRL> __device__ __forceinline__ unsigned dppu(unsigned x) { return (unsigned)__builtin_amdgcn_mov_dpp((int)x, CTRL, 0xf, 0xf, true); }
template <int CTRL> __device__ __forceinline__ float dpp(float x) { return __uint_as_float(dppu<CTRL>(__float_as_uint(x))); }
template <int CTRL> __device__ __forceinline__ double dppd(double x) {
  const v2u u = __builtin_bit_cast(v2u, x);
  v2u r; r[0] = dppu<CTRL>(u[0]); r[1] = dppu<CTRL>(u[1]);
  return __builtin_bit_cast(double, r);
}
__device__ __forceinline__ f32x4 mfma(v4u a, v4u b, f32x4 c) {
  return __builtin_amdgcn_mfma_f32_16x16x32_bf16(__builtin_bit_cast(bf16x8, a), __builtin_bit_cast(bf16x8, b), c, 0, 0, 0);
}
__device__ __forceinline__ float swap16_sum(float a, float b) {
  auto s = __builtin_amdgcn_permlane16_swap(__float_as_uint(a), __float_as_uint(b), false, false);
  return __uint_as_float(s[0]) + __uint_as_float(s[1]);
}
__device__ __forceinline__ float swap32_sum(float a, float b) {
  auto s = __builtin_amdgcn_permlane32_swap(__float_as_uint(a), __float_as_uint(b), false, false);
  return __uint_as_float(s[0]) + __uint_as_float(s[1]);
}
__device__ __forceinline__ float xrow_max(float x) {
  auto s = __builtin_amdgcn_permlane16_swap(__float_as_uint(x), __float_as_uint(x), false, false);
  x = fmaxf(__uint_as_float(s[0]), __uint_as_float(s[1]));
  auto t = __builtin_amdgcn_permlane32_swap(__float_as_uint(x), __float_as_uint(x), false, false);
  return fmaxf(__uint_as_float(t[0]), __uint_as_float(t[1]));
}
__device__ __forceinline__ float xrow_sum(float x) { x = swap16_sum(x, x); return swap32_sum(x, x); }
__device__ __forceinline__ double xrow_sumd(double x) {
  v2u u = __builtin_bit_cast(v2u, x), a, b2;
  { auto s0 = __builtin_amdgcn_permlane16_swap(u[0], u[0], false, false); auto s1 = __builtin_amdgcn_permlane16_swap(u[1], u[1], false, false);
    a[0] = s0[0]; a[1] = s1[0]; b2[0] = s0[1]; b2[1] = s1[1]; }
  x = __builtin_bit_cast(double, a) + __builtin_bit_cast(double, b2);
  u = __builtin_bit_cast(v2u, x);
  { auto s0 = __builtin_amdgcn_permlane32_swap(u[0], u[0], false, false); auto s1 = __builtin_amdgcn_permlane32_swap(u[1], u[1], false, false);
    a[0] = s0[0]; a[1] = s1[0]; b2[0] = s0[1]; b2[1] = s1[1]; }
  return __builtin_bit_cast(double, a) + __builtin_bit_cast(double, b2);
}
__device__ __forceinline__ rsrc_t mk_rsrc(const void* p, int bytes) { return __builtin_amdgcn_make_buffer_rsrc((void*)p, 0, bytes, 0x00020000); }
#define DRAIN() do { __builtin_amdgcn_s_waitcnt(0x0F70); asm volatile("s_waitcnt vmcnt(0)" ::: "memory"); } while (0)
#define LDSW() asm volatile("s_waitcnt lgkmcnt(0)" ::: "memory")
#define FENCE() __builtin_amdgcn_sched_barrier(0)

constexpr float SCALE = 0.08838834764831845f, L2E = 1.4426950408889634f;

struct St { float m; double l; f64x4 acc; };
__device__ __forceinline__ void pin(St& st) { asm volatile("" : "+v"(st.m), "+v"(st.l), "+v"(st.acc) :: "memory"); }
__device__ __forceinline__ v4u diag(unsigned pp, const v4u& msk) {
  v4u bb; bb[0] = pp & msk[0]; bb[1] = pp & msk[1]; bb[2] = pp & msk[2]; bb[3] = pp & msk[3];
  return bb;
}

__device__ __forceinline__ v2u scores(const v4u* KB, const v4u& q0, const v4u& q1, bool hi8) {
  float s[4];
#pragma unroll
  for (int t = 0; t < 4; ++t) {
    float a0 = dot2(KB[t][0], q0[0], 0.f), a1 = dot2(KB[t][0], q1[0], 0.f);
#pragma unroll
    for (int k = 1; k < 4; ++k) { a0 = dot2(KB[t][k], q0[k], a0); a1 = dot2(KB[t][k], q1[k], a1); }
    const float keep = hi8 ? a1 : a0, send = hi8 ? a0 : a1;
    float v = keep + dpp<ROR8>(send);
    v += dpp<HMIR>(v); v += dpp<XOR2>(v); v += dpp<XOR1>(v);
    s[t] = v;
  }
  const unsigned u0 = pk_bf16(s[0], s[1]), u1 = pk_bf16(s[2], s[3]);
  v2u r; r[0] = pk_bf16(bflo(u0) * SCALE, bfhi(u0) * SCALE); r[1] = pk_bf16(bflo(u1) * SCALE, bfhi(u1) * SCALE);
  return r;
}
__device__ __forceinline__ void pv_unit(const v4u (&VB)[8], const v4u sc, const v4u& msk, St& st) {
  float s[8];
#pragma unroll
  for (int t = 0; t < 4; ++t) { s[2 * t] = bflo(sc[t]); s[2 * t + 1] = bfhi(sc[t]); }
  float tm = fmaxf(fmaxf(fmaxf(s[0], s[1]), fmaxf(s[2], s[3])), fmaxf(fmaxf(s[4], s[5]), fmaxf(s[6], s[7])));
  tm = xrow_max(tm);
  const float tl = tm * L2E, mt = __builtin_ceilf(tl), fr = tl - mt;
  const float mn = fmaxf(st.m, mt);
  const double alpha = (double)__builtin_amdgcn_exp2f(st.m - mn);
  const float beta = __builtin_amdgcn_exp2f(mt - mn);
  st.m = mn;
  float p[8];
#pragma unroll
  for (int t = 0; t < 8; ++t) p[t] = __builtin_amdgcn_exp2f(fmaf(s[t] - tm, L2E, fr));
  f32x4 ta = {0.f, 0.f, 0.f, 0.f};
#pragma unroll
  for (int t = 0; t < 8; ++t) ta = mfma(VB[t], diag(pk_bf16(p[t], p[t]), msk), ta);
  st.l = __builtin_fma(st.l, alpha, (double)((((p[0] + p[1]) + (p[2] + p[3])) + ((p[4] + p[5]) + (p[6] + p[7]))) * beta));
#pragma unroll
  for (int r = 0; r < 4; ++r) st.acc[r] = __builtin_fma(st.acc[r], alpha, (double)(ta[r] * beta));
}
__device__ __forceinline__ void issue(v4u (&B)[8], rsrc_t r, int voff, unsigned unit) {
  const int so = (int)(unit * 8192u);
#pragma unroll
  for (int t = 0; t < 4; ++t) B[t] = __builtin_amdgcn_raw_buffer_load_b128(r, voff + t * 1024, so, AUX_NT);
#pragma unroll
  for (int t = 0; t < 4; ++t) B[4 + t] = __builtin_amdgcn_raw_buffer_load_b128(r, voff + t * 1024, so + 4096, AUX_NT);
  FENCE();
}
__device__ __forceinline__ unsigned atomic_inc(rsrc_t r, int voff0, bool on, int so) {
  return (unsigned)__builtin_amdgcn_raw_ptr_buffer_atomic_add_i32(1, r, voff0, on ? so : OOB, 0);
}
__device__ __forceinline__ unsigned lds_inc(unsigned char* smem, int idx, int lane) {
  LDSW();
  unsigned old = 0;
  if (lane == 0) old = __hip_atomic_fetch_add((LAS unsigned*)(LAS unsigned char*)smem + (L_SYNC / 4 + idx), 1u, __ATOMIC_RELAXED, __HIP_MEMORY_SCOPE_WORKGROUP);
  return old;
}
template <int SLEEP> __device__ __forceinline__ void lds_wait(unsigned char* smem, int idx, unsigned want) {
#pragma unroll 1
  for (unsigned spins = 0; spins < 8000000u / (1 + SLEEP); ++spins) {
    const unsigned v = __builtin_amdgcn_readfirstlane(*((volatile LAS unsigned*)(LAS unsigned char*)smem + (L_SYNC / 4 + idx)));
    if (v >= want) break;
    if (SLEEP) __builtin_amdgcn_s_sleep(SLEEP);
  }
  asm volatile("" ::: "memory");
}
__device__ __forceinline__ float val(unsigned u) { return __uint_as_float(u & ~1u); }

__global__ void __launch_bounds__(NTHR, 2)
k_fused(const unsigned short* __restrict__ x, const unsigned char* Kc, const unsigned char* Vc,
        const unsigned char* __restrict__ wq, const unsigned char* wo, const unsigned char* wg,
        const unsigned char* wu, const unsigned char* wd, const unsigned short* __restrict__ gw,
        unsigned char* ws, unsigned* out) {
  __shared__ __attribute__((aligned(16))) unsigned char smem[LDS_BYTES];
  const int tid = threadIdx.x, lane = tid & 63, w = __builtin_amdgcn_readfirstlane(tid >> 6);
  const int bid = blockIdx.x, grp = bid >> 3, sp = bid & 7, b = grp >> 4, kvh = grp & 15;
  const int g = lane >> 4, c = lane & 15;
  const int voff = lane * 16;
  const int ridx = (grp + 4 * sp) & 31;
  const bool is_final = ridx < 9;
  const int fid = sp * 9 + ridx;
  const bool hi8 = (c & 8) != 0;
  const int cm = bid % 36, rb = bid / 36, mat1 = cm >= 18 ? 1 : 0, cblk = cm - 18 * mat1;
  const int u0 = 36 * (bid & 127);
#define MAKE_MSK() v4u msk; _Pragma("unroll") for (int d = 0; d < 4; ++d) msk[d] = (((c & 7) >> 1) == d) ? ((c & 1) ? 0xffff0000u : 0x0000ffffu) : 0u;

  if (w >= 4) {
    const int sw = w - 4, stid = tid - 256;
    const rsrc_t kr = mk_rsrc(Kc + (size_t)grp * (SK * 256), SK * 256);
    const rsrc_t vr = mk_rsrc(Vc + (size_t)grp * (SK * 256), SK * 256);
    const rsrc_t wsr = mk_rsrc(ws, WS_END);
    const unsigned ubase = (unsigned)(sp * 128 + sw);
    v4u Ba[8], Bb[8], Be0[8], Be1[8];
    issue(Be0, kr, voff, ubase);
    asm volatile("" ::: "memory");
    __builtin_amdgcn_s_barrier();
    asm volatile("" ::: "memory");
    FENCE();
    if (sw == 0) {
      const unsigned char* tp = Vc + (size_t)bid * (1 << 20);
      if (lane == 1) tp = (mat1 ? wu : wg) + (size_t)(rb < 7 ? (2 * rb) * 72 / 7 * 4 : 0) * (F * 2);
      if (lane == 2) tp = (mat1 ? wu : wg) + (size_t)(rb < 7 ? (2 * rb + 2) * 72 / 7 * 4 - 1 : 0) * (F * 2);
      if (lane == 3) tp = wd + (size_t)u0 * 1152;
      if (lane == 4) tp = wo + (size_t)(kvh * 256 + 32 * sp) * 1152;
      if (lane == 5) tp = (const unsigned char*)gw;
      unsigned tch = 0;
      if (lane < 6) tch = __builtin_nontemporal_load((const unsigned*)tp);
      asm volatile("" :: "v"(tch));
    }
    MAKE_MSK();
    DRAIN(); issue(Be1, kr, voff, ubase + 4);
    DRAIN(); issue(Ba, kr, voff, ubase + 8);
    DRAIN(); issue(Bb, kr, voff, ubase + 12);
    lds_wait<1>(smem, S_Q, 1u);
    const v4u q0 = *(const LAS v4u*)(smem + L_QS + c * 16), q1 = *(const LAS v4u*)(smem + L_QS + 256 + c * 16);
    const int sc_off = ((c >> 3) * 4 + g) * 16;
#define KSC(B) const v2u s0_ = scores(&B[0], q0, q1, hi8), s1_ = scores(&B[4], q0, q1, hi8);                       \
      v4u sc_; sc_[0] = s0_[0]; sc_[1] = s0_[1]; sc_[2] = s1_[0]; sc_[3] = s1_[1];
#define KSTEP(B, i)                                                                                                \
    {                                                                                                              \
      KSC(B)                                                                                                       \
      if ((c & 7) == 0) *(LAS v4u*)(smem + L_SC + (4 * (i) + sw) * 128 + sc_off) = sc_;                            \
      asm volatile("" ::: "memory");                                                                               \
      FENCE();                                                                                                     \
    }
    int nsl = 0;
    unsigned did = 0;
#define SFLUSH()                                                                                                   \
    {                                                                                                              \
      const v4u v_ = *(const LAS v4u*)(smem + L_SB + sw * 1024 + voff);                                            \
      __builtin_amdgcn_raw_buffer_store_b128(v_, wsr, (lane >> 3) < nsl ? WS_SG + (int)((grp * ND + did) * 128u) + (lane & 7) * 16 : OOB, 0, AUX_SC1); \
      nsl = 0;                                                                                                     \
    }
#define KSTEPG(B, dd)                                                                                              \
    {                                                                                                              \
      KSC(B)                                                                                                       \
      if ((c & 7) == 0) *(LAS v4u*)(smem + L_SB + sw * 1024 + nsl * 128 + sc_off) = sc_;                           \
      did = (lane >> 3) == nsl ? (dd) : did;                                                                       \
      ++nsl;                                                                                                       \
      if (nsl == 8) SFLUSH()                                                                                       \
      asm volatile("" ::: "memory");                                                                               \
      FENCE();                                                                                                     \
    }
#define PACE() do { int lag_; while ((lag_ = (int)((unsigned)__builtin_amdgcn_s_memrealtime() * 16u - due)) < 0) __builtin_amdgcn_s_sleep(1); \
                    if (lag_ > PACE_CREDIT) due += (unsigned)(lag_ - PACE_CREDIT); due += PACE_T; } while (0)
#define MAPD(d) (((d) & 7u) * 128u + NSU + ((d) >> 3))
    const int cnt0 = lane == 0 ? WS_CNT + grp * 256 : OOB;
    unsigned a = 0u;
    KSTEP(Ba, 2);
    issue(Ba, kr, voff, ubase + 16);
    KSTEP(Be0, 0);
    KSTEP(Be1, 1);
    KSTEP(Bb, 3);
    unsigned due = (unsigned)__builtin_amdgcn_s_memrealtime() * 16u;
#pragma unroll 1
    for (int i = 4; i < NST - 1; i += 2) {
      PACE();
      DRAIN();
      issue(Bb, kr, voff, ubase + 4 * (i + 1));
      KSTEP(Ba, i);
      PACE();
      DRAIN();
      a = atomic_inc(wsr, cnt0, i + 3 >= NST, 0);
      issue(Ba, kr, voff, ubase + 4 * (i + 2));
      KSTEP(Bb, i + 1);
    }
    {
      unsigned da, db;
      DRAIN();
      db = __builtin_amdgcn_readfirstlane(a);
      a = atomic_inc(wsr, cnt0, db < ND, 0);
      issue(Bb, db < ND ? kr : vr, voff, db < ND ? MAPD(db) : ubase);
      KSTEP(Ba, NST - 1);
      bool in_b = true;
      while (db < ND) {
        DRAIN();
        da = __builtin_amdgcn_readfirstlane(a);
        a = atomic_inc(wsr, cnt0, da < ND, 0);
        issue(Ba, da < ND ? kr : vr, voff, da < ND ? MAPD(da) : ubase);
        KSTEPG(Bb, db);
        if (da >= ND) { in_b = false; break; }
        DRAIN();
        db = __builtin_amdgcn_readfirstlane(a);
        a = atomic_inc(wsr, cnt0, db < ND, 0);
        issue(Bb, db < ND ? kr : vr, voff, db < ND ? MAPD(db) : ubase);
        KSTEPG(Ba, da);
      }
      SFLUSH()
      DRAIN();
      if (in_b) {
#pragma unroll
        for (int t = 0; t < 8; ++t) Ba[t] = Bb[t];
      }
    }
    lds_inc(smem, S_KDONE, lane);
    St st; st.m = -INFINITY; st.l = 0.0; st.acc = f64x4{0.0, 0.0, 0.0, 0.0};
#define LSC(i) (*(const LAS v4u*)(smem + L_SC + (4 * (i) + sw) * 128 + sc_off))
    due = (unsigned)__builtin_amdgcn_s_memrealtime() * 16u;
#pragma unroll 1
    for (int i = 0; i < NST - 1; i += 2) {
      PACE();
      DRAIN();
      issue(Bb, vr, voff, ubase + 4 * (i + 1));
      pv_unit(Ba, LSC(i), msk, st); pin(st);
      FENCE();
      PACE();
      DRAIN();
      a = atomic_inc(wsr, cnt0, i + 3 >= NST, 4);
      issue(Ba, vr, voff, ubase + 4 * (i + 2));
      pv_unit(Bb, LSC(i + 1), msk, st); pin(st);
      FENCE();
    }
    lds_wait<2>(smem, S_SHARED, 1u);
    lds_inc(smem, S_NEAR, lane);
    v4u sa = LSC(NST - 1), sb;
    const int sg_ld = WS_SG + grp * (int)ND * 128 + sc_off;
    while (true) {
      DRAIN();
      unsigned d = __builtin_amdgcn_readfirstlane(a);
      a = atomic_inc(wsr, cnt0, d < ND, 4);
      sb = __builtin_amdgcn_raw_buffer_load_b128(wsr, sg_ld, d < ND ? (int)(d * 128u) : OOB, AUX_SC1);
      issue(Bb, vr, voff, d < ND ? MAPD(d) : NONE);
      if (d >= ND) lds_inc(smem, S_LAST, lane);
      pv_unit(Ba, sa, msk, st); pin(st);
      FENCE();
      if (d >= ND) break;
      DRAIN();
      d = __builtin_amdgcn_readfirstlane(a);
      a = atomic_inc(wsr, cnt0, d < ND, 4);
      sa = __builtin_amdgcn_raw_buffer_load_b128(wsr, sg_ld, d < ND ? (int)(d * 128u) : OOB, AUX_SC1);
      issue(Ba, vr, voff, d < ND ? MAPD(d) : NONE);
      if (d >= ND) lds_inc(smem, S_LAST, lane);
      pv_unit(Bb, sb, msk, st); pin(st);
      FENCE();
      if (d >= ND) break;
    }
    {
      const double l = xrow_sumd(st.l);
      const int h = c >> 3;
#pragma unroll
      for (int r = 0; r < 4; ++r) {
        const int dim = 8 * (4 * g + r) + (c & 7);
        *(LAS double*)(smem + L_MRG + (sw * 256 + h * 128 + (dim & 96) + (dim & 3) * 8 + ((dim >> 2) & 7)) * 8) = st.acc[r];
      }
      if (g == 0 && (c & 7) == 0) { *(LAS float*)(smem + L_ML + (h * 4 + sw) * 4) = st.m; *(LAS double*)(smem + L_ML + 32 + (h * 4 + sw) * 8) = l; }
    }
    lds_inc(smem, S_MRG, lane);
    lds_wait<0>(smem, S_MRG, 4u);
    {
      const int hh = sw >> 1;
      const f32x4 m4 = *(const LAS f32x4*)(smem + L_ML + hh * 16);
      const f64x4 l4 = *(const LAS f64x4*)(smem + L_ML + 32 + hh * 32);
      double a4[4];
#pragma unroll
      for (int ww = 0; ww < 4; ++ww) a4[ww] = *(const LAS double*)(smem + L_MRG + (ww * 256 + stid) * 8);
      const float M = fmaxf(fmaxf(m4[0], m4[1]), fmaxf(m4[2], m4[3]));
      double L = 0.0, o = 0.0;
#pragma unroll
      for (int ww = 0; ww < 4; ++ww) {
        const double wt = (double)__builtin_amdgcn_exp2f(m4[ww] - M);
        L = __builtin_fma(wt, l4[ww], L); o = __builtin_fma(wt, a4[ww], o);
      }
      v2u ov = __builtin_bit_cast(v2u, o); ov[0] |= 1u;
      __builtin_amdgcn_raw_buffer_store_b64(ov, wsr, WS_H1 + bid * H1_STRIDE + stid * 8, 0, AUX_SC1);
      if ((stid & 127) < 2) {
        v2u ml = __builtin_bit_cast(v2u, L); ml[0] |= 1u;
        if (stid & 1) { ml[0] = 1u; ml[1] = __float_as_uint(M); }
        __builtin_amdgcn_raw_buffer_store_b64(ml, wsr, WS_H1 + bid * H1_STRIDE + 2048 + hh * 16 + (stid & 1) * 8, 0, AUX_SC1);
      }
    }
    for (int i = 0; i < sw; ++i) __builtin_amdgcn_s_sleep(4);
    {
      const int o_acc = WS_H1 + (grp * 8 + 4 * (c >> 3)) * H1_STRIDE + (32 * sp + 8 * g + (c & 7)) * 8;
      const int o_ml = WS_H1 + (grp * 8 + (c & 7)) * H1_STRIDE + 2048 + (sp >> 2) * 16;
      v2u pv[4];
      v4u pm = {0u, 0u, 0u, 0u};
#pragma unroll
      for (int i = 0; i < 4; ++i) pv[i] = v2u{0u, 0u};
      bool mine = true;
      for (unsigned spins = 0;; ++spins) {
        if (!(pv[0][0] & pv[1][0] & pv[2][0] & pv[3][0] & 1u)) {
#pragma unroll
          for (int i = 0; i < 4; ++i) pv[i] = __builtin_amdgcn_raw_buffer_load_b64(wsr, o_acc, i * H1_STRIDE, AUX_SC1);
        }
        if (!(pm[0] & pm[2] & 1u)) pm = __builtin_amdgcn_raw_buffer_load_b128(wsr, o_ml, 0, AUX_SC1);
        if (__all((pv[0][0] & pv[1][0] & pv[2][0] & pv[3][0] & pm[0] & pm[2] & 1u) != 0u) || spins > SPIN_CAP) break;
        if (__builtin_amdgcn_readfirstlane(*((volatile LAS unsigned*)(smem + L_OS) + 1)) != 0u) { mine = false; break; }
        __builtin_amdgcn_s_sleep(2);
      }
      if (mine) {
        const float Ms = __uint_as_float(pm[3]);
        float Mx = fmaxf(Ms, dpp<XOR1>(Ms)); Mx = fmaxf(Mx, dpp<XOR2>(Mx)); Mx = fmaxf(Mx, dpp<HMIR>(Mx));
        const float wt = __builtin_amdgcn_exp2f(Ms - Mx);
        v2u lv; lv[0] = pm[0]; lv[1] = pm[1];
        double den = (double)wt * __builtin_bit_cast(double, lv);
        den += dppd<XOR1>(den); den += dppd<XOR2>(den); den += dppd<HMIR>(den);
        double num = 0.0;
#define ACC1(i) { const float wl_ = dpp<BCAST + (i)>(wt), wh_ = dpp<BCAST + 4 + (i)>(wt); num = __builtin_fma((double)(hi8 ? wh_ : wl_), __builtin_bit_cast(double, pv[i]), num); }
        ACC1(0) ACC1(1) ACC1(2) ACC1(3)
        num += dppd<ROR8>(num);
        const float ov = (float)num * __builtin_amdgcn_rcpf((float)den);
        v2u om; om[0] = pk_bf16(ov, ov); om[1] = 1u;
        *(volatile LAS unsigned long long*)(smem + L_OS + lane * 8) = __builtin_bit_cast(unsigned long long, om);
      }
    }
    if (is_final) {
      const int src = WS_H2 + fid * 8192 + sw * 2048 + voff;
      lds_wait<8>(smem, S_ALL, 1u);
      v4u p0, p1;
      for (unsigned spins = 0;; ++spins) {
        p0 = __builtin_amdgcn_raw_buffer_load_b128(wsr, src, 0, AUX_SC1);
        p1 = __builtin_amdgcn_raw_buffer_load_b128(wsr, src, 1024, AUX_SC1);
        const unsigned m = (p0[0] & p0[1] & p0[2] & p0[3]) & (p1[0] & p1[1] & p1[2] & p1[3]) & 1u;
        if (__all(m != 0u) || spins > SPIN_CAP) break;
        __builtin_amdgcn_s_sleep(2);
      }
      f32x4 t4;
#pragma unroll
      for (int j = 0; j < 4; ++j) {
        float t = val(p0[j]) + val(p1[j]);
        t += dpp<ROR4>(t); t += dpp<ROR8>(t);
        t4[j] = xrow_sum(t);
      }
      if (lane < 4) *(LAS f32x4*)(smem + L_FB + (sw * 4 + lane) * 16) = t4;
      const unsigned arrived = __builtin_amdgcn_readfirstlane(lds_inc(smem, S_FIN, lane));
      asm volatile("" ::: "memory");
      if (arrived == 3u && lane < 8) {
        f32x2 t = *(const LAS f32x2*)(smem + L_FB + lane * 8);
#pragma unroll
        for (int ww = 1; ww < 4; ++ww) t += *(const LAS f32x2*)(smem + L_FB + ww * 64 + lane * 8);
        out[fid * 8 + lane] = pk_bf16(t[0], t[1]);
      }
    }
  } else {
    const int hw = w;
    const v4u cpf = __builtin_amdgcn_raw_buffer_load_b128(mk_rsrc((const void*)(__builtin_amdgcn_s_getpc() & ~255ull), CODE_BYTES),
                                                          hw == 0 ? grp * 1024 + voff : OOB, 0, 0);
    unsigned short xq[9];
    v4u wqv[9];
    {
      const int r = lane >> 3, row0 = 288 * (sp & 1) + 72 * hw;
      const rsrc_t qr = mk_rsrc(wq, D * 8192);
      const int base = (row0 + r) * 8192 + (kvh * 256 + (sp >> 1) * 64) * 2 + (lane & 7) * 16;
#pragma unroll
      for (int i = 0; i < 9; ++i) wqv[i] = __builtin_amdgcn_raw_buffer_load_b128(qr, base, i * 8 * 8192, AUX_W);
#pragma unroll
      for (int i = 0; i < 9; ++i) xq[i] = x[b * D + row0 + 8 * i + r];
    }
    const int xi = tid < 144 ? tid : 143;
    const v2u xa = ((const v2u*)x)[xi], xb = ((const v2u*)x)[144 + xi];
    if (tid < 3) { const v4u z = {0u, 0u, 0u, 0u}; *(LAS v4u*)(smem + L_SYNC + tid * 16) = z; }
    if (tid >= 64 && tid < 96) { const v4u z = {0u, 0u, 0u, 0u}; *(LAS v4u*)(smem + L_OS + (tid - 64) * 16) = z; }
    FENCE();
    LDSW();
    __builtin_amdgcn_s_barrier();
    asm volatile("" ::: "memory");
    const rsrc_t wsr = mk_rsrc(ws, WS_END);
    v2u evv = {0u, 0u};
    if (hw == 3) {
      evv = __builtin_amdgcn_raw_buffer_load_b64(wsr, WS_E, 0, AUX_SC1);
      const v4u z = {0u, 0u, 0u, 0u};
      __builtin_amdgcn_raw_buffer_store_b128(z, wsr, WS_H1 + bid * H1_STRIDE + voff, 0, AUX_SC1);
      __builtin_amdgcn_raw_buffer_store_b128(z, wsr, WS_H1 + bid * H1_STRIDE + 1024 + voff, 0, AUX_SC1);
      const int h2o = WS_H2 + (b * 36 * 128 + (bid & 127)) * 64 + (lane >> 2) * 8192 + (lane & 3) * 16;
      __builtin_amdgcn_raw_buffer_store_b128(z, wsr, h2o, 0, AUX_SC1);
      __builtin_amdgcn_raw_buffer_store_b128(z, wsr, h2o, 16 * 8192, AUX_SC1);
      int off = h2o + 32 * 8192;
      if (lane >= 16) off = WS_H1 + bid * H1_STRIDE + 2048 + (lane & 1) * 16;
      if (lane >= 32) off = sp == 0 ? WS_CNT + grp * 256 : OOB;
      __builtin_amdgcn_raw_buffer_store_b128(z, wsr, off, 0, AUX_SC1);
    }
    FENCE();
    {
      float acc[8];
#pragma unroll
      for (int j = 0; j < 8; ++j) acc[j] = 0.f;
#pragma unroll
      for (int i = 0; i < 9; ++i) {
        const float xv = bflo(xq[i]);
#pragma unroll
        for (int k = 0; k < 4; ++k) { acc[2 * k] = fmaf(xv, bflo(wqv[i][k]), acc[2 * k]); acc[2 * k + 1] = fmaf(xv, bfhi(wqv[i][k]), acc[2 * k + 1]); }
      }
      float a4[4];
#pragma unroll
      for (int j = 0; j < 4; ++j) a4[j] = swap32_sum(acc[j], acc[j + 4]);
      f32x2 r2; r2[0] = swap16_sum(a4[0], a4[2]); r2[1] = swap16_sum(a4[1], a4[3]);
      r2[0] += dpp<ROR8>(r2[0]); r2[1] += dpp<ROR8>(r2[1]);
      if ((lane & 8) == 0) *(LAS f32x2*)(smem + L_QP + (hw * 64 + (lane & 7) * 8 + 4 * (lane >> 5) + 2 * ((lane >> 4) & 1)) * 4) = r2;
      if (tid < 144) {
        v4u xs4;
        xs4[0] = (xa[0] & 0xffffu) | (xb[0] << 16); xs4[1] = (xa[0] >> 16) | (xb[0] & 0xffff0000u);
        xs4[2] = (xa[1] & 0xffffu) | (xb[1] << 16); xs4[3] = (xa[1] >> 16) | (xb[1] & 0xffff0000u);
        *(LAS v4u*)(smem + L_XS + tid * 16) = xs4;
      }
      lds_inc(smem, S_QP, lane);
      lds_wait<0>(smem, S_QP, 4u);
    }
    MAKE_MSK();
    unsigned tag_lo = 0, tag_hi = 0;
    if (hw == 3) {
      const unsigned long long ev = ((unsigned long long)(unsigned)__builtin_amdgcn_readfirstlane(evv[1]) << 32) | (unsigned)__builtin_amdgcn_readfirstlane(evv[0]);
      const unsigned long long tag64 = ev * 6364136223846793005ull + 1442695040888963407ull;
      tag_lo = (unsigned)tag64; tag_hi = (unsigned)(tag64 >> 32);
      {
        float s = 0.f;
#pragma unroll
        for (int ww = 0; ww < 4; ++ww) s += *(const LAS float*)(smem + L_QP + (ww * 64 + lane) * 4);
        DRAIN();
        v2u gr; gr[0] = __float_as_uint(s); gr[1] = (lane & 1) ? tag_lo : tag_hi;
        __builtin_amdgcn_raw_buffer_store_b64(gr, wsr, WS_QG + ((grp * 2 + (sp & 1)) * 256 + (sp >> 1) * 64 + lane) * 8, 0, AUX_SC1);
        v2u fl; fl[0] = tag_lo; fl[1] = tag_hi;
        __builtin_amdgcn_raw_buffer_store_b64(fl, wsr, lane == 0 ? WS_ST + bid * 8 : OOB, 0, AUX_SC1);
      }
      for (unsigned spins = 0;; ++spins) {
        v4u p[4];
#pragma unroll
        for (int i = 0; i < 4; ++i) p[i] = __builtin_amdgcn_raw_buffer_load_b128(wsr, WS_QG + grp * 4096 + lane * 32 + (i & 1) * 16 + (i >> 1) * 2048, 0, AUX_SC1);
        unsigned bad = 0;
#pragma unroll
        for (int i = 0; i < 4; ++i) bad |= (p[i][1] ^ tag_hi) | (p[i][3] ^ tag_lo);
        if (__all(bad == 0u) || spins > SPIN_CAP) {
          float qv[4];
#pragma unroll
          for (int i = 0; i < 4; ++i)
            qv[i] = __uint_as_float(p[i >> 1][(i & 1) * 2]) + __uint_as_float(p[2 + (i >> 1)][(i & 1) * 2]);
          v2u qq; qq[0] = pk_bf16(qv[0], qv[1]); qq[1] = pk_bf16(qv[2], qv[3]);
          *(LAS v2u*)(smem + L_QS + lane * 8) = qq;
          break;
        }
        __builtin_amdgcn_s_sleep(2);
      }
      lds_inc(smem, S_Q, lane);
    }
    v4u G1[11], WD[18];
    const int hb = 2 * rb + (hw >> 1);
    const int rg0 = hb * 72 / 7, nrg = (hb + 1) * 72 / 7 - rg0;
    const bool has_s1 = bid < 252;
    unsigned short gwv[9];
    {
      const rsrc_t gr = mk_rsrc(mat1 ? wu : wg, has_s1 ? D * F * 2 : 0);
      const int go = (4 * rg0 + g) * (F * 2) + cblk * 512 + (hw & 1) * 256 + c * 16;
#pragma unroll
      for (int i = 0; i < 11; ++i) G1[i] = __builtin_amdgcn_raw_buffer_load_b128(gr, go, (i < 10 || nrg == 11) ? i * 4 * (F * 2) : OOB, AUX_W);
      const rsrc_t dr = mk_rsrc(wd, F * D * 2);
      const int wd_lo = (u0 + g) * 1152 + hw * 256 + c * 16;
      const int wd_hi = (hw == 0 && c < 8) ? wd_lo + 1024 : OOB;
#pragma unroll
      for (int i = 0; i < 9; ++i) WD[i] = __builtin_amdgcn_raw_buffer_load_b128(dr, wd_lo, i * 4608, AUX_W);
#pragma unroll
      for (int i = 9; i < 18; ++i) WD[i] = __builtin_amdgcn_raw_buffer_load_b128(dr, wd_hi, (i - 9) * 4608, AUX_W);
#pragma unroll
      for (int i = 0; i < 9; ++i) gwv[i] = gw[i * 64 + lane];
    }
    FENCE();
    {
      f32x4 ga = {0.f, 0.f, 0.f, 0.f};
#pragma unroll
      for (int i = 0; i < 11; ++i) {
        const int row = 4 * (rg0 + i) + g;
        const unsigned xv = *(const LAS unsigned*)(smem + L_XS + (row < D ? row : 0) * 4);
        ga = mfma(G1[i], diag(hi8 ? ((xv & 0xffff0000u) | (xv >> 16)) : ((xv << 16) | (xv & 0xffffu)), msk), ga);
      }
      if (hw < 2) {
        *(LAS f32x4*)(smem + L_GU + hw * 1024 + voff) = ga;
        lds_inc(smem, S_GU, lane);
      } else {
        lds_wait<2>(smem, S_GU, 2u);
        ga = *(const LAS f32x4*)(smem + L_GU + (hw & 1) * 1024 + voff) + ga;
        __builtin_amdgcn_raw_buffer_store_b128(__builtin_bit_cast(v4u, ga), wsr, has_s1 ? WS_G1 + (((cblk * 2 + (hw & 1)) * 2 + mat1) * 7 + rb) * 1024 + voff : OOB, 0, AUX_SC1);
        DRAIN();
        lds_inc(smem, S_G1, lane);
      }
    }
    float gt;
    {
      float z = 0.f;
#pragma unroll
      for (int i = 0; i < 9; ++i) {
        const unsigned xv = *(const LAS unsigned*)(smem + L_XS + (i * 64 + lane) * 4);
        z += rbf((b ? bfhi(xv) : bflo(xv)) * bflo(gwv[i]));
      }
      z += dpp<XOR1>(z); z += dpp<XOR2>(z); z += dpp<ROR4>(z); z += dpp<ROR8>(z);
      z = rbf(xrow_sum(z));
      gt = rbf(1.f / (1.f + __expf(-z)));
    }
    if (hw == 3) {
      lds_wait<4>(smem, S_G1, 2u);
      v2u fl; fl[0] = tag_lo; fl[1] = tag_hi;
      __builtin_amdgcn_raw_buffer_store_b64(fl, wsr, (lane == 0 && has_s1) ? WS_F1 + cblk * 128 + (mat1 * 7 + rb) * 8 : OOB, 0, AUX_SC1);
      __builtin_amdgcn_s_sleep(100);
      {
        const int fblk = lane < 32 ? (u0 >> 8) : ((u0 + 35) >> 8);
        const int fo = WS_F1 + fblk * 128 + ((lane & 31) < 14 ? (lane & 31) : 0) * 8;
        for (unsigned spins = 0;; ++spins) {
          const v2u p = __builtin_amdgcn_raw_buffer_load_b64(wsr, fo, 0, AUX_SC1);
          if (__all(((p[1] ^ tag_hi) | (p[0] ^ tag_lo)) == 0u) || spins > SPIN_CAP) break;
          __builtin_amdgcn_s_sleep(64);
        }
      }
      __builtin_amdgcn_fence(__ATOMIC_ACQUIRE, "wavefront");
      const int u = u0 + 4 * c + g;
      const int so = c < 9 ? WS_G1 + (u >> 7) * 14 * 1024 + (((u >> 5) & 3) * 16 + b * 8 + (u & 7)) * 16 + ((u >> 3) & 3) * 4 : OOB;
      float pv[14];
#pragma unroll
      for (int i = 0; i < 14; ++i) pv[i] = __uint_as_float(__builtin_amdgcn_raw_buffer_load_b32(wsr, so, i * 1024, AUX_SC1));
      const float gv = rbf(((pv[0] + pv[1]) + (pv[2] + pv[3])) + ((pv[4] + pv[5]) + pv[6]));
      const float uv = rbf(((pv[7] + pv[8]) + (pv[9] + pv[10])) + ((pv[11] + pv[12]) + pv[13]));
      const float inner = 0.7978845608028654f * (gv + 0.044715f * gv * gv * gv);
      const float hv = rbf(0.5f * gv * (1.f + tanhf(inner))) * uv;
      *(LAS unsigned*)(smem + L_HS + lane * 4) = pk_bf16(hv, hv);
      lds_inc(smem, S_H, lane);
    }
    f32x4 yf = {0.f, 0.f, 0.f, 0.f}, zf = {0.f, 0.f, 0.f, 0.f};
    {
      lds_wait<16>(smem, S_H, 1u);
      const unsigned hp = *(const LAS unsigned*)(smem + L_HS + lane * 4);
#define PRD(i) { const v4u bb = diag(dppu<BCAST + (i)>(hp), msk); yf = mfma(WD[i], bb, yf); zf = mfma(WD[9 + (i)], bb, zf); }
      PRD(0) PRD(1) PRD(2) PRD(3) PRD(4) PRD(5) PRD(6) PRD(7) PRD(8)
      const float og = rbf(1.f - gt);
      yf *= og; zf *= og;
      asm volatile("" : "+v"(yf), "+v"(zf));
    }
    FENCE();
    if (hw == 2) {
      const unsigned char* tp = ((lane & 8) ? Vc : Kc) + (size_t)grp * (SK * 256) + (size_t)(lane & 7) * (1 << 20) + NSU * 8192;
      if (lane == 16) tp = (const unsigned char*)out;
      unsigned tch = 0;
      if (lane < 17) tch = __builtin_nontemporal_load((const unsigned*)tp);
      asm volatile("" :: "v"(tch));
    }
    asm volatile("" :: "v"(cpf));
    if (hw == 3) {
      lds_wait<32>(smem, S_KDONE, 4u);
      v2u fl; fl[0] = tag_lo; fl[1] = tag_hi;
      __builtin_amdgcn_raw_buffer_store_b64(fl, wsr, lane == 0 ? WS_SF + bid * 64 : OOB, 0, AUX_SC1);
      for (unsigned spins = 0;; ++spins) {
        const v2u p = __builtin_amdgcn_raw_buffer_load_b64(wsr, WS_SF + (grp * 8 + (lane & 7)) * 64, 0, AUX_SC1);
        if (__all(((p[1] ^ tag_hi) | (p[0] ^ tag_lo)) == 0u) || spins > SPIN_CAP) break;
        __builtin_amdgcn_s_sleep(32);
      }
      lds_inc(smem, S_SHARED, lane);
      if (is_final) {
        for (unsigned spins = 0;; ++spins) {
          unsigned bad = 0;
#pragma unroll
          for (int i = 0; i < 2; ++i) {
            const v4u p = __builtin_amdgcn_raw_buffer_load_b128(wsr, WS_ST + i * 1024 + voff, 0, AUX_SC1);
            bad |= (p[1] ^ tag_hi) | (p[0] ^ tag_lo) | (p[3] ^ tag_hi) | (p[2] ^ tag_lo);
          }
          if (__all(bad == 0u) || spins > SPIN_CAP) break;
          __builtin_amdgcn_s_sleep(64);
        }
        __builtin_amdgcn_raw_buffer_store_b64(fl, wsr, (fid == 0 && lane == 0) ? WS_E : OOB, 0, AUX_SC1);
        lds_inc(smem, S_ALL, lane);
      }
    }
    FENCE();
    DRAIN();
    lds_wait<32>(smem, S_NEAR, 1u);
    lds_wait<1>(smem, S_LAST, 4u);
    v4u WO[16];
    {
      const rsrc_t wor = mk_rsrc(wo, 4096 * D * 2);
      const int wo_lo = (kvh * 256 + 32 * sp + g) * 1152 + hw * 256 + c * 16;
      const int wo_hi = (hw == 0 && c < 8) ? wo_lo + 1024 : OOB;
#pragma unroll
      for (int i = 0; i < 8; ++i) WO[i] = __builtin_amdgcn_raw_buffer_load_b128(wor, wo_lo, i * 4608, AUX_W);
#pragma unroll
      for (int i = 8; i < 16; ++i) WO[i] = __builtin_amdgcn_raw_buffer_load_b128(wor, wo_hi, (i - 8) * 4608, AUX_W);
    }
    FENCE();
    DRAIN();
    unsigned pp = 0;
#pragma unroll 1
    for (unsigned spins = 0; spins < 8000000u; ++spins) {
      const unsigned long long om = *(volatile LAS unsigned long long*)(smem + L_OS + lane * 8);
      pp = (unsigned)om;
      if (__all((om >> 32) != 0ull)) break;
    }
    {
      f32x4 y0 = {0.f, 0.f, 0.f, 0.f}, y1 = {0.f, 0.f, 0.f, 0.f}, z0 = {0.f, 0.f, 0.f, 0.f};
#define PRJ(i, y) { const v4u bb = diag(dppu<BCAST + (i)>(pp), msk); y = mfma(WO[i], bb, y); z0 = mfma(WO[8 + (i)], bb, z0); }
      PRJ(0, y0) PRJ(1, y1) PRJ(2, y0) PRJ(3, y1) PRJ(4, y0) PRJ(5, y1) PRJ(6, y0) PRJ(7, y1)
      y0 = (y0 + y1) * gt + yf;
      z0 = z0 * gt + zf;
      const int dst = WS_H2 + ((b * 36 + hw * 8 + 2 * g) * 128 + (bid & 127)) * 64 + c * 4;
      __builtin_amdgcn_raw_buffer_store_b32(__float_as_uint(hi8 ? y0[1] : y0[0]) | 1u, wsr, dst, 0, AUX_SC1);
      __builtin_amdgcn_raw_buffer_store_b32(__float_as_uint(hi8 ? y0[3] : y0[2]) | 1u, wsr, dst, 8192, AUX_SC1);
      if (hw == 0) {
        const int dz = g < 2 ? dst + 32 * 8192 : OOB;
        __builtin_amdgcn_raw_buffer_store_b32(__float_as_uint(hi8 ? z0[1] : z0[0]) | 1u, wsr, dz, 0, AUX_SC1);
        __builtin_amdgcn_raw_buffer_store_b32(__float_as_uint(hi8 ? z0[3] : z0[2]) | 1u, wsr, dz, 8192, AUX_SC1);
      }
    }
  }
}

extern "C" void kernel_launch(void* const* d_in, const int* in_sizes, int n_in, void* d_out, int out_size, void* d_ws, size_t ws_size,
                              hipStream_t stream) {
  k_fused<<<256, NTHR, 0, stream>>>((const unsigned short*)d_in[0], (const unsigned char*)d_in[1], (const unsigned char*)d_in[2],
                                    (const unsigned char*)d_in[3], (const unsigned char*)d_in[4], (const unsigned char*)d_in[6],
                                    (const unsigned char*)d_in[7], (const unsigned char*)d_in[8], (const unsigned short*)d_in[9],
                                    (unsigned char*)d_ws, (unsigned*)d_out);
}
```
